```python
import torch

def _softmax(x, axis=-1):
    return torch.softmax(x, dim=axis)
def _gelu(x):
    return torch.nn.functional.gelu(x, approximate="tanh")
def _silu(x):
    return torch.nn.functional.silu(x)
def _sigmoid(x):
    return torch.sigmoid(x)
def _layernorm(x, axis=-1, eps=1e-5):
    m = x.mean(dim=axis, keepdim=True)
    v = x.var(dim=axis, keepdim=True, unbiased=False)
    return (x - m) / torch.sqrt(v + eps)
def _scatter_add(tgt, idx, src, axis=0):
    return tgt.clone().index_add_(axis, idx, src)
def _segment_sum(src, seg, n):
    out = torch.zeros((n,) + src.shape[1:], dtype=src.dtype)
    return out.index_add_(0, seg, src)
def _pad(x, pw):
    flat = tuple(v for pair in reversed(pw) for v in pair)
    return torch.nn.functional.pad(x, flat)

kernel_name = 'parallel_sum__spectral_gfnet__glu_gated_geglu__prompt'

def setup_inputs():

    _w = (lambda a: (lambda t: t.to(torch.bfloat16) if t.is_floating_point() else t)(torch.as_tensor(a)))
    def _A_setup():
        B, S, D = 1, 2048, 512
        Df = D // 2 + 1
        return {
            "x": _w((lambda *s: torch.randn(*s, dtype=torch.bfloat16) * 0.05)(B, S, D)),
            "w_r": _w((lambda *s: torch.randn(*s, dtype=torch.bfloat16) * 0.8)(S, Df) * 0.02),
            "w_i": _w((lambda *s: torch.randn(*s, dtype=torch.bfloat16) * 0.8)(S, Df) * 0.02),
        }
    _A_in = _A_setup()

    def _B_setup():
        B, S, Dm, Df = 1, 2048, 512, 1792
        return {
            "x": _w((lambda *s: torch.randn(*s, dtype=torch.bfloat16) * 0.05)(B, S, Dm)),
            "w_gate": _w((lambda *s: torch.randn(*s, dtype=torch.bfloat16) * 0.4)(Dm, Df) * (Dm ** -0.5)),
            "w_up": _w((lambda *s: torch.randn(*s, dtype=torch.bfloat16) * 0.4)(Dm, Df) * (Dm ** -0.5)),
            "w_down": _w((lambda *s: torch.randn(*s, dtype=torch.bfloat16) * 0.4)(Df, Dm) * (Df ** -0.5)),
        }
    _B_in = _B_setup()

    return {
        "A_x": _A_in["x"],
        "A_w_r": _A_in["w_r"],
        "A_w_i": _A_in["w_i"],
        "B_x": _B_in["x"],
        "B_w_gate": _B_in["w_gate"],
        "B_w_up": _B_in["w_up"],
        "B_w_down": _B_in["w_down"],
    }

def reference(A_x, A_w_r, A_w_i, B_x, B_w_gate, B_w_up, B_w_down):
    def _A_ref(x, w_r, w_i):
        B, S, D = x.shape
        X = (lambda x, axes=(-2,-1): torch.fft.rfft2(x.float(), dim=axes))(x, axes=(-2, -1))
        W = w_r + 1j * w_i
        X = X * W
        y = (lambda x, s, axes=(-2,-1): torch.fft.irfft2(x, s=s, dim=axes).to(torch.bfloat16))(X, (S, D), axes=(-2, -1))
        return y

    def _B_ref(x, w_gate, w_up, w_down):
        g = torch.einsum("bsd,df->bsf", x, w_gate)
        u = torch.einsum("bsd,df->bsf", x, w_up)
        h = _gelu(g) * u
        return torch.einsum("bsf,fd->bsd", h, w_down)

    x = A_x
    xn = _layernorm(x, axis=-1)
    return x + _A_ref(xn, A_w_r, A_w_i) + _B_ref(xn, B_w_gate, B_w_up, B_w_down)
```

```cpp
#include <hip/hip_runtime.h>
namespace naive {
constexpr int S = 2048, D = 512, F = 1792, DF = 257;
__device__ __forceinline__ float bf2f(unsigned short v) { return __uint_as_float((unsigned)v << 16); }
__device__ __forceinline__ unsigned short f2bf(float f) { unsigned u = __float_as_uint(f); u += 0x7fffu + ((u >> 16) & 1u); return (unsigned short)(u >> 16); }
__device__ __forceinline__ float rbf(float f) { return bf2f(f2bf(f)); }
__global__ void k_ln(const unsigned short* x, float* xn, float2* tw) {
  int s = blockIdx.x, t = threadIdx.x;
  float v[8]; float sum = 0;
  for (int i = 0; i < 8; ++i) { v[i] = bf2f(x[s * D + t + 64 * i]); sum += v[i]; }
  for (int o = 32; o; o >>= 1) sum += __shfl_xor(sum, o);
  float m = sum / D, q = 0;
  for (int i = 0; i < 8; ++i) q += (v[i] - m) * (v[i] - m);
  for (int o = 32; o; o >>= 1) q += __shfl_xor(q, o);
  float mb = rbf(m), vb = rbf(q / D), sd = rbf(sqrtf(rbf(vb + 1e-5f)));
  for (int i = 0; i < 8; ++i) xn[s * D + t + 64 * i] = rbf(rbf(v[i] - mb) / sd);
  if (t == 0) { double a = -2.0 * 3.14159265358979323846 * s / S; tw[s] = make_float2((float)cos(a), (float)sin(a)); }
}
__global__ void k_dfwd(const float* xn, const float2* tw, float2* Z) {
  __shared__ float row[D]; __shared__ float2 t[D];
  int s = blockIdx.x;
  for (int i = threadIdx.x; i < D; i += blockDim.x) { row[i] = xn[s * D + i]; t[i] = tw[i * 4]; }
  __syncthreads();
  int k = threadIdx.x; if (k >= DF) return;
  float re = 0, im = 0;
  for (int d = 0; d < D; ++d) { float2 w = t[(k * d) & (D - 1)]; re += row[d] * w.x; im += row[d] * w.y; }
  Z[s * DF + k] = make_float2(re, im);
}
template <int INV>
__global__ void k_sdft(const float2* in, const float2* tw, const unsigned short* wr, const unsigned short* wi, float2* out) {
  __shared__ float2 t[S];
  for (int i = threadIdx.x; i < S; i += blockDim.x) t[i] = tw[i];
  __syncthreads();
  int j0 = blockIdx.x * 4, k = threadIdx.x; if (k >= DF) return;
  float re[4] = {0, 0, 0, 0}, im[4] = {0, 0, 0, 0};
  for (int s = 0; s < S; ++s) {
    float2 z = in[s * DF + k];
#pragma unroll
    for (int u = 0; u < 4; ++u) { float2 w = t[((j0 + u) * s) & (S - 1)]; if (INV) w.y = -w.y; re[u] += z.x * w.x - z.y * w.y; im[u] += z.x * w.y + z.y * w.x; }
  }
  for (int u = 0; u < 4; ++u) {
    int j = j0 + u;
    if (!INV) { float a = bf2f(wr[j * DF + k]), b = bf2f(wi[j * DF + k]); out[j * DF + k] = make_float2(re[u] * a - im[u] * b, re[u] * b + im[u] * a); }
    else out[j * DF + k] = make_float2(re[u] / S, im[u] / S);
  }
}
__global__ void k_h(const float* xn, const unsigned short* wg, const unsigned short* wu, float* h) {
  __shared__ float row[8][D];
  int s0 = blockIdx.y * 8, f = blockIdx.x * 256 + threadIdx.x;
  for (int i = threadIdx.x; i < 8 * D; i += 256) row[i / D][i % D] = xn[s0 * D + i];
  __syncthreads();
  float g[8] = {}, u[8] = {};
  for (int d = 0; d < D; ++d) { float a = bf2f(wg[d * F + f]), b = bf2f(wu[d * F + f]);
#pragma unroll
    for (int r = 0; r < 8; ++r) { g[r] += row[r][d] * a; u[r] += row[r][d] * b; } }
  for (int r = 0; r < 8; ++r) { float gg = rbf(g[r]), uu = rbf(u[r]);
    float ge = rbf(0.5f * gg * (1.f + tanhf(0.7978845608f * (gg + 0.044715f * gg * gg * gg))));
    h[(s0 + r) * F + f] = rbf(ge * uu); }
}
__global__ void k_out(const unsigned short* x, const float* h, const unsigned short* wd, const float2* Y, const float2* tw, unsigned short* out, float* dbgA, float* dbgB) {
  __shared__ float hr[4][F]; __shared__ float2 yr[4][DF]; __shared__ float2 t[D];
  int s0 = blockIdx.x * 4, d = threadIdx.x;
  for (int i = threadIdx.x; i < 4 * F; i += 512) hr[i / F][i % F] = h[s0 * F + i];
  for (int i = threadIdx.x; i < 4 * DF; i += 512) yr[i / DF][i % DF] = Y[s0 * DF + i];
  t[d] = tw[d * 4];
  __syncthreads();
  float b[4] = {}, a[4] = {};
  for (int f = 0; f < F; ++f) { float w = bf2f(wd[f * D + d]);
#pragma unroll
    for (int r = 0; r < 4; ++r) b[r] += hr[r][f] * w; }
  for (int k = 0; k < DF; ++k) { float2 w = t[(k * d) & (D - 1)]; float c = (k == 0 || k == 256) ? 1.f : 2.f; float wy = (k == 0 || k == 256) ? 0.f : w.y;
#pragma unroll
    for (int r = 0; r < 4; ++r) a[r] += c * (yr[r][k].x * w.x + yr[r][k].y * wy); }
  for (int r = 0; r < 4; ++r) { int i = (s0 + r) * D + d; float av = a[r] / D;
    if (dbgA) { dbgA[i] = av; dbgB[i] = b[r]; }
    out[i] = f2bf(rbf(bf2f(x[i]) + rbf(av)) + rbf(b[r])); }
}
inline void run(void* const* d_in, void* d_out, void* d_ws, hipStream_t st, float* dbgA = nullptr, float* dbgB = nullptr) {
  const unsigned short *x = (const unsigned short*)d_in[0], *wr = (const unsigned short*)d_in[1], *wi = (const unsigned short*)d_in[2];
  const unsigned short *wg = (const unsigned short*)d_in[4], *wu = (const unsigned short*)d_in[5], *wd = (const unsigned short*)d_in[6];
  char* ws = (char*)d_ws;
  float* xn = (float*)ws; float2* tw = (float2*)(ws + (4 << 20)); float2* Z = (float2*)(ws + (5 << 20)); float2* X = (float2*)(ws + (10 << 20)); float2* Y = (float2*)(ws + (15 << 20)); float* h = (float*)(ws + (20 << 20));
  k_ln<<<S, 64, 0, st>>>(x, xn, tw);
  k_dfwd<<<S, 320, 0, st>>>(xn, tw, Z);
  k_sdft<0><<<S / 4, 320, 0, st>>>(Z, tw, wr, wi, X);
  k_sdft<1><<<S / 4, 320, 0, st>>>(X, tw, wr, wi, Y);
  k_h<<<dim3(F / 256, S / 8), 256, 0, st>>>(xn, wg, wu, h);
  k_out<<<S / 4, 512, 0, st>>>(x, h, wd, Y, tw, (unsigned short*)d_out, dbgA, dbgB);
}
}

__global__ void k_probe(const char* p, unsigned* sink) {
  asm volatile("s_waitcnt lgkmcnt(0)");
  unsigned r0, r1;
  const char* q = p + threadIdx.x * 128;
  asm volatile("global_load_dword %0, %1, off\n s_waitcnt vmcnt(0)" : "=&v"(r0) : "v"(q + 65536) : "memory");
  unsigned long long t0 = __builtin_amdgcn_s_memtime();
  asm volatile("s_waitcnt lgkmcnt(0)");
  asm volatile("global_load_dword %0, %1, off\n s_waitcnt vmcnt(0)" : "=&v"(r1) : "v"(q) : "memory");
  unsigned long long t1 = __builtin_amdgcn_s_memtime();
  unsigned long long lat = t1 - t0;
  if (lat > 5000) lat = 5000;
  unsigned long long s0 = __builtin_amdgcn_s_memrealtime();
  while (__builtin_amdgcn_s_memrealtime() - s0 < lat * 0) __builtin_amdgcn_s_sleep(8);
  if (r0 == 0x12345678 && r1 == 0x9abcdef0) sink[0] = 1;
}
extern "C" void kernel_launch(void* const* d_in, const int* in_sizes, int n_in, void* d_out, int out_size, void* d_ws, size_t ws_size, hipStream_t stream) {
  k_probe<<<1, 64, 0, stream>>>((const char*)d_in[0], (unsigned*)((char*)d_ws + (100 << 20)));
  naive::run(d_in, d_out, d_ws, stream);
}
```

```cpp
#include <hip/hip_runtime.h>
#pragma clang diagnostic ignored "-Wint-to-pointer-cast"

#define TOUCH 8
#define TOUCH2 0
#define NPRE0 4
#define T2POS 3
#define KSTART 0
namespace {
typedef __bf16 bf16x8 __attribute__((ext_vector_type(8)));
typedef __bf16 bf16x2 __attribute__((ext_vector_type(2)));
typedef float f32x4 __attribute__((ext_vector_type(4)));
typedef float f32x2 __attribute__((ext_vector_type(2)));
typedef unsigned u32x4 __attribute__((ext_vector_type(4)));
typedef unsigned u32x2 __attribute__((ext_vector_type(2)));
typedef short s16x4 __attribute__((ext_vector_type(4)));
typedef unsigned long long u64;
typedef __amdgpu_buffer_rsrc_t rsrc_t;
#define LDS __attribute__((address_space(3)))
#define GLB __attribute__((address_space(1)))
#define DI __device__ __forceinline__
#define UNROLL _Pragma("unroll")

constexpr unsigned WS_ZF = 256, WS_SF = 3072, WS_HF = 512, WS_YF = 4096, WS_ST = 8192, WS_Z = 1u << 20, WS_WT = 3u << 20, WS_Y = 6u << 20,
                   WS_H = 8u << 20, WS_END = 16u << 20;
constexpr unsigned LDS_BYTES = 163840;
constexpr unsigned L_Z = 131072, L_W0 = L_Z + 8192, L_Q0 = L_Z + 16384, L_W1 = L_Z + 24576;
constexpr unsigned L_WR = 65536, L_WI = L_WR + 33792, L_BN = L_WI + 33792;
constexpr int SC1 = 16;

DI unsigned pk(float a, float b) { f32x2 v = {a, b}; return __builtin_bit_cast(unsigned, __builtin_convertvector(v, bf16x2)); }
DI float lo16(unsigned v) { return __uint_as_float(v << 16); }
DI float hi16(unsigned v) { return __uint_as_float(v & 0xffff0000u); }
DI f32x4 mfma(u32x4 a, u32x4 b, f32x4 c) {
  return __builtin_amdgcn_mfma_f32_16x16x32_bf16(__builtin_bit_cast(bf16x8, a), __builtin_bit_cast(bf16x8, b), c, 0, 0, 0);
}
DI u32x4 pk8(f32x4 a, f32x4 b) { u32x4 r = {pk(a.x, a.y), pk(a.z, a.w), pk(b.x, b.y), pk(b.z, b.w)}; return r; }
DI u32x4 rd128(unsigned a) { return *(LDS u32x4*)a; }
DI void wr128(unsigned a, u32x4 v) { *(LDS u32x4*)a = v; }
DI u32x4 rdtr(unsigned a0, unsigned a1) {
  u32x2 p = __builtin_bit_cast(u32x2, __builtin_amdgcn_ds_read_tr16_b64_v4i16((LDS s16x4*)a0));
  u32x2 q = __builtin_bit_cast(u32x2, __builtin_amdgcn_ds_read_tr16_b64_v4i16((LDS s16x4*)a1));
  u32x4 r = {p.x, p.y, q.x, q.y};
  return r;
}
DI void dma(rsrc_t rs, unsigned lds, unsigned voff, unsigned soff) {
  asm volatile("s_mov_b32 m0, %0\n\ts_nop 0\n\tbuffer_load_dwordx4 %1, %2, %3 offen lds" ::"s"(lds), "v"(voff), "s"(rs), "s"(soff) : "memory");
}
DI void touch(rsrc_t rs, unsigned lds, unsigned voff) {
  asm volatile("s_mov_b32 m0, %0\n\ts_nop 0\n\tbuffer_load_dword %1, %2, 0 offen lds" ::"s"(lds), "v"(voff), "s"(rs) : "memory");
}
DI void st128(u32x4 v, rsrc_t rs, unsigned voff, unsigned soff) {
  asm volatile("buffer_store_dwordx4 %0, %1, %2, %3 offen sc1\n\ts_nop 1" ::"v"(v), "v"(voff), "s"(rs), "s"(soff) : "memory");
}
DI void ald64(u32x2& v, rsrc_t rs, unsigned voff, unsigned soff) {
  asm volatile("buffer_load_dwordx2 %0, %1, %2, %3 offen sc1" : "=v"(v) : "v"(voff), "s"(rs), "s"(soff) : "memory");
}
DI void ald128(u32x4& v, rsrc_t rs, unsigned voff, unsigned soff) {
  asm volatile("buffer_load_dwordx4 %0, %1, %2, %3 offen sc1" : "=v"(v) : "v"(voff), "s"(rs), "s"(soff) : "memory");
}
template <int OFF> DI void ald128p(u32x4& v, rsrc_t rs, unsigned voff, unsigned soff) {
  asm volatile("buffer_load_dwordx4 %0, %1, %2, %3 offen offset:%4" : "=v"(v) : "v"(voff), "s"(rs), "s"(soff), "i"(OFF) : "memory");
}
DI u32x2 rdtr1(unsigned a) { return __builtin_bit_cast(u32x2, __builtin_amdgcn_ds_read_tr16_b64_v4i16((LDS s16x4*)a)); }
DI f32x2 lo2(f32x4 v) { return __builtin_shufflevector(v, v, 0, 1); }
DI f32x2 hi2(f32x4 v) { return __builtin_shufflevector(v, v, 2, 3); }
DI f32x2 fma2(f32x2 a, f32x2 b, f32x2 c) { return __builtin_elementwise_fma(a, b, c); }
DI f32x2 sp(float a) { return f32x2{a, a}; }
DI unsigned pk2(f32x2 v) { return __builtin_bit_cast(unsigned, __builtin_convertvector(v, bf16x2)); }
DI float fcos(float rev) { return __builtin_amdgcn_cosf(rev); }
DI float fsin(float rev) { return __builtin_amdgcn_sinf(rev); }
#define WAITV(n) asm volatile("s_waitcnt vmcnt(" #n ")" ::: "memory")
#define WAITV_(n, ...) asm volatile("s_waitcnt vmcnt(" #n ")" : __VA_ARGS__ :: "memory")
#define BARRIER() do { asm volatile("s_waitcnt lgkmcnt(0)" ::: "memory"); __builtin_amdgcn_s_barrier(); asm volatile("" ::: "memory"); } while (0)
#define SCHED() __builtin_amdgcn_sched_barrier(0)
#define STAMP(i)
#define DIAG_BEGIN()
#define DIAG_END()

DI void wait_tag(char* ws, unsigned off, u64 tag) {
#pragma unroll 1
  for (int it = 0; it < (1 << 18); ++it) {
    u64 v = __hip_atomic_load((GLB u64*)(ws + off), __ATOMIC_RELAXED, __HIP_MEMORY_SCOPE_AGENT);
    if (__all(v == tag)) break;
    __builtin_amdgcn_s_sleep(2);
  }
  __builtin_amdgcn_fence(__ATOMIC_ACQUIRE, "wavefront");
  asm volatile("" ::: "memory");
}
DI void put_tag(char* ws, unsigned off, u64 tag, int who = 0) {
  if ((int)threadIdx.x == who) __hip_atomic_store((GLB u64*)(ws + off), tag, __ATOMIC_RELAXED, __HIP_MEMORY_SCOPE_AGENT);
}

struct FC { u32x4 c64, s64, c32, s32; float t1c[8], t1s[8], t2c[8], t2s[8]; };
DI void fft_consts(FC& k, int c, int g, int T, float scale, bool full) {
  const float j1 = (float)(16 * T + c) * (1.f / 64.f), g4 = (float)(4 * g);
  UNROLL for (int i = 0; i < 4; ++i) {
    const float o = (float)(i < 2 ? 2 * i : 12 + 2 * i);
    const f32x2 an = (sp(g4) + f32x2{o, o + 1.f}) * sp(j1);
    k.c64[i] = pk(fcos(an.x), fcos(an.y)); k.s64[i] = pk(fsin(an.x), fsin(an.y));
  }
  asm volatile("" : "+v"(k.c64), "+v"(k.s64));
  if (full) {
    const float cf = (float)c * (1.f / 32.f), j2 = j1 * (1.f / 32.f), r0 = (float)(16 * T) + g4;
    UNROLL for (int i = 0; i < 4; ++i) {
      const float o = (float)(i < 2 ? 2 * i : 12 + 2 * i);
      const f32x2 sl = sp(g4) + f32x2{o, o + 1.f}, a1 = sl * sp(cf), a2 = sl * sp(j2);
      const f32x2 a3 = (sp(r0) + f32x2{(float)(2 * (i & 1)), (float)(2 * (i & 1) + 1)}) * sp((cf + (float)(i >> 1) * 0.5f) * (1.f / 64.f));
      k.c32[i] = pk(fcos(a1.x), fcos(a1.y)); k.s32[i] = pk(fsin(a1.x), fsin(a1.y));
      k.t1c[2 * i] = fcos(a2.x); k.t1c[2 * i + 1] = fcos(a2.y); k.t1s[2 * i] = fsin(a2.x); k.t1s[2 * i + 1] = fsin(a2.y);
      const f32x2 cc = f32x2{fcos(a3.x), fcos(a3.y)} * sp(scale), ss = f32x2{fsin(a3.x), fsin(a3.y)} * sp(scale);
      k.t2c[2 * i] = cc.x; k.t2c[2 * i + 1] = cc.y; k.t2s[2 * i] = ss.x; k.t2s[2 * i + 1] = ss.y;
    }
    asm volatile("" : "+v"(k.c32), "+v"(k.s32));
    UNROLL for (int j = 0; j < 8; ++j) asm volatile("" : "+v"(k.t1c[j]), "+v"(k.t1s[j]), "+v"(k.t2c[j]), "+v"(k.t2s[j]));
  }
}
DI void dft_table(u32x4 (&t)[4], float kf, float ph, int g) {
  const float st = kf * (1.f / 512.f), a0 = st * (float)(8 * g) + ph;
  const f32x2 m2 = sp(2.f * fcos(st * 2.f)), m32 = sp(2.f * fcos(st * 32.f));
  f32x2 v[4][4];
  UNROLL for (int s = 0; s < 2; ++s) {
    const f32x2 p = fma2(sp(st), f32x2{32.f * s, 32.f * s + 1.f}, sp(a0)), q = fma2(sp(st), f32x2{32.f * s + 2.f, 32.f * s + 3.f}, sp(a0));
    v[s][0] = f32x2{fcos(p.x), fcos(p.y)}; v[s][1] = f32x2{fcos(q.x), fcos(q.y)};
    v[s][2] = fma2(m2, v[s][1], -v[s][0]); v[s][3] = fma2(m2, v[s][2], -v[s][1]);
  }
  UNROLL for (int i = 0; i < 4; ++i) { v[2][i] = fma2(m32, v[1][i], -v[0][i]); v[3][i] = fma2(m32, v[2][i], -v[1][i]); }
  UNROLL for (int s = 0; s < 4; ++s) { UNROLL for (int i = 0; i < 4; ++i) t[s][i] = pk2(v[s][i]); asm volatile("" : "+v"(t[s])); }
}
template <int CTRL> DI float dpp_add(float v) {
  return v + __builtin_bit_cast(float, __builtin_amdgcn_update_dpp(0, __builtin_bit_cast(int, v), CTRL, 0xf, 0xf, true));
}
DI float row16_sum(float v) { v = dpp_add<0x128>(v); v = dpp_add<0x124>(v); v = dpp_add<0x122>(v); return dpp_add<0x121>(v); }
DI float rbf(float v) { return lo16(pk(v, 0.f)); }

__global__ void __launch_bounds__(512, 2)
fused_kernel(const char* __restrict__ x, const char* __restrict__ wr, const char* __restrict__ wi, const char* __restrict__ wg,
             const char* __restrict__ wu, const char* __restrict__ wd, char* __restrict__ out, char* ws) {
  const int tid = threadIdx.x, lane = tid & 63, c = lane & 15, g = lane >> 4;
  const int w = __builtin_amdgcn_readfirstlane(tid >> 6);
  const int bid = blockIdx.x, xcd = bid & 7, idx = bid >> 3;
  const rsrc_t rX = __builtin_amdgcn_make_buffer_rsrc((void*)x, 0, 2048 * 512 * 2, 0x00020000);
  const rsrc_t rD = __builtin_amdgcn_make_buffer_rsrc((void*)wd, 0, 1792 * 512 * 2, 0x00020000);
  const rsrc_t rS = __builtin_amdgcn_make_buffer_rsrc((void*)ws, 0, WS_END, 0x00020000);
  DIAG_BEGIN();
  const int col = (bid + 1) & 255;
  const bool special = (col == 0);
  const int bo = 4 * xcd + (idx >> 3), nb = idx & 7;
  const float yscale = special ? (1.f / 1048576.f) : (2.f / 1048576.f);
  const unsigned vWd = (unsigned)(((14 * (w >> 1)) * 32 + ((16 * (w & 1) + (lane >> 3)) ^ (16 * (bo & 1)))) * 1024 + nb * 128 +
                                  ((((lane & 7) >> 1) ^ g) * 32) + (lane & 1) * 16);
  auto issueWd = [&](int step, int slot) {
    dma(rD, slot * 16384 + w * 2048, vWd, step * 32768);
    dma(rD, slot * 16384 + w * 2048 + 1024, vWd, step * 32768 + 8192);
  };
  u64 epoch, tag;
  u32x2 ev;
  auto make_tag = [&]() {
    epoch = ((u64)__builtin_amdgcn_readfirstlane(ev.y) << 32) | (unsigned)__builtin_amdgcn_readfirstlane(ev.x);
    tag = epoch * 0x9E3779B97F4A7C15ull + 0x632BE59BD9B4E019ull;
  };
  FC fc;
  const int th = w >> 2, kq = w & 3, q4 = (lane >> 2) & 3, p4 = lane & 3;
  unsigned aD[4];
  UNROLL for (int ct = 0; ct < 4; ++ct) aD[ct] = kq * 4096 + (4 * g + q4) * 128 + ((ct ^ ((2 * g + (q4 >> 1)) & 3)) * 32) + 8 * p4;
  u32x4 park[4][4];
  auto read_park = [&](int i) { UNROLL for (int ct = 0; ct < 4; ++ct) park[i][ct] = rdtr(aD[ct] + i * 16384, aD[ct] + i * 16384 + 2048); };
  auto stZ = [&](int t) { return L_Z + (t >> 3) * 128 + (((t & 7) ^ ((t >> 4) & 7)) * 16); };
  auto stW = [&](int t) { return (t >> 4) * 256 + (((t & 15) ^ (((t >> 6) & 1) * 4)) * 16); };
  constexpr unsigned L_CNT = L_W1 + 4096;
  const rsrc_t rG = __builtin_amdgcn_make_buffer_rsrc((void*)wg, 0, 512 * 1792 * 2, 0x00020000);
  const rsrc_t rU = __builtin_amdgcn_make_buffer_rsrc((void*)wu, 0, 512 * 1792 * 2, 0x00020000);
  const rsrc_t rR = __builtin_amdgcn_make_buffer_rsrc((void*)wr, 0, 2048 * 257 * 2, 0x00020000);
  const rsrc_t rI = __builtin_amdgcn_make_buffer_rsrc((void*)wi, 0, 2048 * 257 * 2, 0x00020000);
  auto touches = [&](int mask, unsigned lds) {
    const int l56 = lane < 56 ? lane : 55, wrow = (l56 * 37) >> 9;
    if (w == 0 && (mask & 1)) touch(rX, lds, ((32 * (xcd & 1) + idx) * 32 + 8 * (xcd >> 1)) * 1024 + lane * 128);
    if (w == 1 && (mask & 2)) touch(rG, lds + 256, (wrow * 128 + (xcd >> 1) * 32 + idx) * 3584 + (xcd & 1) * 1792 + (l56 - 14 * wrow) * 128);
    if (w == 2 && (mask & 4)) touch(rU, lds + 512, (wrow * 128 + (xcd >> 1) * 32 + idx) * 3584 + (xcd & 1) * 1792 + (l56 - 14 * wrow) * 128);
    if (w == 3 && (mask & 8)) touch(rD, lds + 768, (bid * 56 + l56) * 128);
    if (w == 4 && (mask & 16)) touch(rR, lds + 1024, xcd * 131584 + (idx * 32 + (lane < 36 ? lane : 35)) * 128);
    if (w == 5 && (mask & 16)) touch(rI, lds + 1280, xcd * 131584 + (idx * 32 + (lane < 36 ? lane : 35)) * 128);
  };

  if (idx < 28) {
    const int rb = 4 * (xcd >> 1) + (idx & 3), cb = 7 * (xcd & 1) + (idx >> 2);
    const int wt = w >> 2, wf = w & 3;
    const int xr = 8 * w + (lane >> 3);
    const int xa = 32 * (xr >> 5) + 2 * (xr & 15) + ((xr >> 4) & 1);
    const unsigned vX = (unsigned)((32 * xa + 2 * rb) * 1024 + (((lane & 7) ^ ((xr >> 1) & 7)) * 16));
    const unsigned vW = (unsigned)((8 * w + g) * 3584 + cb * 256 + (((c >> 1) ^ (g | ((w & 1) << 2))) * 32) + (c & 1) * 16);
    const int kstart = KSTART;
    auto slot = [](int ch) { return 16384u + ((ch + 1) % 3) * 49152u; };
    auto issue = [&](int n) {
      const int ch = n / 6, pc = n % 6;
      if (ch < 8) {
        const int kc = (ch + kstart) & 7, hf = pc / 3;
        if (pc % 3 == 0) dma(rX, slot(ch) + hf * 8192 + w * 1024, vX, kc * 128 + hf * 1024);
        else dma(pc % 3 == 1 ? rG : rU, slot(ch) + (pc % 3) * 16384 + w * 2048 + hf * 1024, vW, kc * 229376 + hf * 14336);
      } else {
        const int st = 1 + (n - 48) / 2, hf = n & 1;
        dma(rD, st * 16384 + w * 2048 + hf * 1024, vWd, st * 32768 + hf * 8192);
      }
    };
    touches(TOUCH, 0);
    ald64(ev, rS, 0, 0);
    UNROLL for (int n = 0; n < NPRE0; ++n) issue(n);
    fft_consts(fc, c, g, w & 3, yscale, w < 4);
    STAMP(13);
    UNROLL for (int n = NPRE0; n < 10; ++n) issue(n);
    const int sw = q4 | ((g & 1) << 2);
    const unsigned aB0 = (64 * wt + c) * 128 + ((g ^ (c >> 1)) * 16), aB1 = aB0 ^ 64;
    const unsigned aA0 = 16384 + (8 * g + q4) * 256 + (((2 * wf + wt) ^ sw) * 32) + 8 * p4, aA1 = aA0 ^ 32;
    f32x4 acc[4][4], cs[2];
    UNROLL for (int m = 0; m < 4; ++m) UNROLL for (int t = 0; t < 4; ++t) acc[m][t] = f32x4{0, 0, 0, 0};
    cs[0] = cs[1] = f32x4{0, 0, 0, 0};
    u32x4 ones = {0x3f803f80u, 0x3f803f80u, 0x3f803f80u, 0x3f803f80u};
    asm volatile("" : "+v"(ones));
    u32x4 A[2][4], B[2][4];
    auto rdgrp = [&](int gi, int ks, unsigned base) {
      const unsigned b = base + (ks ? aB1 : aB0), a0 = base + aA0 + ks * 8192, a1 = base + aA1 + ks * 8192;
      if (gi == 0) { A[ks][0] = rdtr(a0, a0 + 1024); B[ks][0] = rd128(b); }
      if (gi == 1) { B[ks][1] = rd128(b + 2048); B[ks][2] = rd128(b + 4096); B[ks][3] = rd128(b + 6144); }
      if (gi == 2) { A[ks][2] = rdtr(a0 + 16384, a0 + 17408); A[ks][1] = rdtr(a1, a1 + 1024); }
      if (gi == 3) { A[ks][3] = rdtr(a1 + 16384, a1 + 17408); }
    };
    auto mm1 = [&](int i, int ks) {
      if (i == 4) cs[0] = mfma(A[ks][0], ones, cs[0]);
      else if (i == 9) cs[1] = mfma(A[ks][2], ones, cs[1]);
      else { const int m = i < 4 ? 0 : i < 9 ? 2 : i < 14 ? 1 : 3, t = i < 4 ? i : i < 9 ? i - 5 : (i - 10) & 3; acc[m][t] = mfma(A[ks][m], B[ks][t], acc[m][t]); }
    };
    STAMP(1);
    WAITV_(4, "+v"(ev)); BARRIER();
    STAMP(2);
    make_tag();
    u32x2 sf; u32x4 st;
    UNROLL for (int gi = 0; gi < 4; ++gi) rdgrp(gi, 0, slot(0));
    SCHED();
    UNROLL for (int hp = 0; hp < 16; ++hp) {
      const int ks = hp & 1, nx = hp + 1;
      if (hp == T2POS) touches(TOUCH2, 0);
      if (hp == 9) issueWd(0, 0);
      if (hp == 11) ald64(sf, rS, 0, WS_SF + (2 * rb + wt) * 8);
      if (hp == 13) ald128(st, rS, c * 16, WS_ST + (2 * rb + wt) * 256);
      UNROLL for (int gi = 0; gi < 6; ++gi) {
        if (gi < 4) { if (nx < 16) rdgrp(gi, nx & 1, slot(nx >> 1)); else read_park(gi); }
        UNROLL for (int i = 0; i < 3; ++i) mm1(3 * gi + i, ks);
        UNROLL for (int i = 0; i < 3; ++i) { __builtin_amdgcn_sched_group_barrier(0x008, 1, 0); if (nx < 16) __builtin_amdgcn_sched_group_barrier(0x100, 1, 0); else __builtin_amdgcn_sched_group_barrier(0x100, 3, 0); }
        SCHED();
        if (hp == 0) { issue(10 + gi); if (gi >= 4) issue(12 + gi); }
        else if (hp < 15) { if ((gi & 1) == 0) issue(15 + 3 * hp + gi / 2); }
        else if (gi == 0 || gi == 2) issue(60 + gi / 2);
        if (hp == 15 && gi == 3 && c == 0) { wr128(L_W1 + w * 128 + g * 16, __builtin_bit_cast(u32x4, cs[0])); wr128(L_W1 + w * 128 + 64 + g * 16, __builtin_bit_cast(u32x4, cs[1])); }
        if (hp == 15 && gi == 3 && tid == 0) *(LDS unsigned*)L_CNT = 0;
        SCHED();
      }
      if (ks == 0) {
        if (hp == 12) {
          WAITV_(6, "+v"(sf));
          if (__builtin_amdgcn_readfirstlane(sf.x) != (unsigned)tag || __builtin_amdgcn_readfirstlane(sf.y) != (unsigned)(tag >> 32))
            wait_tag(ws, WS_SF + (2 * rb + wt) * 8, tag);
        } else if (hp == 14) WAITV_(6, "+v"(st));
        else { STAMP(22 + (hp >> 1)); WAITV(6); }
        BARRIER();
        STAMP(14 + (hp >> 1));
        SCHED();
      }
    }
    BARRIER();
    STAMP(3);
    const f32x4 cg1 = __builtin_bit_cast(f32x4, rd128(L_W1 + (w ^ 4) * 128 + g * 16)), cu1 = __builtin_bit_cast(f32x4, rd128(L_W1 + (w ^ 4) * 128 + 64 + g * 16));
    if (w >= 4) {
      wait_tag(ws, WS_ZF + (lane & 31) * 8, tag);
      STAMP(4);
      u32x4 z0, z1, w0, w1;
      ald128(z0, rS, tid * 16, WS_Z + col * 8192 - 4096); ald128(z1, rS, tid * 16, WS_Z + col * 8192);
      ald128(w0, rS, tid * 16, WS_WT + col * 8192 - 4096); ald128(w1, rS, tid * 16, WS_WT + col * 8192);
      UNROLL for (int i = 0; i < 4; ++i) issueWd(10 + i, i);
      WAITV_(8, "+v"(z0), "+v"(z1), "+v"(w0), "+v"(w1));
      wr128(stZ(tid) - 4096, z0); wr128(stZ(tid), z1); wr128(L_W0 + stW(tid) - 4096, w0); wr128(L_W0 + stW(tid), w1);
      BARRIER();
      __builtin_amdgcn_s_setprio(1);
    }
    STAMP(5);
    UNROLL for (int t = 0; t < 4; ++t) {
      const float sd = hi16(st[t]), rs = __builtin_amdgcn_rcpf(sd);
      const f32x2 nm = sp(-lo16(st[t])), k1 = sp(-2.302208198f * rs), k3 = sp(-0.102943240f * rs * rs * rs), v2 = sp(sd * sd);
      u32x4 h;
      UNROLL for (int i = 0; i < 4; ++i) {
        const int m = i >> 1;
        const f32x4 ag = acc[m][t], au = acc[2 + m][t], sg = m ? cg1 : cs[0], su = m ? cu1 : cs[1];
        const f32x2 tg = fma2(nm, (i & 1) ? hi2(sg) : lo2(sg), (i & 1) ? hi2(ag) : lo2(ag)), tu = fma2(nm, (i & 1) ? hi2(su) : lo2(su), (i & 1) ? hi2(au) : lo2(au));
        const f32x2 ar = tg * fma2(k3, tg * tg, k1);
        const f32x2 dn = fma2(f32x2{__builtin_amdgcn_exp2f(ar.x), __builtin_amdgcn_exp2f(ar.y)}, v2, v2);
        h[i] = pk2(tg * tu * f32x2{__builtin_amdgcn_rcpf(dn.x), __builtin_amdgcn_rcpf(dn.y)});
      }
      st128(h, rS, lane * 16, WS_H + ((2 * rb + wt) * 56 + 4 * cb + wf) * 4096 + t * 1024);
    }
    STAMP(6);
    if (w < 4) {
      BARRIER();
      UNROLL for (int i = 0; i < 4; ++i) issueWd(10 + i, i);
    } else {
      __builtin_amdgcn_s_setprio(0);
    }
  } else {
    const int b = 4 * xcd + (idx - 28);
    touches(TOUCH | TOUCH2, L_BN + 20480);
    u32x4 xv[2][4];
    u32x4 tc[4], ts[4], t0[4], t2[4];
    const float ko = (float)(32 * w + 2 * c + 1), ke = (float)(32 * w + 4 * (c & 7)), ph = (c & 8) ? 0.25f : 0.f;
    const bool nyq = (w == 0 && c == 8);
    const unsigned vx = g * 32768 + c * 16, sx = (256 * w + b) * 1024;
    ald128p<0>(xv[0][0], rX, vx, sx); ald128p<256>(xv[0][1], rX, vx, sx); ald128p<512>(xv[0][2], rX, vx, sx); ald128p<768>(xv[0][3], rX, vx, sx);
    SCHED();
    dft_table(tc, ko, 0.f, g); dft_table(ts, ko, 0.25f, g);
    SCHED();
    ald128p<0>(xv[1][0], rX, vx, sx + 131072); ald128p<256>(xv[1][1], rX, vx, sx + 131072); ald128p<512>(xv[1][2], rX, vx, sx + 131072); ald128p<768>(xv[1][3], rX, vx, sx + 131072);
    ald64(ev, rS, 0, 0);
    SCHED();
    dft_table(t0, nyq ? 256.f : ke, nyq ? 0.f : ph, g); dft_table(t2, ke + 2.f, ph, g);
    SCHED();
    STAMP(1);
    UNROLL for (int tp = 0; tp < 2; ++tp) {
      UNROLL for (int i = 0; i < 4; ++i) dma(tp ? rI : rR, (tp ? L_WI : L_WR) + (4 * w + i) * 1024, lane * 16, b * 32896 + (4 * w + i) * 1024);
      if (tp == 0) WAITV_(9, "+v"(xv[0][0]), "+v"(xv[0][1]), "+v"(xv[0][2]), "+v"(xv[0][3]));
      else WAITV_(8, "+v"(xv[1][0]), "+v"(xv[1][1]), "+v"(xv[1][2]), "+v"(xv[1][3]), "+v"(ev));
      f32x2 o[4][4], sum = {0.f, 0.f};
      UNROLL for (int i = 0; i < 4; ++i) {
        f32x2 v[4];
        UNROLL for (int m = 0; m < 4; ++m) v[m] = f32x2{lo16(xv[tp][m][i]), hi16(xv[tp][m][i])};
        const f32x2 ua = v[0] + v[2], ub = v[1] + v[3];
        o[0][i] = v[0] - v[2]; o[1][i] = v[1] - v[3]; o[2][i] = ua + ub; o[3][i] = ua - ub;
        sum += o[2][i];
      }
      const float mean = row16_sum(sum.x + sum.y) * (1.f / 512.f);
      f32x2 q1 = {0.f, 0.f}, q2 = {0.f, 0.f};
      UNROLL for (int i = 0; i < 4; ++i) {
        o[2][i] -= sp(4.f * mean);
        q1 = fma2(o[0][i], o[0][i], q1); q1 = fma2(o[1][i], o[1][i], q1); q2 = fma2(o[2][i], o[2][i], q2); q2 = fma2(o[3][i], o[3][i], q2);
      }
      q1 = fma2(q2, sp(0.5f), q1);
      const float var = row16_sum(q1.x + q1.y) * (1.f / 1024.f);
      const float mb = rbf(mean), sd = rbf(__builtin_amdgcn_sqrtf(rbf(rbf(var) + 1e-5f)));
      const float rs = __builtin_amdgcn_rcpf(sd), dl = 4.f * (mean - mb) * rs;
      const int a = 8 * w + 4 * tp + g;
      if (c == 0) *(LDS unsigned*)(L_BN + 16384 + (((a & 31) >> 1) * 4 + 2 * (a >> 5) + (a & 1)) * 4) = pk(mb, sd);
      UNROLL for (int m = 0; m < 4; ++m) {
        u32x4 pv;
        UNROLL for (int i = 0; i < 4; ++i) pv[i] = pk2(m == 2 ? fma2(o[m][i], sp(rs), sp(dl)) : o[m][i] * sp(rs));
        wr128(m * 16384 + a * 256 + ((c ^ (a & 15)) * 16), pv);
      }
    }
    if (w == 0) dma(rR, L_WR + 32768, (lane & 7) * 16, b * 32896 + 32768);
    if (w == 1) dma(rI, L_WI + 32768, (lane & 7) * 16, b * 32896 + 32768);
    make_tag();
    STAMP(13);
    WAITV(0); BARRIER();
    STAMP(2);
    if (w == 3) __builtin_amdgcn_raw_buffer_store_b32(*(LDS unsigned*)(L_BN + 16384 + lane * 4), rS, lane * 4, WS_ST + b * 256, SC1);
    const unsigned bn = L_BN + w * 2048;
    auto putZ = [&](f32x4 (&d)[4], unsigned r0, unsigned r1) {
      wr128(bn + c * 128 + ((g ^ (c & 7)) * 16), pk8(d[0], d[1]));
      wr128(bn + c * 128 + (((4 + g) ^ (c & 7)) * 16), pk8(d[2], d[3]));
      const unsigned ra = bn + (lane >> 3) * 128 + (((lane & 7) ^ (lane >> 3)) * 16);
      const u32x4 p0 = rd128(ra), p1 = rd128(ra + 1024);
      st128(p0, rS, r0 * 4096 + (lane & 7) * 16, WS_Z + b * 128);
      st128(p1, rS, r1 * 4096 + (lane & 7) * 16, WS_Z + b * 128);
    };
    const int l8 = lane >> 3;
#pragma unroll 1
    for (int pass = 0; pass < 2; ++pass) {
      if (pass == (w >> 2)) {
        f32x4 d0[4], d2[4], dc[4], ds[4];
        const unsigned sg = (c & 1) ? 0x80008000u : 0u;
        UNROLL for (int t = 0; t < 4; ++t) d0[t] = d2[t] = dc[t] = ds[t] = f32x4{0, 0, 0, 0};
        UNROLL for (int s = 0; s < 4; ++s) {
          const u32x4 hc = ts[s] ^ sg, hs = tc[s] ^ (sg ^ 0x80008000u);
          UNROLL for (int t = 0; t < 4; ++t) {
            const unsigned a = c * 256 + (((4 * s + g) ^ c) * 16) + t * 4096;
            const u32x4 v0 = rd128(a), v1 = rd128(a + 16384);
            dc[t] = mfma(v0, tc[s], dc[t]); ds[t] = mfma(v0, ts[s], ds[t]);
            dc[t] = mfma(v1, hc, dc[t]); ds[t] = mfma(v1, hs, ds[t]);
          }
        }
        if (w == 3) { WAITV(0); put_tag(ws, WS_SF + b * 8, tag, 192); }
        putZ(dc, 2 * (32 * w + 2 * l8 + 1), 2 * (32 * w + 2 * l8 + 17));
        putZ(ds, 2 * (32 * w + 2 * l8 + 1) + 1, 2 * (32 * w + 2 * l8 + 17) + 1);
        UNROLL for (int s = 0; s < 4; ++s) UNROLL for (int t = 0; t < 4; ++t) {
          const unsigned a = c * 256 + (((4 * s + g) ^ c) * 16) + t * 4096;
          d0[t] = mfma(rd128(a + 32768), t0[s], d0[t]);
          d2[t] = mfma(rd128(a + 49152), t2[s], d2[t]);
        }
        putZ(d0, 2 * (32 * w + 4 * l8), 2 * (32 * w + 4 * l8) + 1);
        putZ(d2, 2 * (32 * w + 4 * l8 + 2), 2 * (32 * w + 4 * l8 + 2) + 1);
        STAMP(3);
      } else {
        const unsigned a = c * 2056 + 8 * w + 2 * g;
        u32x4 o[8];
        UNROLL for (int it = 0; it < 8; ++it) UNROLL for (int i = 0; i < 4; ++i)
          o[it][i] = (unsigned)*(LDS unsigned short*)(L_WR + a + i * 514 + it * 64) | ((unsigned)*(LDS unsigned short*)(L_WI + a + i * 514 + it * 64) << 16);
        UNROLL for (int it = 0; it < 8; ++it) st128(o[it], rS, g * 8192 + c * 16, WS_WT + b * 256 + (32 * it + 4 * w) * 8192);
        if (w == 7) {
          const unsigned v = (unsigned)*(LDS unsigned short*)(L_WR + lane * 514 + 512) | ((unsigned)*(LDS unsigned short*)(L_WI + lane * 514 + 512) << 16);
          __builtin_amdgcn_raw_buffer_store_b32(v, rS, lane * 4, WS_WT + b * 256 + 256 * 8192, SC1);
        }
        STAMP(14);
      }
    }
    WAITV(0); BARRIER();
    put_tag(ws, WS_ZF + b * 8, tag);
    STAMP(4);
    UNROLL for (int i = 0; i < 8; ++i) issueWd(i, i);
    fft_consts(fc, c, g, w & 3, yscale, true);
    WAITV(8); BARRIER();
    UNROLL for (int i = 0; i < 4; ++i) read_park(i);
    wait_tag(ws, WS_ZF + (lane & 31) * 8, tag);
    const u32x4 zr = __builtin_amdgcn_raw_buffer_load_b128(rS, tid * 16, WS_Z + col * 8192, SC1);
    const u32x4 w0 = __builtin_amdgcn_raw_buffer_load_b128(rS, tid * 16, WS_WT + col * 8192, SC1);
    if (special) wr128(L_W1 + stW(tid), __builtin_amdgcn_raw_buffer_load_b128(rS, tid * 16, WS_WT + 256 * 8192, SC1));
    wr128(stZ(tid), zr); wr128(L_W0 + stW(tid), w0);
    BARRIER();
    UNROLL for (int i = 0; i < 4; ++i) issueWd(10 + i, i);
    STAMP(6);
  }
  auto h_done = [&]() {
    WAITV(0);
    if (idx < 28 && lane == 0) {
      const unsigned n = __hip_atomic_fetch_add((LDS unsigned*)L_CNT, 1u, __ATOMIC_RELAXED, __HIP_MEMORY_SCOPE_WORKGROUP);
      if (n == 7) __hip_atomic_store((GLB u64*)(ws + WS_HF + (4 * (xcd >> 1) + (idx & 3)) * 128 + (7 * (xcd & 1) + (idx >> 2)) * 8), tag, __ATOMIC_RELAXED, __HIP_MEMORY_SCOPE_AGENT);
    }
  };

  const unsigned NEG = 0x80008000u, sgc = (c & 1) ? NEG : 0u;
  const u32x4 c64h = fc.c64 ^ sgc, s64h = fc.s64 ^ sgc, n64 = fc.s64 ^ NEG, n64h = s64h ^ NEG;
  if (w < 4 || special) {
    const int T = w & 3;
    u32x4 zf[2][2][2];
    const unsigned za = L_Z + c * 128 + ((g ^ (c >> 1)) * 16) + (w >> 2) * 4096;
    UNROLL for (int bt = 0; bt < 2; ++bt) UNROLL for (int hh = 0; hh < 2; ++hh) {
      zf[0][bt][hh] = rd128((za ^ (hh * 64)) + bt * 2048);
      zf[1][bt][hh] = rd128((za ^ (hh * 64)) + bt * 2048 + 4096);
    }
    unsigned wv[8];
    const unsigned wa = L_W0 + (w >> 2) * 16384 + g * 1024 + (((16 * T + c) ^ ((g & 1) * 16)) * 4);
    UNROLL for (int j = 0; j < 8; ++j) wv[j] = *(LDS unsigned*)(wa + (j >> 2) * 4096 + (j & 3) * 256);
    if (special) {
      UNROLL for (int bt = 0; bt < 2; ++bt) UNROLL for (int hh = 0; hh < 2; ++hh) zf[1][bt][hh] = u32x4{0, 0, 0, 0};
      BARRIER();
    }
    f32x4 re[2], im[2];
    UNROLL for (int bt = 0; bt < 2; ++bt) {
      re[bt] = mfma(zf[0][bt][0], fc.c64, f32x4{0, 0, 0, 0}); im[bt] = mfma(zf[0][bt][0], n64, f32x4{0, 0, 0, 0});
      re[bt] = mfma(zf[0][bt][1], c64h, re[bt]);             im[bt] = mfma(zf[0][bt][1], n64h, im[bt]);
      re[bt] = mfma(zf[1][bt][0], fc.s64, re[bt]);           im[bt] = mfma(zf[1][bt][0], fc.c64, im[bt]);
      re[bt] = mfma(zf[1][bt][1], s64h, re[bt]);             im[bt] = mfma(zf[1][bt][1], c64h, im[bt]);
    }
    UNROLL for (int j = 0; j < 8; ++j) {
      const float a = re[j >> 2][j & 3], b = im[j >> 2][j & 3];
      re[j >> 2][j & 3] = a * fc.t1c[j] + b * fc.t1s[j]; im[j >> 2][j & 3] = b * fc.t1c[j] - a * fc.t1s[j];
    }
    u32x4 pr = pk8(re[0], re[1]), pi = pk8(im[0], im[1]);
    h_done();
    const u32x4 c32h = fc.c32 ^ 0x80000000u, s32h = fc.s32 ^ 0x80000000u, n32 = fc.s32 ^ NEG, n32h = fc.s32 ^ 0x00008000u;
    re[0] = mfma(fc.c32, pr, f32x4{0, 0, 0, 0}); im[0] = mfma(fc.c32, pi, f32x4{0, 0, 0, 0});
    re[1] = mfma(c32h, pr, f32x4{0, 0, 0, 0});   im[1] = mfma(c32h, pi, f32x4{0, 0, 0, 0});
    re[0] = mfma(fc.s32, pi, re[0]); im[0] = mfma(n32, pr, im[0]);
    re[1] = mfma(s32h, pi, re[1]);   im[1] = mfma(n32h, pr, im[1]);
    UNROLL for (int j = 0; j < 8; ++j) {
      const float a = re[j >> 2][j & 3], b = im[j >> 2][j & 3], fr = lo16(wv[j]), fi = hi16(wv[j]);
      re[j >> 2][j & 3] = a * fr - b * fi; im[j >> 2][j & 3] = a * fi + b * fr;
    }
    pr = pk8(re[0], re[1]); pi = pk8(im[0], im[1]);
    re[0] = mfma(pr, fc.c32, f32x4{0, 0, 0, 0}); im[0] = mfma(pr, fc.s32, f32x4{0, 0, 0, 0});
    re[1] = mfma(pr, c32h, f32x4{0, 0, 0, 0});   im[1] = mfma(pr, s32h, f32x4{0, 0, 0, 0});
    re[0] = mfma(pi, n32, re[0]);  im[0] = mfma(pi, fc.c32, im[0]);
    re[1] = mfma(pi, n32h, re[1]); im[1] = mfma(pi, c32h, im[1]);
    const unsigned qa = ((w >> 2) ? L_Z : L_Q0) + (T >> 1) * 1024 + lane * 16 + (T & 1) * 8;
    UNROLL for (int bt = 0; bt < 2; ++bt) {
      float qr[4], qi[4];
      UNROLL for (int r = 0; r < 4; ++r) {
        const float a = re[bt][r], b = im[bt][r], tc_ = fc.t2c[4 * bt + r], ts_ = fc.t2s[4 * bt + r];
        qr[r] = a * tc_ - b * ts_; qi[r] = a * ts_ + b * tc_;
      }
      *(LDS u32x2*)(qa + bt * 2048) = u32x2{pk(qr[0], qr[1]), pk(qr[2], qr[3])};
      *(LDS u32x2*)(qa + bt * 2048 + 4096) = u32x2{pk(qi[0], qi[1]), pk(qi[2], qi[3])};
    }
  }
  else h_done();
  BARRIER();
  STAMP(7);
  u32x4 yout;
  {
    const unsigned qa = L_Q0 + (w >> 2) * 2048 + lane * 16;
    u32x4 r0 = rd128(qa), r1 = rd128(qa + 1024), i0 = rd128(qa + 4096), i1 = rd128(qa + 5120);
    f32x4 yr = mfma(fc.c64, r0, f32x4{0, 0, 0, 0}), yi;
    yr = mfma(c64h, r1, yr); yr = mfma(n64, i0, yr); yr = mfma(n64h, i1, yr);
    if (special) {
      const unsigned qb = qa - 16384;
      r0 = rd128(qb); r1 = rd128(qb + 1024); i0 = rd128(qb + 4096); i1 = rd128(qb + 5120);
      yi = mfma(fc.c64, r0, f32x4{0, 0, 0, 0}); yi = mfma(c64h, r1, yi); yi = mfma(n64, i0, yi); yi = mfma(n64h, i1, yi);
    } else {
      yi = mfma(fc.s64, r0, f32x4{0, 0, 0, 0}); yi = mfma(s64h, r1, yi); yi = mfma(fc.c64, i0, yi); yi = mfma(c64h, i1, yi);
    }
    yout = u32x4{pk(yr.x, yi.x), pk(yr.y, yi.y), pk(yr.z, yi.z), pk(yr.w, yi.w)};
  }
  if (w == 7) wait_tag(ws, WS_HF + (bo >> 1) * 128 + (c < 14 ? c : 13) * 8, tag);
  BARRIER();
  STAMP(8);
  {
    const unsigned sH = WS_H + (bo * 56 + 14 * kq) * 4096 + th * 2048;
    u32x4 hb[14][2];
    auto ldh = [&](int i) { ald128(hb[i][0], rS, lane * 16, sH + i * 4096); ald128(hb[i][1], rS, lane * 16, sH + i * 4096 + 1024); };
    ldh(0); ldh(1);
    st128(yout, rS, c * 65536 + g * 16, WS_Y + (w >> 2) * 1048576 + col * 256 + (w & 3) * 64);
    issueWd(8, 8); issueWd(9, 9);
    unsigned G[4][16];
    auto genG = [&](int ct) {
      const float st = (float)(64 * nb + 16 * ct + c) * (1.f / 512.f), a0 = __builtin_amdgcn_fractf(st * (float)(64 * kq + 16 * g));
      f32x2 p0 = {fcos(a0), fsin(-a0)}, p1 = {fcos(a0 + st), fsin(-a0 - st)};
      const f32x2 m = sp(2.f * fcos(st));
      G[ct][0] = (kq == 0 && g == 0) ? ((c & 1) ? 0xbf803f80u : 0x3f803f80u) : pk2(p0);
      G[ct][1] = pk2(p1);
      UNROLL for (int n = 2; n < 16; ++n) { const f32x2 p2 = fma2(m, p1, -p0); G[ct][n] = pk2(p2); p0 = p1; p1 = p2; }
      UNROLL for (int n = 0; n < 16; n += 4) asm volatile("" : "+v"(G[ct][n]), "+v"(G[ct][n + 1]), "+v"(G[ct][n + 2]), "+v"(G[ct][n + 3]));
    };
    genG(0); genG(1);
    SCHED();
    WAITV_(0, "+v"(hb[0][0]), "+v"(hb[0][1]), "+v"(hb[1][0]), "+v"(hb[1][1]));
    STAMP(9);
    ldh(2); ldh(3); ldh(4); ldh(5);
    f32x4 acc[4][2];
    UNROLL for (int ct = 0; ct < 4; ++ct) acc[ct][0] = acc[ct][1] = f32x4{0, 0, 0, 0};
    u32x2 yv[16], yf;
    u32x4 xres, A[2][4];
    UNROLL for (int i = 0; i < 14; ++i) {
      if (i >= 3 && i < 13) {
        const unsigned so = (i + 1 < 10 ? i + 1 : i - 9) * 16384;
        UNROLL for (int ct = 0; ct < 4; ++ct) A[(i + 1) & 1][ct] = rdtr(aD[ct] + so, aD[ct] + so + 2048);
      }
      if (i >= 2 && i <= 5) WAITV_(6, "+v"(hb[i][0]), "+v"(hb[i][1]));
      if (i >= 6 && i <= 8) WAITV_(7, "+v"(hb[i][0]), "+v"(hb[i][1]));
      if (i == 9) WAITV_(6, "+v"(hb[i][0]), "+v"(hb[i][1]), "+v"(yf));
      if (i == 10) WAITV_(23, "+v"(hb[i][0]), "+v"(hb[i][1]));
      if (i == 11) WAITV_(21, "+v"(hb[i][0]), "+v"(hb[i][1]));
      if (i == 12) WAITV_(19, "+v"(hb[i][0]), "+v"(hb[i][1]));
      if (i == 13) WAITV_(17, "+v"(hb[i][0]), "+v"(hb[i][1]));
      UNROLL for (int ct = 0; ct < 4; ++ct) {
        const u32x4 a = i < 4 ? park[i][ct] : A[i & 1][ct];
        acc[ct][0] = mfma(a, hb[i][0], acc[ct][0]); acc[ct][1] = mfma(a, hb[i][1], acc[ct][1]);
      }
      SCHED();
      if (i == 1) { BARRIER(); put_tag(ws, WS_YF + col * 8, tag); genG(2); }
      if (i == 2) genG(3);
      if (i == 8) BARRIER();
      if (i == 5) ald64(yf, rS, lane * 8, WS_YF + 512 * kq);
      if (i >= 2 && i + 4 < 14) ldh(i + 4);
      if (i == 9) {
        if (!__all(yf.x == (unsigned)tag && yf.y == (unsigned)(tag >> 32))) wait_tag(ws, WS_YF + (64 * kq + lane) * 8, tag);
        STAMP(10);
        UNROLL for (int n = 0; n < 16; ++n) ald64(yv[n], rS, g * 4096 + c * 8, WS_Y + bo * 65536 + kq * 16384 + th * 128 + n * 256);
        ald128p<0>(xres, rX, (tid >> 3) * 32768 + (tid & 7) * 16, bo * 1024 + nb * 128);
      }
      SCHED();
    }
    UNROLL for (int s = 0; s < 4; ++s) {
      if (s == 0) WAITV_(13, "+v"(yv[0]), "+v"(yv[1]), "+v"(yv[2]), "+v"(yv[3]));
      if (s == 1) WAITV_(9, "+v"(yv[4]), "+v"(yv[5]), "+v"(yv[6]), "+v"(yv[7]));
      if (s == 2) WAITV_(5, "+v"(yv[8]), "+v"(yv[9]), "+v"(yv[10]), "+v"(yv[11]));
      if (s == 3) WAITV_(0, "+v"(yv[12]), "+v"(yv[13]), "+v"(yv[14]), "+v"(yv[15]), "+v"(xres));
      const u32x4 y0 = {yv[4 * s].x, yv[4 * s + 1].x, yv[4 * s + 2].x, yv[4 * s + 3].x}, y1 = {yv[4 * s].y, yv[4 * s + 1].y, yv[4 * s + 2].y, yv[4 * s + 3].y};
      UNROLL for (int ct = 0; ct < 4; ++ct) {
        const u32x4 a = {G[ct][4 * s], G[ct][4 * s + 1], G[ct][4 * s + 2], G[ct][4 * s + 3]};
        acc[ct][0] = mfma(a, y0, acc[ct][0]); acc[ct][1] = mfma(a, y1, acc[ct][1]);
      }
    }
    STAMP(11);
    UNROLL for (int ct = 0; ct < 4; ++ct) UNROLL for (int t = 0; t < 2; ++t)
      wr128(65536 + kq * 16384 + (32 * th + 2 * c + t) * 256 + (((4 * ct + g) ^ (c & 7)) * 16), __builtin_bit_cast(u32x4, acc[ct][t]));
    BARRIER();
    const unsigned ra = 65536 + (tid >> 3) * 256 + (((2 * (tid & 7)) ^ ((tid >> 4) & 7)) * 16);
    f32x4 o0 = {lo16(xres.x), hi16(xres.x), lo16(xres.y), hi16(xres.y)}, o1 = {lo16(xres.z), hi16(xres.z), lo16(xres.w), hi16(xres.w)};
    UNROLL for (int k = 0; k < 4; ++k) {
      o0 += __builtin_bit_cast(f32x4, rd128(ra + k * 16384));
      o1 += __builtin_bit_cast(f32x4, rd128((ra ^ 16) + k * 16384));
    }
    *(GLB u32x4*)(out + (size_t)((tid >> 3) * 32 + bo) * 1024 + nb * 128 + (tid & 7) * 16) = pk8(o0, o1);
    if (bid == 0) put_tag(ws, 0, epoch + 1);
    STAMP(12); DIAG_END();
  }
}
}

extern "C" void kernel_launch(void* const* d_in, const int* in_sizes, int n_in, void* d_out, int out_size, void* d_ws, size_t ws_size,
                              hipStream_t stream) {
  (void)in_sizes; (void)n_in; (void)out_size; (void)ws_size;
  (void)hipFuncSetAttribute((const void*)fused_kernel, hipFuncAttributeMaxDynamicSharedMemorySize, (int)LDS_BYTES);
  fused_kernel<<<256, 512, LDS_BYTES, stream>>>((const char*)d_in[0], (const char*)d_in[1], (const char*)d_in[2], (const char*)d_in[4],
                                                (const char*)d_in[5], (const char*)d_in[6], (char*)d_out, (char*)d_ws);
}
```

```cpp
#define DIAG
#define TOUCH 8
#include <hip/hip_runtime.h>
#pragma clang diagnostic ignored "-Wint-to-pointer-cast"

#define TOUCH2 0
#define NPRE0 4
#define T2POS 3
#define KSTART 0
namespace {
typedef __bf16 bf16x8 __attribute__((ext_vector_type(8)));
typedef __bf16 bf16x2 __attribute__((ext_vector_type(2)));
typedef float f32x4 __attribute__((ext_vector_type(4)));
typedef float f32x2 __attribute__((ext_vector_type(2)));
typedef unsigned u32x4 __attribute__((ext_vector_type(4)));
typedef unsigned u32x2 __attribute__((ext_vector_type(2)));
typedef short s16x4 __attribute__((ext_vector_type(4)));
typedef unsigned long long u64;
typedef __amdgpu_buffer_rsrc_t rsrc_t;
#define LDS __attribute__((address_space(3)))
#define GLB __attribute__((address_space(1)))
#define DI __device__ __forceinline__
#define UNROLL _Pragma("unroll")

constexpr unsigned WS_ZF = 256, WS_SF = 3072, WS_HF = 512, WS_YF = 4096, WS_ST = 8192, WS_Z = 1u << 20, WS_WT = 3u << 20, WS_Y = 6u << 20,
                   WS_H = 8u << 20, WS_END = 16u << 20;
constexpr unsigned LDS_BYTES = 163840;
constexpr unsigned L_Z = 131072, L_W0 = L_Z + 8192, L_Q0 = L_Z + 16384, L_W1 = L_Z + 24576;
constexpr unsigned L_WR = 65536, L_WI = L_WR + 33792, L_BN = L_WI + 33792;
constexpr int SC1 = 16;

DI unsigned pk(float a, float b) { f32x2 v = {a, b}; return __builtin_bit_cast(unsigned, __builtin_convertvector(v, bf16x2)); }
DI float lo16(unsigned v) { return __uint_as_float(v << 16); }
DI float hi16(unsigned v) { return __uint_as_float(v & 0xffff0000u); }
DI f32x4 mfma(u32x4 a, u32x4 b, f32x4 c) {
  return __builtin_amdgcn_mfma_f32_16x16x32_bf16(__builtin_bit_cast(bf16x8, a), __builtin_bit_cast(bf16x8, b), c, 0, 0, 0);
}
DI u32x4 pk8(f32x4 a, f32x4 b) { u32x4 r = {pk(a.x, a.y), pk(a.z, a.w), pk(b.x, b.y), pk(b.z, b.w)}; return r; }
DI u32x4 rd128(unsigned a) { return *(LDS u32x4*)a; }
DI void wr128(unsigned a, u32x4 v) { *(LDS u32x4*)a = v; }
DI u32x4 rdtr(unsigned a0, unsigned a1) {
  u32x2 p = __builtin_bit_cast(u32x2, __builtin_amdgcn_ds_read_tr16_b64_v4i16((LDS s16x4*)a0));
  u32x2 q = __builtin_bit_cast(u32x2, __builtin_amdgcn_ds_read_tr16_b64_v4i16((LDS s16x4*)a1));
  u32x4 r = {p.x, p.y, q.x, q.y};
  return r;
}
DI void dma(rsrc_t rs, unsigned lds, unsigned voff, unsigned soff) {
  asm volatile("s_mov_b32 m0, %0\n\ts_nop 0\n\tbuffer_load_dwordx4 %1, %2, %3 offen lds" ::"s"(lds), "v"(voff), "s"(rs), "s"(soff) : "memory");
}
DI void touch(rsrc_t rs, unsigned lds, unsigned voff) {
  asm volatile("s_mov_b32 m0, %0\n\ts_nop 0\n\tbuffer_load_dword %1, %2, 0 offen lds" ::"s"(lds), "v"(voff), "s"(rs) : "memory");
}
DI void st128(u32x4 v, rsrc_t rs, unsigned voff, unsigned soff) {
  asm volatile("buffer_store_dwordx4 %0, %1, %2, %3 offen sc1\n\ts_nop 1" ::"v"(v), "v"(voff), "s"(rs), "s"(soff) : "memory");
}
DI void ald64(u32x2& v, rsrc_t rs, unsigned voff, unsigned soff) {
  asm volatile("buffer_load_dwordx2 %0, %1, %2, %3 offen sc1" : "=v"(v) : "v"(voff), "s"(rs), "s"(soff) : "memory");
}
DI void ald128(u32x4& v, rsrc_t rs, unsigned voff, unsigned soff) {
  asm volatile("buffer_load_dwordx4 %0, %1, %2, %3 offen sc1" : "=v"(v) : "v"(voff), "s"(rs), "s"(soff) : "memory");
}
template <int OFF> DI void ald128p(u32x4& v, rsrc_t rs, unsigned voff, unsigned soff) {
  asm volatile("buffer_load_dwordx4 %0, %1, %2, %3 offen offset:%4" : "=v"(v) : "v"(voff), "s"(rs), "s"(soff), "i"(OFF) : "memory");
}
DI u32x2 rdtr1(unsigned a) { return __builtin_bit_cast(u32x2, __builtin_amdgcn_ds_read_tr16_b64_v4i16((LDS s16x4*)a)); }
DI f32x2 lo2(f32x4 v) { return __builtin_shufflevector(v, v, 0, 1); }
DI f32x2 hi2(f32x4 v) { return __builtin_shufflevector(v, v, 2, 3); }
DI f32x2 fma2(f32x2 a, f32x2 b, f32x2 c) { return __builtin_elementwise_fma(a, b, c); }
DI f32x2 sp(float a) { return f32x2{a, a}; }
DI unsigned pk2(f32x2 v) { return __builtin_bit_cast(unsigned, __builtin_convertvector(v, bf16x2)); }
DI float fcos(float rev) { return __builtin_amdgcn_cosf(rev); }
DI float fsin(float rev) { return __builtin_amdgcn_sinf(rev); }
#define WAITV(n) asm volatile("s_waitcnt vmcnt(" #n ")" ::: "memory")
#define WAITV_(n, ...) asm volatile("s_waitcnt vmcnt(" #n ")" : __VA_ARGS__ :: "memory")
#define BARRIER() do { asm volatile("s_waitcnt lgkmcnt(0)" ::: "memory"); __builtin_amdgcn_s_barrier(); asm volatile("" ::: "memory"); } while (0)
#define SCHED() __builtin_amdgcn_sched_barrier(0)
#define NSTAMP 30
#define ISDFT(b) (((b) >> 3) >= 28)
#define STAMP(i) do { SCHED(); stp[i] = (unsigned)__builtin_amdgcn_s_memtime(); SCHED(); } while (0)
#define DIAG_BEGIN() unsigned stp[NSTAMP]; UNROLL for (int i_ = 0; i_ < NSTAMP; ++i_) stp[i_] = 0; const u64 rt0_ = __builtin_amdgcn_s_memrealtime(); STAMP(0)
#define DIAG_END() do { if ((tid & 255) == 0) { GLB u64* o_ = (GLB u64*)(ws + WS_END) + (blockIdx.x * 2 + (tid >> 8)) * 32; \
    UNROLL for (int i_ = 0; i_ < NSTAMP; ++i_) o_[i_] = stp[i_]; o_[30] = rt0_; o_[31] = __builtin_amdgcn_s_memrealtime(); } } while (0)

DI void wait_tag(char* ws, unsigned off, u64 tag) {
#pragma unroll 1
  for (int it = 0; it < (1 << 18); ++it) {
    u64 v = __hip_atomic_load((GLB u64*)(ws + off), __ATOMIC_RELAXED, __HIP_MEMORY_SCOPE_AGENT);
    if (__all(v == tag)) break;
    __builtin_amdgcn_s_sleep(2);
  }
  __builtin_amdgcn_fence(__ATOMIC_ACQUIRE, "wavefront");
  asm volatile("" ::: "memory");
}
DI void put_tag(char* ws, unsigned off, u64 tag, int who = 0) {
  if ((int)threadIdx.x == who) __hip_atomic_store((GLB u64*)(ws + off), tag, __ATOMIC_RELAXED, __HIP_MEMORY_SCOPE_AGENT);
}

struct FC { u32x4 c64, s64, c32, s32; float t1c[8], t1s[8], t2c[8], t2s[8]; };
DI void fft_consts(FC& k, int c, int g, int T, float scale, bool full) {
  const float j1 = (float)(16 * T + c) * (1.f / 64.f), g4 = (float)(4 * g);
  UNROLL for (int i = 0; i < 4; ++i) {
    const float o = (float)(i < 2 ? 2 * i : 12 + 2 * i);
    const f32x2 an = (sp(g4) + f32x2{o, o + 1.f}) * sp(j1);
    k.c64[i] = pk(fcos(an.x), fcos(an.y)); k.s64[i] = pk(fsin(an.x), fsin(an.y));
  }
  asm volatile("" : "+v"(k.c64), "+v"(k.s64));
  if (full) {
    const float cf = (float)c * (1.f / 32.f), j2 = j1 * (1.f / 32.f), r0 = (float)(16 * T) + g4;
    UNROLL for (int i = 0; i < 4; ++i) {
      const float o = (float)(i < 2 ? 2 * i : 12 + 2 * i);
      const f32x2 sl = sp(g4) + f32x2{o, o + 1.f}, a1 = sl * sp(cf), a2 = sl * sp(j2);
      const f32x2 a3 = (sp(r0) + f32x2{(float)(2 * (i & 1)), (float)(2 * (i & 1) + 1)}) * sp((cf + (float)(i >> 1) * 0.5f) * (1.f / 64.f));
      k.c32[i] = pk(fcos(a1.x), fcos(a1.y)); k.s32[i] = pk(fsin(a1.x), fsin(a1.y));
      k.t1c[2 * i] = fcos(a2.x); k.t1c[2 * i + 1] = fcos(a2.y); k.t1s[2 * i] = fsin(a2.x); k.t1s[2 * i + 1] = fsin(a2.y);
      const f32x2 cc = f32x2{fcos(a3.x), fcos(a3.y)} * sp(scale), ss = f32x2{fsin(a3.x), fsin(a3.y)} * sp(scale);
      k.t2c[2 * i] = cc.x; k.t2c[2 * i + 1] = cc.y; k.t2s[2 * i] = ss.x; k.t2s[2 * i + 1] = ss.y;
    }
    asm volatile("" : "+v"(k.c32), "+v"(k.s32));
    UNROLL for (int j = 0; j < 8; ++j) asm volatile("" : "+v"(k.t1c[j]), "+v"(k.t1s[j]), "+v"(k.t2c[j]), "+v"(k.t2s[j]));
  }
}
DI void dft_table(u32x4 (&t)[4], float kf, float ph, int g) {
  const float st = kf * (1.f / 512.f), a0 = st * (float)(8 * g) + ph;
  const f32x2 m2 = sp(2.f * fcos(st * 2.f)), m32 = sp(2.f * fcos(st * 32.f));
  f32x2 v[4][4];
  UNROLL for (int s = 0; s < 2; ++s) {
    const f32x2 p = fma2(sp(st), f32x2{32.f * s, 32.f * s + 1.f}, sp(a0)), q = fma2(sp(st), f32x2{32.f * s + 2.f, 32.f * s + 3.f}, sp(a0));
    v[s][0] = f32x2{fcos(p.x), fcos(p.y)}; v[s][1] = f32x2{fcos(q.x), fcos(q.y)};
    v[s][2] = fma2(m2, v[s][1], -v[s][0]); v[s][3] = fma2(m2, v[s][2], -v[s][1]);
  }
  UNROLL for (int i = 0; i < 4; ++i) { v[2][i] = fma2(m32, v[1][i], -v[0][i]); v[3][i] = fma2(m32, v[2][i], -v[1][i]); }
  UNROLL for (int s = 0; s < 4; ++s) { UNROLL for (int i = 0; i < 4; ++i) t[s][i] = pk2(v[s][i]); asm volatile("" : "+v"(t[s])); }
}
template <int CTRL> DI float dpp_add(float v) {
  return v + __builtin_bit_cast(float, __builtin_amdgcn_update_dpp(0, __builtin_bit_cast(int, v), CTRL, 0xf, 0xf, true));
}
DI float row16_sum(float v) { v = dpp_add<0x128>(v); v = dpp_add<0x124>(v); v = dpp_add<0x122>(v); return dpp_add<0x121>(v); }
DI float rbf(float v) { return lo16(pk(v, 0.f)); }

__global__ void __launch_bounds__(512, 2)
fused_kernel(const char* __restrict__ x, const char* __restrict__ wr, const char* __restrict__ wi, const char* __restrict__ wg,
             const char* __restrict__ wu, const char* __restrict__ wd, char* __restrict__ out, char* ws) {
  const int tid = threadIdx.x, lane = tid & 63, c = lane & 15, g = lane >> 4;
  const int w = __builtin_amdgcn_readfirstlane(tid >> 6);
  const int bid = blockIdx.x, xcd = bid & 7, idx = bid >> 3;
  const rsrc_t rX = __builtin_amdgcn_make_buffer_rsrc((void*)x, 0, 2048 * 512 * 2, 0x00020000);
  const rsrc_t rD = __builtin_amdgcn_make_buffer_rsrc((void*)wd, 0, 1792 * 512 * 2, 0x00020000);
  const rsrc_t rS = __builtin_amdgcn_make_buffer_rsrc((void*)ws, 0, WS_END, 0x00020000);
  DIAG_BEGIN();
  const int col = (bid + 1) & 255;
  const bool special = (col == 0);
  const int bo = 4 * xcd + (idx >> 3), nb = idx & 7;
  const float yscale = special ? (1.f / 1048576.f) : (2.f / 1048576.f);
  const unsigned vWd = (unsigned)(((14 * (w >> 1)) * 32 + ((16 * (w & 1) + (lane >> 3)) ^ (16 * (bo & 1)))) * 1024 + nb * 128 +
                                  ((((lane & 7) >> 1) ^ g) * 32) + (lane & 1) * 16);
  auto issueWd = [&](int step, int slot) {
    dma(rD, slot * 16384 + w * 2048, vWd, step * 32768);
    dma(rD, slot * 16384 + w * 2048 + 1024, vWd, step * 32768 + 8192);
  };
  u64 epoch, tag;
  u32x2 ev;
  auto make_tag = [&]() {
    epoch = ((u64)__builtin_amdgcn_readfirstlane(ev.y) << 32) | (unsigned)__builtin_amdgcn_readfirstlane(ev.x);
    tag = epoch * 0x9E3779B97F4A7C15ull + 0x632BE59BD9B4E019ull;
  };
  FC fc;
  const int th = w >> 2, kq = w & 3, q4 = (lane >> 2) & 3, p4 = lane & 3;
  unsigned aD[4];
  UNROLL for (int ct = 0; ct < 4; ++ct) aD[ct] = kq * 4096 + (4 * g + q4) * 128 + ((ct ^ ((2 * g + (q4 >> 1)) & 3)) * 32) + 8 * p4;
  u32x4 park[4][4];
  auto read_park = [&](int i) { UNROLL for (int ct = 0; ct < 4; ++ct) park[i][ct] = rdtr(aD[ct] + i * 16384, aD[ct] + i * 16384 + 2048); };
  auto stZ = [&](int t) { return L_Z + (t >> 3) * 128 + (((t & 7) ^ ((t >> 4) & 7)) * 16); };
  auto stW = [&](int t) { return (t >> 4) * 256 + (((t & 15) ^ (((t >> 6) & 1) * 4)) * 16); };
  constexpr unsigned L_CNT = L_W1 + 4096;
  const rsrc_t rG = __builtin_amdgcn_make_buffer_rsrc((void*)wg, 0, 512 * 1792 * 2, 0x00020000);
  const rsrc_t rU = __builtin_amdgcn_make_buffer_rsrc((void*)wu, 0, 512 * 1792 * 2, 0x00020000);
  const rsrc_t rR = __builtin_amdgcn_make_buffer_rsrc((void*)wr, 0, 2048 * 257 * 2, 0x00020000);
  const rsrc_t rI = __builtin_amdgcn_make_buffer_rsrc((void*)wi, 0, 2048 * 257 * 2, 0x00020000);
  auto touches = [&](int mask, unsigned lds) {
    const int l56 = lane < 56 ? lane : 55, wrow = (l56 * 37) >> 9;
    if (w == 0 && (mask & 1)) touch(rX, lds, ((32 * (xcd & 1) + idx) * 32 + 8 * (xcd >> 1)) * 1024 + lane * 128);
    if (w == 1 && (mask & 2)) touch(rG, lds + 256, (wrow * 128 + (xcd >> 1) * 32 + idx) * 3584 + (xcd & 1) * 1792 + (l56 - 14 * wrow) * 128);
    if (w == 2 && (mask & 4)) touch(rU, lds + 512, (wrow * 128 + (xcd >> 1) * 32 + idx) * 3584 + (xcd & 1) * 1792 + (l56 - 14 * wrow) * 128);
    if (w == 3 && (mask & 8)) touch(rD, lds + 768, (bid * 56 + l56) * 128);
    if (w == 4 && (mask & 16)) touch(rR, lds + 1024, xcd * 131584 + (idx * 32 + (lane < 36 ? lane : 35)) * 128);
    if (w == 5 && (mask & 16)) touch(rI, lds + 1280, xcd * 131584 + (idx * 32 + (lane < 36 ? lane : 35)) * 128);
  };

  if (idx < 28) {
    const int rb = 4 * (xcd >> 1) + (idx & 3), cb = 7 * (xcd & 1) + (idx >> 2);
    const int wt = w >> 2, wf = w & 3;
    const int xr = 8 * w + (lane >> 3);
    const int xa = 32 * (xr >> 5) + 2 * (xr & 15) + ((xr >> 4) & 1);
    const unsigned vX = (unsigned)((32 * xa + 2 * rb) * 1024 + (((lane & 7) ^ ((xr >> 1) & 7)) * 16));
    const unsigned vW = (unsigned)((8 * w + g) * 3584 + cb * 256 + (((c >> 1) ^ (g | ((w & 1) << 2))) * 32) + (c & 1) * 16);
    const int kstart = KSTART;
    auto slot = [](int ch) { return 16384u + ((ch + 1) % 3) * 49152u; };
    auto issue = [&](int n) {
      const int ch = n / 6, pc = n % 6;
      if (ch < 8) {
        const int kc = (ch + kstart) & 7, hf = pc / 3;
        if (pc % 3 == 0) dma(rX, slot(ch) + hf * 8192 + w * 1024, vX, kc * 128 + hf * 1024);
        else dma(pc % 3 == 1 ? rG : rU, slot(ch) + (pc % 3) * 16384 + w * 2048 + hf * 1024, vW, kc * 229376 + hf * 14336);
      } else {
        const int st = 1 + (n - 48) / 2, hf = n & 1;
        dma(rD, st * 16384 + w * 2048 + hf * 1024, vWd, st * 32768 + hf * 8192);
      }
    };
    touches(TOUCH, 0);
    ald64(ev, rS, 0, 0);
    UNROLL for (int n = 0; n < NPRE0; ++n) issue(n);
    fft_consts(fc, c, g, w & 3, yscale, w < 4);
    STAMP(13);
    UNROLL for (int n = NPRE0; n < 10; ++n) issue(n);
    const int sw = q4 | ((g & 1) << 2);
    const unsigned aB0 = (64 * wt + c) * 128 + ((g ^ (c >> 1)) * 16), aB1 = aB0 ^ 64;
    const unsigned aA0 = 16384 + (8 * g + q4) * 256 + (((2 * wf + wt) ^ sw) * 32) + 8 * p4, aA1 = aA0 ^ 32;
    f32x4 acc[4][4], cs[2];
    UNROLL for (int m = 0; m < 4; ++m) UNROLL for (int t = 0; t < 4; ++t) acc[m][t] = f32x4{0, 0, 0, 0};
    cs[0] = cs[1] = f32x4{0, 0, 0, 0};
    u32x4 ones = {0x3f803f80u, 0x3f803f80u, 0x3f803f80u, 0x3f803f80u};
    asm volatile("" : "+v"(ones));
    u32x4 A[2][4], B[2][4];
    auto rdgrp = [&](int gi, int ks, unsigned base) {
      const unsigned b = base + (ks ? aB1 : aB0), a0 = base + aA0 + ks * 8192, a1 = base + aA1 + ks * 8192;
      if (gi == 0) { A[ks][0] = rdtr(a0, a0 + 1024); B[ks][0] = rd128(b); }
      if (gi == 1) { B[ks][1] = rd128(b + 2048); B[ks][2] = rd128(b + 4096); B[ks][3] = rd128(b + 6144); }
      if (gi == 2) { A[ks][2] = rdtr(a0 + 16384, a0 + 17408); A[ks][1] = rdtr(a1, a1 + 1024); }
      if (gi == 3) { A[ks][3] = rdtr(a1 + 16384, a1 + 17408); }
    };
    auto mm1 = [&](int i, int ks) {
      if (i == 4) cs[0] = mfma(A[ks][0], ones, cs[0]);
      else if (i == 9) cs[1] = mfma(A[ks][2], ones, cs[1]);
      else { const int m = i < 4 ? 0 : i < 9 ? 2 : i < 14 ? 1 : 3, t = i < 4 ? i : i < 9 ? i - 5 : (i - 10) & 3; acc[m][t] = mfma(A[ks][m], B[ks][t], acc[m][t]); }
    };
    STAMP(1);
    WAITV_(4, "+v"(ev)); BARRIER();
    STAMP(2);
    make_tag();
    u32x2 sf; u32x4 st;
    UNROLL for (int gi = 0; gi < 4; ++gi) rdgrp(gi, 0, slot(0));
    SCHED();
    UNROLL for (int hp = 0; hp < 16; ++hp) {
      const int ks = hp & 1, nx = hp + 1;
      if (hp == T2POS) touches(TOUCH2, 0);
      if (hp == 9) issueWd(0, 0);
      if (hp == 11) ald64(sf, rS, 0, WS_SF + (2 * rb + wt) * 8);
      if (hp == 13) ald128(st, rS, c * 16, WS_ST + (2 * rb + wt) * 256);
      UNROLL for (int gi = 0; gi < 6; ++gi) {
        if (gi < 4) { if (nx < 16) rdgrp(gi, nx & 1, slot(nx >> 1)); else read_park(gi); }
        UNROLL for (int i = 0; i < 3; ++i) mm1(3 * gi + i, ks);
        UNROLL for (int i = 0; i < 3; ++i) { __builtin_amdgcn_sched_group_barrier(0x008, 1, 0); if (nx < 16) __builtin_amdgcn_sched_group_barrier(0x100, 1, 0); else __builtin_amdgcn_sched_group_barrier(0x100, 3, 0); }
        SCHED();
        if (hp == 0) { issue(10 + gi); if (gi >= 4) issue(12 + gi); }
        else if (hp < 15) { if ((gi & 1) == 0) issue(15 + 3 * hp + gi / 2); }
        else if (gi == 0 || gi == 2) issue(60 + gi / 2);
        if (hp == 15 && gi == 3 && c == 0) { wr128(L_W1 + w * 128 + g * 16, __builtin_bit_cast(u32x4, cs[0])); wr128(L_W1 + w * 128 + 64 + g * 16, __builtin_bit_cast(u32x4, cs[1])); }
        if (hp == 15 && gi == 3 && tid == 0) *(LDS unsigned*)L_CNT = 0;
        SCHED();
      }
      if (ks == 0) {
        if (hp == 12) {
          WAITV_(6, "+v"(sf));
          if (__builtin_amdgcn_readfirstlane(sf.x) != (unsigned)tag || __builtin_amdgcn_readfirstlane(sf.y) != (unsigned)(tag >> 32))
            wait_tag(ws, WS_SF + (2 * rb + wt) * 8, tag);
        } else if (hp == 14) WAITV_(6, "+v"(st));
        else { STAMP(22 + (hp >> 1)); WAITV(6); }
        BARRIER();
        STAMP(14 + (hp >> 1));
        SCHED();
      }
    }
    BARRIER();
    STAMP(3);
    const f32x4 cg1 = __builtin_bit_cast(f32x4, rd128(L_W1 + (w ^ 4) * 128 + g * 16)), cu1 = __builtin_bit_cast(f32x4, rd128(L_W1 + (w ^ 4) * 128 + 64 + g * 16));
    if (w >= 4) {
      wait_tag(ws, WS_ZF + (lane & 31) * 8, tag);
      STAMP(4);
      u32x4 z0, z1, w0, w1;
      ald128(z0, rS, tid * 16, WS_Z + col * 8192 - 4096); ald128(z1, rS, tid * 16, WS_Z + col * 8192);
      ald128(w0, rS, tid * 16, WS_WT + col * 8192 - 4096); ald128(w1, rS, tid * 16, WS_WT + col * 8192);
      UNROLL for (int i = 0; i < 4; ++i) issueWd(10 + i, i);
      WAITV_(8, "+v"(z0), "+v"(z1), "+v"(w0), "+v"(w1));
      wr128(stZ(tid) - 4096, z0); wr128(stZ(tid), z1); wr128(L_W0 + stW(tid) - 4096, w0); wr128(L_W0 + stW(tid), w1);
      BARRIER();
      __builtin_amdgcn_s_setprio(1);
    }
    STAMP(5);
    UNROLL for (int t = 0; t < 4; ++t) {
      const float sd = hi16(st[t]), rs = __builtin_amdgcn_rcpf(sd);
      const f32x2 nm = sp(-lo16(st[t])), k1 = sp(-2.302208198f * rs), k3 = sp(-0.102943240f * rs * rs * rs), v2 = sp(sd * sd);
      u32x4 h;
      UNROLL for (int i = 0; i < 4; ++i) {
        const int m = i >> 1;
        const f32x4 ag = acc[m][t], au = acc[2 + m][t], sg = m ? cg1 : cs[0], su = m ? cu1 : cs[1];
        const f32x2 tg = fma2(nm, (i & 1) ? hi2(sg) : lo2(sg), (i & 1) ? hi2(ag) : lo2(ag)), tu = fma2(nm, (i & 1) ? hi2(su) : lo2(su), (i & 1) ? hi2(au) : lo2(au));
        const f32x2 ar = tg * fma2(k3, tg * tg, k1);
        const f32x2 dn = fma2(f32x2{__builtin_amdgcn_exp2f(ar.x), __builtin_amdgcn_exp2f(ar.y)}, v2, v2);
        h[i] = pk2(tg * tu * f32x2{__builtin_amdgcn_rcpf(dn.x), __builtin_amdgcn_rcpf(dn.y)});
      }
      st128(h, rS, lane * 16, WS_H + ((2 * rb + wt) * 56 + 4 * cb + wf) * 4096 + t * 1024);
    }
    STAMP(6);
    if (w < 4) {
      BARRIER();
      UNROLL for (int i = 0; i < 4; ++i) issueWd(10 + i, i);
    } else {
      __builtin_amdgcn_s_setprio(0);
    }
  } else {
    const int b = 4 * xcd + (idx - 28);
    touches(TOUCH | TOUCH2, L_BN + 20480);
    u32x4 xv[2][4];
    u32x4 tc[4], ts[4], t0[4], t2[4];
    const float ko = (float)(32 * w + 2 * c + 1), ke = (float)(32 * w + 4 * (c & 7)), ph = (c & 8) ? 0.25f : 0.f;
    const bool nyq = (w == 0 && c == 8);
    const unsigned vx = g * 32768 + c * 16, sx = (256 * w + b) * 1024;
    ald128p<0>(xv[0][0], rX, vx, sx); ald128p<256>(xv[0][1], rX, vx, sx); ald128p<512>(xv[0][2], rX, vx, sx); ald128p<768>(xv[0][3], rX, vx, sx);
    SCHED();
    dft_table(tc, ko, 0.f, g); dft_table(ts, ko, 0.25f, g);
    SCHED();
    ald128p<0>(xv[1][0], rX, vx, sx + 131072); ald128p<256>(xv[1][1], rX, vx, sx + 131072); ald128p<512>(xv[1][2], rX, vx, sx + 131072); ald128p<768>(xv[1][3], rX, vx, sx + 131072);
    ald64(ev, rS, 0, 0);
    SCHED();
    dft_table(t0, nyq ? 256.f : ke, nyq ? 0.f : ph, g); dft_table(t2, ke + 2.f, ph, g);
    SCHED();
    STAMP(1);
    UNROLL for (int tp = 0; tp < 2; ++tp) {
      UNROLL for (int i = 0; i < 4; ++i) dma(tp ? rI : rR, (tp ? L_WI : L_WR) + (4 * w + i) * 1024, lane * 16, b * 32896 + (4 * w + i) * 1024);
      if (tp == 0) WAITV_(9, "+v"(xv[0][0]), "+v"(xv[0][1]), "+v"(xv[0][2]), "+v"(xv[0][3]));
      else WAITV_(8, "+v"(xv[1][0]), "+v"(xv[1][1]), "+v"(xv[1][2]), "+v"(xv[1][3]), "+v"(ev));
      f32x2 o[4][4], sum = {0.f, 0.f};
      UNROLL for (int i = 0; i < 4; ++i) {
        f32x2 v[4];
        UNROLL for (int m = 0; m < 4; ++m) v[m] = f32x2{lo16(xv[tp][m][i]), hi16(xv[tp][m][i])};
        const f32x2 ua = v[0] + v[2], ub = v[1] + v[3];
        o[0][i] = v[0] - v[2]; o[1][i] = v[1] - v[3]; o[2][i] = ua + ub; o[3][i] = ua - ub;
        sum += o[2][i];
      }
      const float mean = row16_sum(sum.x + sum.y) * (1.f / 512.f);
      f32x2 q1 = {0.f, 0.f}, q2 = {0.f, 0.f};
      UNROLL for (int i = 0; i < 4; ++i) {
        o[2][i] -= sp(4.f * mean);
        q1 = fma2(o[0][i], o[0][i], q1); q1 = fma2(o[1][i], o[1][i], q1); q2 = fma2(o[2][i], o[2][i], q2); q2 = fma2(o[3][i], o[3][i], q2);
      }
      q1 = fma2(q2, sp(0.5f), q1);
      const float var = row16_sum(q1.x + q1.y) * (1.f / 1024.f);
      const float mb = rbf(mean), sd = rbf(__builtin_amdgcn_sqrtf(rbf(rbf(var) + 1e-5f)));
      const float rs = __builtin_amdgcn_rcpf(sd), dl = 4.f * (mean - mb) * rs;
      const int a = 8 * w + 4 * tp + g;
      if (c == 0) *(LDS unsigned*)(L_BN + 16384 + (((a & 31) >> 1) * 4 + 2 * (a >> 5) + (a & 1)) * 4) = pk(mb, sd);
      UNROLL for (int m = 0; m < 4; ++m) {
        u32x4 pv;
        UNROLL for (int i = 0; i < 4; ++i) pv[i] = pk2(m == 2 ? fma2(o[m][i], sp(rs), sp(dl)) : o[m][i] * sp(rs));
        wr128(m * 16384 + a * 256 + ((c ^ (a & 15)) * 16), pv);
      }
    }
    if (w == 0) dma(rR, L_WR + 32768, (lane & 7) * 16, b * 32896 + 32768);
    if (w == 1) dma(rI, L_WI + 32768, (lane & 7) * 16, b * 32896 + 32768);
    make_tag();
    STAMP(13);
    WAITV(0); BARRIER();
    STAMP(2);
    if (w == 3) __builtin_amdgcn_raw_buffer_store_b32(*(LDS unsigned*)(L_BN + 16384 + lane * 4), rS, lane * 4, WS_ST + b * 256, SC1);
    const unsigned bn = L_BN + w * 2048;
    auto putZ = [&](f32x4 (&d)[4], unsigned r0, unsigned r1) {
      wr128(bn + c * 128 + ((g ^ (c & 7)) * 16), pk8(d[0], d[1]));
      wr128(bn + c * 128 + (((4 + g) ^ (c & 7)) * 16), pk8(d[2], d[3]));
      const unsigned ra = bn + (lane >> 3) * 128 + (((lane & 7) ^ (lane >> 3)) * 16);
      const u32x4 p0 = rd128(ra), p1 = rd128(ra + 1024);
      st128(p0, rS, r0 * 4096 + (lane & 7) * 16, WS_Z + b * 128);
      st128(p1, rS, r1 * 4096 + (lane & 7) * 16, WS_Z + b * 128);
    };
    const int l8 = lane >> 3;
#pragma unroll 1
    for (int pass = 0; pass < 2; ++pass) {
      if (pass == (w >> 2)) {
        f32x4 d0[4], d2[4], dc[4], ds[4];
        const unsigned sg = (c & 1) ? 0x80008000u : 0u;
        UNROLL for (int t = 0; t < 4; ++t) d0[t] = d2[t] = dc[t] = ds[t] = f32x4{0, 0, 0, 0};
        UNROLL for (int s = 0; s < 4; ++s) {
          const u32x4 hc = ts[s] ^ sg, hs = tc[s] ^ (sg ^ 0x80008000u);
          UNROLL for (int t = 0; t < 4; ++t) {
            const unsigned a = c * 256 + (((4 * s + g) ^ c) * 16) + t * 4096;
            const u32x4 v0 = rd128(a), v1 = rd128(a + 16384);
            dc[t] = mfma(v0, tc[s], dc[t]); ds[t] = mfma(v0, ts[s], ds[t]);
            dc[t] = mfma(v1, hc, dc[t]); ds[t] = mfma(v1, hs, ds[t]);
          }
        }
        if (w == 3) { WAITV(0); put_tag(ws, WS_SF + b * 8, tag, 192); }
        putZ(dc, 2 * (32 * w + 2 * l8 + 1), 2 * (32 * w + 2 * l8 + 17));
        putZ(ds, 2 * (32 * w + 2 * l8 + 1) + 1, 2 * (32 * w + 2 * l8 + 17) + 1);
        UNROLL for (int s = 0; s < 4; ++s) UNROLL for (int t = 0; t < 4; ++t) {
          const unsigned a = c * 256 + (((4 * s + g) ^ c) * 16) + t * 4096;
          d0[t] = mfma(rd128(a + 32768), t0[s], d0[t]);
          d2[t] = mfma(rd128(a + 49152), t2[s], d2[t]);
        }
        putZ(d0, 2 * (32 * w + 4 * l8), 2 * (32 * w + 4 * l8) + 1);
        putZ(d2, 2 * (32 * w + 4 * l8 + 2), 2 * (32 * w + 4 * l8 + 2) + 1);
        STAMP(3);
      } else {
        const unsigned a = c * 2056 + 8 * w + 2 * g;
        u32x4 o[8];
        UNROLL for (int it = 0; it < 8; ++it) UNROLL for (int i = 0; i < 4; ++i)
          o[it][i] = (unsigned)*(LDS unsigned short*)(L_WR + a + i * 514 + it * 64) | ((unsigned)*(LDS unsigned short*)(L_WI + a + i * 514 + it * 64) << 16);
        UNROLL for (int it = 0; it < 8; ++it) st128(o[it], rS, g * 8192 + c * 16, WS_WT + b * 256 + (32 * it + 4 * w) * 8192);
        if (w == 7) {
          const unsigned v = (unsigned)*(LDS unsigned short*)(L_WR + lane * 514 + 512) | ((unsigned)*(LDS unsigned short*)(L_WI + lane * 514 + 512) << 16);
          __builtin_amdgcn_raw_buffer_store_b32(v, rS, lane * 4, WS_WT + b * 256 + 256 * 8192, SC1);
        }
        STAMP(14);
      }
    }
    WAITV(0); BARRIER();
    put_tag(ws, WS_ZF + b * 8, tag);
    STAMP(4);
    UNROLL for (int i = 0; i < 8; ++i) issueWd(i, i);
    fft_consts(fc, c, g, w & 3, yscale, true);
    WAITV(8); BARRIER();
    UNROLL for (int i = 0; i < 4; ++i) read_park(i);
    wait_tag(ws, WS_ZF + (lane & 31) * 8, tag);
    const u32x4 zr = __builtin_amdgcn_raw_buffer_load_b128(rS, tid * 16, WS_Z + col * 8192, SC1);
    const u32x4 w0 = __builtin_amdgcn_raw_buffer_load_b128(rS, tid * 16, WS_WT + col * 8192, SC1);
    if (special) wr128(L_W1 + stW(tid), __builtin_amdgcn_raw_buffer_load_b128(rS, tid * 16, WS_WT + 256 * 8192, SC1));
    wr128(stZ(tid), zr); wr128(L_W0 + stW(tid), w0);
    BARRIER();
    UNROLL for (int i = 0; i < 4; ++i) issueWd(10 + i, i);
    STAMP(6);
  }
  auto h_done = [&]() {
    WAITV(0);
    if (idx < 28 && lane == 0) {
      const unsigned n = __hip_atomic_fetch_add((LDS unsigned*)L_CNT, 1u, __ATOMIC_RELAXED, __HIP_MEMORY_SCOPE_WORKGROUP);
      if (n == 7) __hip_atomic_store((GLB u64*)(ws + WS_HF + (4 * (xcd >> 1) + (idx & 3)) * 128 + (7 * (xcd & 1) + (idx >> 2)) * 8), tag, __ATOMIC_RELAXED, __HIP_MEMORY_SCOPE_AGENT);
    }
  };

  const unsigned NEG = 0x80008000u, sgc = (c & 1) ? NEG : 0u;
  const u32x4 c64h = fc.c64 ^ sgc, s64h = fc.s64 ^ sgc, n64 = fc.s64 ^ NEG, n64h = s64h ^ NEG;
  if (w < 4 || special) {
    const int T = w & 3;
    u32x4 zf[2][2][2];
    const unsigned za = L_Z + c * 128 + ((g ^ (c >> 1)) * 16) + (w >> 2) * 4096;
    UNROLL for (int bt = 0; bt < 2; ++bt) UNROLL for (int hh = 0; hh < 2; ++hh) {
      zf[0][bt][hh] = rd128((za ^ (hh * 64)) + bt * 2048);
      zf[1][bt][hh] = rd128((za ^ (hh * 64)) + bt * 2048 + 4096);
    }
    unsigned wv[8];
    const unsigned wa = L_W0 + (w >> 2) * 16384 + g * 1024 + (((16 * T + c) ^ ((g & 1) * 16)) * 4);
    UNROLL for (int j = 0; j < 8; ++j) wv[j] = *(LDS unsigned*)(wa + (j >> 2) * 4096 + (j & 3) * 256);
    if (special) {
      UNROLL for (int bt = 0; bt < 2; ++bt) UNROLL for (int hh = 0; hh < 2; ++hh) zf[1][bt][hh] = u32x4{0, 0, 0, 0};
      BARRIER();
    }
    f32x4 re[2], im[2];
    UNROLL for (int bt = 0; bt < 2; ++bt) {
      re[bt] = mfma(zf[0][bt][0], fc.c64, f32x4{0, 0, 0, 0}); im[bt] = mfma(zf[0][bt][0], n64, f32x4{0, 0, 0, 0});
      re[bt] = mfma(zf[0][bt][1], c64h, re[bt]);             im[bt] = mfma(zf[0][bt][1], n64h, im[bt]);
      re[bt] = mfma(zf[1][bt][0], fc.s64, re[bt]);           im[bt] = mfma(zf[1][bt][0], fc.c64, im[bt]);
      re[bt] = mfma(zf[1][bt][1], s64h, re[bt]);             im[bt] = mfma(zf[1][bt][1], c64h, im[bt]);
    }
    UNROLL for (int j = 0; j < 8; ++j) {
      const float a = re[j >> 2][j & 3], b = im[j >> 2][j & 3];
      re[j >> 2][j & 3] = a * fc.t1c[j] + b * fc.t1s[j]; im[j >> 2][j & 3] = b * fc.t1c[j] - a * fc.t1s[j];
    }
    u32x4 pr = pk8(re[0], re[1]), pi = pk8(im[0], im[1]);
    h_done();
    const u32x4 c32h = fc.c32 ^ 0x80000000u, s32h = fc.s32 ^ 0x80000000u, n32 = fc.s32 ^ NEG, n32h = fc.s32 ^ 0x00008000u;
    re[0] = mfma(fc.c32, pr, f32x4{0, 0, 0, 0}); im[0] = mfma(fc.c32, pi, f32x4{0, 0, 0, 0});
    re[1] = mfma(c32h, pr, f32x4{0, 0, 0, 0});   im[1] = mfma(c32h, pi, f32x4{0, 0, 0, 0});
    re[0] = mfma(fc.s32, pi, re[0]); im[0] = mfma(n32, pr, im[0]);
    re[1] = mfma(s32h, pi, re[1]);   im[1] = mfma(n32h, pr, im[1]);
    UNROLL for (int j = 0; j < 8; ++j) {
      const float a = re[j >> 2][j & 3], b = im[j >> 2][j & 3], fr = lo16(wv[j]), fi = hi16(wv[j]);
      re[j >> 2][j & 3] = a * fr - b * fi; im[j >> 2][j & 3] = a * fi + b * fr;
    }
    pr = pk8(re[0], re[1]); pi = pk8(im[0], im[1]);
    re[0] = mfma(pr, fc.c32, f32x4{0, 0, 0, 0}); im[0] = mfma(pr, fc.s32, f32x4{0, 0, 0, 0});
    re[1] = mfma(pr, c32h, f32x4{0, 0, 0, 0});   im[1] = mfma(pr, s32h, f32x4{0, 0, 0, 0});
    re[0] = mfma(pi, n32, re[0]);  im[0] = mfma(pi, fc.c32, im[0]);
    re[1] = mfma(pi, n32h, re[1]); im[1] = mfma(pi, c32h, im[1]);
    const unsigned qa = ((w >> 2) ? L_Z : L_Q0) + (T >> 1) * 1024 + lane * 16 + (T & 1) * 8;
    UNROLL for (int bt = 0; bt < 2; ++bt) {
      float qr[4], qi[4];
      UNROLL for (int r = 0; r < 4; ++r) {
        const float a = re[bt][r], b = im[bt][r], tc_ = fc.t2c[4 * bt + r], ts_ = fc.t2s[4 * bt + r];
        qr[r] = a * tc_ - b * ts_; qi[r] = a * ts_ + b * tc_;
      }
      *(LDS u32x2*)(qa + bt * 2048) = u32x2{pk(qr[0], qr[1]), pk(qr[2], qr[3])};
      *(LDS u32x2*)(qa + bt * 2048 + 4096) = u32x2{pk(qi[0], qi[1]), pk(qi[2], qi[3])};
    }
  }
  else h_done();
  BARRIER();
  STAMP(7);
  u32x4 yout;
  {
    const unsigned qa = L_Q0 + (w >> 2) * 2048 + lane * 16;
    u32x4 r0 = rd128(qa), r1 = rd128(qa + 1024), i0 = rd128(qa + 4096), i1 = rd128(qa + 5120);
    f32x4 yr = mfma(fc.c64, r0, f32x4{0, 0, 0, 0}), yi;
    yr = mfma(c64h, r1, yr); yr = mfma(n64, i0, yr); yr = mfma(n64h, i1, yr);
    if (special) {
      const unsigned qb = qa - 16384;
      r0 = rd128(qb); r1 = rd128(qb + 1024); i0 = rd128(qb + 4096); i1 = rd128(qb + 5120);
      yi = mfma(fc.c64, r0, f32x4{0, 0, 0, 0}); yi = mfma(c64h, r1, yi); yi = mfma(n64, i0, yi); yi = mfma(n64h, i1, yi);
    } else {
      yi = mfma(fc.s64, r0, f32x4{0, 0, 0, 0}); yi = mfma(s64h, r1, yi); yi = mfma(fc.c64, i0, yi); yi = mfma(c64h, i1, yi);
    }
    yout = u32x4{pk(yr.x, yi.x), pk(yr.y, yi.y), pk(yr.z, yi.z), pk(yr.w, yi.w)};
  }
  if (w == 7) wait_tag(ws, WS_HF + (bo >> 1) * 128 + (c < 14 ? c : 13) * 8, tag);
  BARRIER();
  STAMP(8);
  {
    const unsigned sH = WS_H + (bo * 56 + 14 * kq) * 4096 + th * 2048;
    u32x4 hb[14][2];
    auto ldh = [&](int i) { ald128(hb[i][0], rS, lane * 16, sH + i * 4096); ald128(hb[i][1], rS, lane * 16, sH + i * 4096 + 1024); };
    ldh(0); ldh(1);
    st128(yout, rS, c * 65536 + g * 16, WS_Y + (w >> 2) * 1048576 + col * 256 + (w & 3) * 64);
    issueWd(8, 8); issueWd(9, 9);
    unsigned G[4][16];
    auto genG = [&](int ct) {
      const float st = (float)(64 * nb + 16 * ct + c) * (1.f / 512.f), a0 = __builtin_amdgcn_fractf(st * (float)(64 * kq + 16 * g));
      f32x2 p0 = {fcos(a0), fsin(-a0)}, p1 = {fcos(a0 + st), fsin(-a0 - st)};
      const f32x2 m = sp(2.f * fcos(st));
      G[ct][0] = (kq == 0 && g == 0) ? ((c & 1) ? 0xbf803f80u : 0x3f803f80u) : pk2(p0);
      G[ct][1] = pk2(p1);
      UNROLL for (int n = 2; n < 16; ++n) { const f32x2 p2 = fma2(m, p1, -p0); G[ct][n] = pk2(p2); p0 = p1; p1 = p2; }
      UNROLL for (int n = 0; n < 16; n += 4) asm volatile("" : "+v"(G[ct][n]), "+v"(G[ct][n + 1]), "+v"(G[ct][n + 2]), "+v"(G[ct][n + 3]));
    };
    genG(0); genG(1);
    SCHED();
    WAITV_(0, "+v"(hb[0][0]), "+v"(hb[0][1]), "+v"(hb[1][0]), "+v"(hb[1][1]));
    STAMP(9);
    ldh(2); ldh(3); ldh(4); ldh(5);
    f32x4 acc[4][2];
    UNROLL for (int ct = 0; ct < 4; ++ct) acc[ct][0] = acc[ct][1] = f32x4{0, 0, 0, 0};
    u32x2 yv[16], yf;
    u32x4 xres, A[2][4];
    UNROLL for (int i = 0; i < 14; ++i) {
      if (i >= 3 && i < 13) {
        const unsigned so = (i + 1 < 10 ? i + 1 : i - 9) * 16384;
        UNROLL for (int ct = 0; ct < 4; ++ct) A[(i + 1) & 1][ct] = rdtr(aD[ct] + so, aD[ct] + so + 2048);
      }
      if (i >= 2 && i <= 5) WAITV_(6, "+v"(hb[i][0]), "+v"(hb[i][1]));
      if (i >= 6 && i <= 8) WAITV_(7, "+v"(hb[i][0]), "+v"(hb[i][1]));
      if (i == 9) WAITV_(6, "+v"(hb[i][0]), "+v"(hb[i][1]), "+v"(yf));
      if (i == 10) WAITV_(23, "+v"(hb[i][0]), "+v"(hb[i][1]));
      if (i == 11) WAITV_(21, "+v"(hb[i][0]), "+v"(hb[i][1]));
      if (i == 12) WAITV_(19, "+v"(hb[i][0]), "+v"(hb[i][1]));
      if (i == 13) WAITV_(17, "+v"(hb[i][0]), "+v"(hb[i][1]));
      UNROLL for (int ct = 0; ct < 4; ++ct) {
        const u32x4 a = i < 4 ? park[i][ct] : A[i & 1][ct];
        acc[ct][0] = mfma(a, hb[i][0], acc[ct][0]); acc[ct][1] = mfma(a, hb[i][1], acc[ct][1]);
      }
      SCHED();
      if (i == 1) { BARRIER(); put_tag(ws, WS_YF + col * 8, tag); genG(2); }
      if (i == 2) genG(3);
      if (i == 8) BARRIER();
      if (i == 5) ald64(yf, rS, lane * 8, WS_YF + 512 * kq);
      if (i >= 2 && i + 4 < 14) ldh(i + 4);
      if (i == 9) {
        if (!__all(yf.x == (unsigned)tag && yf.y == (unsigned)(tag >> 32))) wait_tag(ws, WS_YF + (64 * kq + lane) * 8, tag);
        STAMP(10);
        UNROLL for (int n = 0; n < 16; ++n) ald64(yv[n], rS, g * 4096 + c * 8, WS_Y + bo * 65536 + kq * 16384 + th * 128 + n * 256);
        ald128p<0>(xres, rX, (tid >> 3) * 32768 + (tid & 7) * 16, bo * 1024 + nb * 128);
      }
      SCHED();
    }
    UNROLL for (int s = 0; s < 4; ++s) {
      if (s == 0) WAITV_(13, "+v"(yv[0]), "+v"(yv[1]), "+v"(yv[2]), "+v"(yv[3]));
      if (s == 1) WAITV_(9, "+v"(yv[4]), "+v"(yv[5]), "+v"(yv[6]), "+v"(yv[7]));
      if (s == 2) WAITV_(5, "+v"(yv[8]), "+v"(yv[9]), "+v"(yv[10]), "+v"(yv[11]));
      if (s == 3) WAITV_(0, "+v"(yv[12]), "+v"(yv[13]), "+v"(yv[14]), "+v"(yv[15]), "+v"(xres));
      const u32x4 y0 = {yv[4 * s].x, yv[4 * s + 1].x, yv[4 * s + 2].x, yv[4 * s + 3].x}, y1 = {yv[4 * s].y, yv[4 * s + 1].y, yv[4 * s + 2].y, yv[4 * s + 3].y};
      UNROLL for (int ct = 0; ct < 4; ++ct) {
        const u32x4 a = {G[ct][4 * s], G[ct][4 * s + 1], G[ct][4 * s + 2], G[ct][4 * s + 3]};
        acc[ct][0] = mfma(a, y0, acc[ct][0]); acc[ct][1] = mfma(a, y1, acc[ct][1]);
      }
    }
    STAMP(11);
    UNROLL for (int ct = 0; ct < 4; ++ct) UNROLL for (int t = 0; t < 2; ++t)
      wr128(65536 + kq * 16384 + (32 * th + 2 * c + t) * 256 + (((4 * ct + g) ^ (c & 7)) * 16), __builtin_bit_cast(u32x4, acc[ct][t]));
    BARRIER();
    const unsigned ra = 65536 + (tid >> 3) * 256 + (((2 * (tid & 7)) ^ ((tid >> 4) & 7)) * 16);
    f32x4 o0 = {lo16(xres.x), hi16(xres.x), lo16(xres.y), hi16(xres.y)}, o1 = {lo16(xres.z), hi16(xres.z), lo16(xres.w), hi16(xres.w)};
    UNROLL for (int k = 0; k < 4; ++k) {
      o0 += __builtin_bit_cast(f32x4, rd128(ra + k * 16384));
      o1 += __builtin_bit_cast(f32x4, rd128((ra ^ 16) + k * 16384));
    }
    *(GLB u32x4*)(out + (size_t)((tid >> 3) * 32 + bo) * 1024 + nb * 128 + (tid & 7) * 16) = pk8(o0, o1);
    if (bid == 0) put_tag(ws, 0, epoch + 1);
    STAMP(12); DIAG_END();
  }
}
}

namespace {
__global__ void k_probe2(const u64* p) { __shared__ u64 a[512], b[512]; __shared__ u64 r; const int t = threadIdx.x; u64 s0 = __builtin_amdgcn_s_memrealtime(); (void)a; (void)b; if(t==0) r=0;
  __syncthreads(); u64 d = r;
  if (d > 1000000) d = 1000000; d += 10000;
  if (t < 64) while (__builtin_amdgcn_s_memrealtime() - s0 < d) __builtin_amdgcn_s_sleep(8); } }
extern "C" void kernel_launch(void* const* d_in, const int* in_sizes, int n_in, void* d_out, int out_size, void* d_ws, size_t ws_size,
                              hipStream_t stream) {
  (void)in_sizes; (void)n_in; (void)out_size; (void)ws_size;
  (void)hipFuncSetAttribute((const void*)fused_kernel, hipFuncAttributeMaxDynamicSharedMemorySize, (int)LDS_BYTES);
  fused_kernel<<<256, 512, LDS_BYTES, stream>>>((const char*)d_in[0], (const char*)d_in[1], (const char*)d_in[2], (const char*)d_in[4],
                                                (const char*)d_in[5], (const char*)d_in[6], (char*)d_out, (char*)d_ws);
  k_probe2<<<1,512,0,stream>>>((const u64*)((char*)d_ws+WS_END));
}
```

```cpp
#define DIAG
#define TOUCH 8
#include <hip/hip_runtime.h>
#pragma clang diagnostic ignored "-Wint-to-pointer-cast"

#define TOUCH2 0
#define NPRE0 4
#define T2POS 3
#define KSTART 0
namespace {
typedef __bf16 bf16x8 __attribute__((ext_vector_type(8)));
typedef __bf16 bf16x2 __attribute__((ext_vector_type(2)));
typedef float f32x4 __attribute__((ext_vector_type(4)));
typedef float f32x2 __attribute__((ext_vector_type(2)));
typedef unsigned u32x4 __attribute__((ext_vector_type(4)));
typedef unsigned u32x2 __attribute__((ext_vector_type(2)));
typedef short s16x4 __attribute__((ext_vector_type(4)));
typedef unsigned long long u64;
typedef __amdgpu_buffer_rsrc_t rsrc_t;
#define LDS __attribute__((address_space(3)))
#define GLB __attribute__((address_space(1)))
#define DI __device__ __forceinline__
#define UNROLL _Pragma("unroll")

constexpr unsigned WS_ZF = 256, WS_SF = 3072, WS_HF = 512, WS_YF = 4096, WS_ST = 8192, WS_Z = 1u << 20, WS_WT = 3u << 20, WS_Y = 6u << 20,
                   WS_H = 8u << 20, WS_END = 16u << 20;
constexpr unsigned LDS_BYTES = 163840;
constexpr unsigned L_Z = 131072, L_W0 = L_Z + 8192, L_Q0 = L_Z + 16384, L_W1 = L_Z + 24576;
constexpr unsigned L_WR = 65536, L_WI = L_WR + 33792, L_BN = L_WI + 33792;
constexpr int SC1 = 16;

DI unsigned pk(float a, float b) { f32x2 v = {a, b}; return __builtin_bit_cast(unsigned, __builtin_convertvector(v, bf16x2)); }
DI float lo16(unsigned v) { return __uint_as_float(v << 16); }
DI float hi16(unsigned v) { return __uint_as_float(v & 0xffff0000u); }
DI f32x4 mfma(u32x4 a, u32x4 b, f32x4 c) {
  return __builtin_amdgcn_mfma_f32_16x16x32_bf16(__builtin_bit_cast(bf16x8, a), __builtin_bit_cast(bf16x8, b), c, 0, 0, 0);
}
DI u32x4 pk8(f32x4 a, f32x4 b) { u32x4 r = {pk(a.x, a.y), pk(a.z, a.w), pk(b.x, b.y), pk(b.z, b.w)}; return r; }
DI u32x4 rd128(unsigned a) { return *(LDS u32x4*)a; }
DI void wr128(unsigned a, u32x4 v) { *(LDS u32x4*)a = v; }
DI u32x4 rdtr(unsigned a0, unsigned a1) {
  u32x2 p = __builtin_bit_cast(u32x2, __builtin_amdgcn_ds_read_tr16_b64_v4i16((LDS s16x4*)a0));
  u32x2 q = __builtin_bit_cast(u32x2, __builtin_amdgcn_ds_read_tr16_b64_v4i16((LDS s16x4*)a1));
  u32x4 r = {p.x, p.y, q.x, q.y};
  return r;
}
DI void dma(rsrc_t rs, unsigned lds, unsigned voff, unsigned soff) {
  asm volatile("s_mov_b32 m0, %0\n\ts_nop 0\n\tbuffer_load_dwordx4 %1, %2, %3 offen lds" ::"s"(lds), "v"(voff), "s"(rs), "s"(soff) : "memory");
}
DI void touch(rsrc_t rs, unsigned lds, unsigned voff) {
  asm volatile("s_mov_b32 m0, %0\n\ts_nop 0\n\tbuffer_load_dword %1, %2, 0 offen lds" ::"s"(lds), "v"(voff), "s"(rs) : "memory");
}
DI void st128(u32x4 v, rsrc_t rs, unsigned voff, unsigned soff) {
  asm volatile("buffer_store_dwordx4 %0, %1, %2, %3 offen sc1\n\ts_nop 1" ::"v"(v), "v"(voff), "s"(rs), "s"(soff) : "memory");
}
DI void ald64(u32x2& v, rsrc_t rs, unsigned voff, unsigned soff) {
  asm volatile("buffer_load_dwordx2 %0, %1, %2, %3 offen sc1" : "=v"(v) : "v"(voff), "s"(rs), "s"(soff) : "memory");
}
DI void ald128(u32x4& v, rsrc_t rs, unsigned voff, unsigned soff) {
  asm volatile("buffer_load_dwordx4 %0, %1, %2, %3 offen sc1" : "=v"(v) : "v"(voff), "s"(rs), "s"(soff) : "memory");
}
template <int OFF> DI void ald128p(u32x4& v, rsrc_t rs, unsigned voff, unsigned soff) {
  asm volatile("buffer_load_dwordx4 %0, %1, %2, %3 offen offset:%4" : "=v"(v) : "v"(voff), "s"(rs), "s"(soff), "i"(OFF) : "memory");
}
DI u32x2 rdtr1(unsigned a) { return __builtin_bit_cast(u32x2, __builtin_amdgcn_ds_read_tr16_b64_v4i16((LDS s16x4*)a)); }
DI f32x2 lo2(f32x4 v) { return __builtin_shufflevector(v, v, 0, 1); }
DI f32x2 hi2(f32x4 v) { return __builtin_shufflevector(v, v, 2, 3); }
DI f32x2 fma2(f32x2 a, f32x2 b, f32x2 c) { return __builtin_elementwise_fma(a, b, c); }
DI f32x2 sp(float a) { return f32x2{a, a}; }
DI unsigned pk2(f32x2 v) { return __builtin_bit_cast(unsigned, __builtin_convertvector(v, bf16x2)); }
DI float fcos(float rev) { return __builtin_amdgcn_cosf(rev); }
DI float fsin(float rev) { return __builtin_amdgcn_sinf(rev); }
#define WAITV(n) asm volatile("s_waitcnt vmcnt(" #n ")" ::: "memory")
#define WAITV_(n, ...) asm volatile("s_waitcnt vmcnt(" #n ")" : __VA_ARGS__ :: "memory")
#define BARRIER() do { asm volatile("s_waitcnt lgkmcnt(0)" ::: "memory"); __builtin_amdgcn_s_barrier(); asm volatile("" ::: "memory"); } while (0)
#define SCHED() __builtin_amdgcn_sched_barrier(0)
#define NSTAMP 30
#define ISDFT(b) (((b) >> 3) >= 28)
#define STAMP(i) do { SCHED(); stp[i] = (unsigned)__builtin_amdgcn_s_memtime(); SCHED(); } while (0)
#define DIAG_BEGIN() unsigned stp[NSTAMP]; UNROLL for (int i_ = 0; i_ < NSTAMP; ++i_) stp[i_] = 0; const u64 rt0_ = __builtin_amdgcn_s_memrealtime(); STAMP(0)
#define DIAG_END() do { if ((tid & 255) == 0) { GLB u64* o_ = (GLB u64*)(ws + WS_END) + (blockIdx.x * 2 + (tid >> 8)) * 32; \
    UNROLL for (int i_ = 0; i_ < NSTAMP; ++i_) o_[i_] = stp[i_]; o_[30] = rt0_; o_[31] = __builtin_amdgcn_s_memrealtime(); } } while (0)

DI void wait_tag(char* ws, unsigned off, u64 tag) {
#pragma unroll 1
  for (int it = 0; it < (1 << 18); ++it) {
    u64 v = __hip_atomic_load((GLB u64*)(ws + off), __ATOMIC_RELAXED, __HIP_MEMORY_SCOPE_AGENT);
    if (__all(v == tag)) break;
    __builtin_amdgcn_s_sleep(2);
  }
  __builtin_amdgcn_fence(__ATOMIC_ACQUIRE, "wavefront");
  asm volatile("" ::: "memory");
}
DI void put_tag(char* ws, unsigned off, u64 tag, int who = 0) {
  if ((int)threadIdx.x == who) __hip_atomic_store((GLB u64*)(ws + off), tag, __ATOMIC_RELAXED, __HIP_MEMORY_SCOPE_AGENT);
}

struct FC { u32x4 c64, s64, c32, s32; float t1c[8], t1s[8], t2c[8], t2s[8]; };
DI void fft_consts(FC& k, int c, int g, int T, float scale, bool full) {
  const float j1 = (float)(16 * T + c) * (1.f / 64.f), g4 = (float)(4 * g);
  UNROLL for (int i = 0; i < 4; ++i) {
    const float o = (float)(i < 2 ? 2 * i : 12 + 2 * i);
    const f32x2 an = (sp(g4) + f32x2{o, o + 1.f}) * sp(j1);
    k.c64[i] = pk(fcos(an.x), fcos(an.y)); k.s64[i] = pk(fsin(an.x), fsin(an.y));
  }
  asm volatile("" : "+v"(k.c64), "+v"(k.s64));
  if (full) {
    const float cf = (float)c * (1.f / 32.f), j2 = j1 * (1.f / 32.f), r0 = (float)(16 * T) + g4;
    UNROLL for (int i = 0; i < 4; ++i) {
      const float o = (float)(i < 2 ? 2 * i : 12 + 2 * i);
      const f32x2 sl = sp(g4) + f32x2{o, o + 1.f}, a1 = sl * sp(cf), a2 = sl * sp(j2);
      const f32x2 a3 = (sp(r0) + f32x2{(float)(2 * (i & 1)), (float)(2 * (i & 1) + 1)}) * sp((cf + (float)(i >> 1) * 0.5f) * (1.f / 64.f));
      k.c32[i] = pk(fcos(a1.x), fcos(a1.y)); k.s32[i] = pk(fsin(a1.x), fsin(a1.y));
      k.t1c[2 * i] = fcos(a2.x); k.t1c[2 * i + 1] = fcos(a2.y); k.t1s[2 * i] = fsin(a2.x); k.t1s[2 * i + 1] = fsin(a2.y);
      const f32x2 cc = f32x2{fcos(a3.x), fcos(a3.y)} * sp(scale), ss = f32x2{fsin(a3.x), fsin(a3.y)} * sp(scale);
      k.t2c[2 * i] = cc.x; k.t2c[2 * i + 1] = cc.y; k.t2s[2 * i] = ss.x; k.t2s[2 * i + 1] = ss.y;
    }
    asm volatile("" : "+v"(k.c32), "+v"(k.s32));
    UNROLL for (int j = 0; j < 8; ++j) asm volatile("" : "+v"(k.t1c[j]), "+v"(k.t1s[j]), "+v"(k.t2c[j]), "+v"(k.t2s[j]));
  }
}
DI void dft_table(u32x4 (&t)[4], float kf, float ph, int g) {
  const float st = kf * (1.f / 512.f), a0 = st * (float)(8 * g) + ph;
  const f32x2 m2 = sp(2.f * fcos(st * 2.f)), m32 = sp(2.f * fcos(st * 32.f));
  f32x2 v[4][4];
  UNROLL for (int s = 0; s < 2; ++s) {
    const f32x2 p = fma2(sp(st), f32x2{32.f * s, 32.f * s + 1.f}, sp(a0)), q = fma2(sp(st), f32x2{32.f * s + 2.f, 32.f * s + 3.f}, sp(a0));
    v[s][0] = f32x2{fcos(p.x), fcos(p.y)}; v[s][1] = f32x2{fcos(q.x), fcos(q.y)};
    v[s][2] = fma2(m2, v[s][1], -v[s][0]); v[s][3] = fma2(m2, v[s][2], -v[s][1]);
  }
  UNROLL for (int i = 0; i < 4; ++i) { v[2][i] = fma2(m32, v[1][i], -v[0][i]); v[3][i] = fma2(m32, v[2][i], -v[1][i]); }
  UNROLL for (int s = 0; s < 4; ++s) { UNROLL for (int i = 0; i < 4; ++i) t[s][i] = pk2(v[s][i]); asm volatile("" : "+v"(t[s])); }
}
template <int CTRL> DI float dpp_add(float v) {
  return v + __builtin_bit_cast(float, __builtin_amdgcn_update_dpp(0, __builtin_bit_cast(int, v), CTRL, 0xf, 0xf, true));
}
DI float row16_sum(float v) { v = dpp_add<0x128>(v); v = dpp_add<0x124>(v); v = dpp_add<0x122>(v); return dpp_add<0x121>(v); }
DI float rbf(float v) { return lo16(pk(v, 0.f)); }

__global__ void __launch_bounds__(512, 2)
fused_kernel(const char* __restrict__ x, const char* __restrict__ wr, const char* __restrict__ wi, const char* __restrict__ wg,
             const char* __restrict__ wu, const char* __restrict__ wd, char* __restrict__ out, char* ws) {
  u64 tb_; asm volatile("s_memtime %0" : "=s"(tb_));
  const int tid = threadIdx.x, lane = tid & 63, c = lane & 15, g = lane >> 4;
  const int w = __builtin_amdgcn_readfirstlane(tid >> 6);
  const int bid = blockIdx.x, xcd = bid & 7, idx = bid >> 3;
  const rsrc_t rX = __builtin_amdgcn_make_buffer_rsrc((void*)x, 0, 2048 * 512 * 2, 0x00020000);
  const rsrc_t rD = __builtin_amdgcn_make_buffer_rsrc((void*)wd, 0, 1792 * 512 * 2, 0x00020000);
  const rsrc_t rS = __builtin_amdgcn_make_buffer_rsrc((void*)ws, 0, WS_END, 0x00020000);
  DIAG_BEGIN();
  stp[29] = stp[0]; asm volatile("s_waitcnt lgkmcnt(0)" : "+s"(tb_)); stp[0] = (unsigned)tb_;
  const int col = (bid + 1) & 255;
  const bool special = (col == 0);
  const int bo = 4 * xcd + (idx >> 3), nb = idx & 7;
  const float yscale = special ? (1.f / 1048576.f) : (2.f / 1048576.f);
  const unsigned vWd = (unsigned)(((14 * (w >> 1)) * 32 + ((16 * (w & 1) + (lane >> 3)) ^ (16 * (bo & 1)))) * 1024 + nb * 128 +
                                  ((((lane & 7) >> 1) ^ g) * 32) + (lane & 1) * 16);
  auto issueWd = [&](int step, int slot) {
    dma(rD, slot * 16384 + w * 2048, vWd, step * 32768);
    dma(rD, slot * 16384 + w * 2048 + 1024, vWd, step * 32768 + 8192);
  };
  u64 epoch, tag;
  u32x2 ev;
  auto make_tag = [&]() {
    epoch = ((u64)__builtin_amdgcn_readfirstlane(ev.y) << 32) | (unsigned)__builtin_amdgcn_readfirstlane(ev.x);
    tag = epoch * 0x9E3779B97F4A7C15ull + 0x632BE59BD9B4E019ull;
  };
  FC fc;
  const int th = w >> 2, kq = w & 3, q4 = (lane >> 2) & 3, p4 = lane & 3;
  unsigned aD[4];
  UNROLL for (int ct = 0; ct < 4; ++ct) aD[ct] = kq * 4096 + (4 * g + q4) * 128 + ((ct ^ ((2 * g + (q4 >> 1)) & 3)) * 32) + 8 * p4;
  u32x4 park[4][4];
  auto read_park = [&](int i) { UNROLL for (int ct = 0; ct < 4; ++ct) park[i][ct] = rdtr(aD[ct] + i * 16384, aD[ct] + i * 16384 + 2048); };
  auto stZ = [&](int t) { return L_Z + (t >> 3) * 128 + (((t & 7) ^ ((t >> 4) & 7)) * 16); };
  auto stW = [&](int t) { return (t >> 4) * 256 + (((t & 15) ^ (((t >> 6) & 1) * 4)) * 16); };
  constexpr unsigned L_CNT = L_W1 + 4096;
  const rsrc_t rG = __builtin_amdgcn_make_buffer_rsrc((void*)wg, 0, 512 * 1792 * 2, 0x00020000);
  const rsrc_t rU = __builtin_amdgcn_make_buffer_rsrc((void*)wu, 0, 512 * 1792 * 2, 0x00020000);
  const rsrc_t rR = __builtin_amdgcn_make_buffer_rsrc((void*)wr, 0, 2048 * 257 * 2, 0x00020000);
  const rsrc_t rI = __builtin_amdgcn_make_buffer_rsrc((void*)wi, 0, 2048 * 257 * 2, 0x00020000);
  auto touches = [&](int mask, unsigned lds) {
    const int l56 = lane < 56 ? lane : 55, wrow = (l56 * 37) >> 9;
    if (w == 0 && (mask & 1)) touch(rX, lds, ((32 * (xcd & 1) + idx) * 32 + 8 * (xcd >> 1)) * 1024 + lane * 128);
    if (w == 1 && (mask & 2)) touch(rG, lds + 256, (wrow * 128 + (xcd >> 1) * 32 + idx) * 3584 + (xcd & 1) * 1792 + (l56 - 14 * wrow) * 128);
    if (w == 2 && (mask & 4)) touch(rU, lds + 512, (wrow * 128 + (xcd >> 1) * 32 + idx) * 3584 + (xcd & 1) * 1792 + (l56 - 14 * wrow) * 128);
    if (w == 3 && (mask & 8)) touch(rD, lds + 768, (bid * 56 + l56) * 128);
    if (w == 4 && (mask & 16)) touch(rR, lds + 1024, xcd * 131584 + (idx * 32 + (lane < 36 ? lane : 35)) * 128);
    if (w == 5 && (mask & 16)) touch(rI, lds + 1280, xcd * 131584 + (idx * 32 + (lane < 36 ? lane : 35)) * 128);
  };

  if (idx < 28) {
    const int rb = 4 * (xcd >> 1) + (idx & 3), cb = 7 * (xcd & 1) + (idx >> 2);
    const int wt = w >> 2, wf = w & 3;
    const int xr = 8 * w + (lane >> 3);
    const int xa = 32 * (xr >> 5) + 2 * (xr & 15) + ((xr >> 4) & 1);
    const unsigned vX = (unsigned)((32 * xa + 2 * rb) * 1024 + (((lane & 7) ^ ((xr >> 1) & 7)) * 16));
    const unsigned vW = (unsigned)((8 * w + g) * 3584 + cb * 256 + (((c >> 1) ^ (g | ((w & 1) << 2))) * 32) + (c & 1) * 16);
    const int kstart = KSTART;
    auto slot = [](int ch) { return 16384u + ((ch + 1) % 3) * 49152u; };
    auto issue = [&](int n) {
      const int ch = n / 6, pc = n % 6;
      if (ch < 8) {
        const int kc = (ch + kstart) & 7, hf = pc / 3;
        if (pc % 3 == 0) dma(rX, slot(ch) + hf * 8192 + w * 1024, vX, kc * 128 + hf * 1024);
        else dma(pc % 3 == 1 ? rG : rU, slot(ch) + (pc % 3) * 16384 + w * 2048 + hf * 1024, vW, kc * 229376 + hf * 14336);
      } else {
        const int st = 1 + (n - 48) / 2, hf = n & 1;
        dma(rD, st * 16384 + w * 2048 + hf * 1024, vWd, st * 32768 + hf * 8192);
      }
    };
    touches(TOUCH, 0);
    ald64(ev, rS, 0, 0);
    UNROLL for (int n = 0; n < NPRE0; ++n) issue(n);
    fft_consts(fc, c, g, w & 3, yscale, w < 4);
    STAMP(13);
    UNROLL for (int n = NPRE0; n < 10; ++n) issue(n);
    const int sw = q4 | ((g & 1) << 2);
    const unsigned aB0 = (64 * wt + c) * 128 + ((g ^ (c >> 1)) * 16), aB1 = aB0 ^ 64;
    const unsigned aA0 = 16384 + (8 * g + q4) * 256 + (((2 * wf + wt) ^ sw) * 32) + 8 * p4, aA1 = aA0 ^ 32;
    f32x4 acc[4][4], cs[2];
    UNROLL for (int m = 0; m < 4; ++m) UNROLL for (int t = 0; t < 4; ++t) acc[m][t] = f32x4{0, 0, 0, 0};
    cs[0] = cs[1] = f32x4{0, 0, 0, 0};
    u32x4 ones = {0x3f803f80u, 0x3f803f80u, 0x3f803f80u, 0x3f803f80u};
    asm volatile("" : "+v"(ones));
    u32x4 A[2][4], B[2][4];
    auto rdgrp = [&](int gi, int ks, unsigned base) {
      const unsigned b = base + (ks ? aB1 : aB0), a0 = base + aA0 + ks * 8192, a1 = base + aA1 + ks * 8192;
      if (gi == 0) { A[ks][0] = rdtr(a0, a0 + 1024); B[ks][0] = rd128(b); }
      if (gi == 1) { B[ks][1] = rd128(b + 2048); B[ks][2] = rd128(b + 4096); B[ks][3] = rd128(b + 6144); }
      if (gi == 2) { A[ks][2] = rdtr(a0 + 16384, a0 + 17408); A[ks][1] = rdtr(a1, a1 + 1024); }
      if (gi == 3) { A[ks][3] = rdtr(a1 + 16384, a1 + 17408); }
    };
    auto mm1 = [&](int i, int ks) {
      if (i == 4) cs[0] = mfma(A[ks][0], ones, cs[0]);
      else if (i == 9) cs[1] = mfma(A[ks][2], ones, cs[1]);
      else { const int m = i < 4 ? 0 : i < 9 ? 2 : i < 14 ? 1 : 3, t = i < 4 ? i : i < 9 ? i - 5 : (i - 10) & 3; acc[m][t] = mfma(A[ks][m], B[ks][t], acc[m][t]); }
    };
    STAMP(1);
    WAITV_(4, "+v"(ev)); BARRIER();
    STAMP(2);
    make_tag();
    u32x2 sf; u32x4 st;
    UNROLL for (int gi = 0; gi < 4; ++gi) rdgrp(gi, 0, slot(0));
    SCHED();
    UNROLL for (int hp = 0; hp < 16; ++hp) {
      const int ks = hp & 1, nx = hp + 1;
      if (hp == T2POS) touches(TOUCH2, 0);
      if (hp == 9) issueWd(0, 0);
      if (hp == 11) ald64(sf, rS, 0, WS_SF + (2 * rb + wt) * 8);
      if (hp == 13) ald128(st, rS, c * 16, WS_ST + (2 * rb + wt) * 256);
      UNROLL for (int gi = 0; gi < 6; ++gi) {
        if (gi < 4) { if (nx < 16) rdgrp(gi, nx & 1, slot(nx >> 1)); else read_park(gi); }
        UNROLL for (int i = 0; i < 3; ++i) mm1(3 * gi + i, ks);
        UNROLL for (int i = 0; i < 3; ++i) { __builtin_amdgcn_sched_group_barrier(0x008, 1, 0); if (nx < 16) __builtin_amdgcn_sched_group_barrier(0x100, 1, 0); else __builtin_amdgcn_sched_group_barrier(0x100, 3, 0); }
        SCHED();
        if (hp == 0) { issue(10 + gi); if (gi >= 4) issue(12 + gi); }
        else if (hp < 15) { if ((gi & 1) == 0) issue(15 + 3 * hp + gi / 2); }
        else if (gi == 0 || gi == 2) issue(60 + gi / 2);
        if (hp == 15 && gi == 3 && c == 0) { wr128(L_W1 + w * 128 + g * 16, __builtin_bit_cast(u32x4, cs[0])); wr128(L_W1 + w * 128 + 64 + g * 16, __builtin_bit_cast(u32x4, cs[1])); }
        if (hp == 15 && gi == 3 && tid == 0) *(LDS unsigned*)L_CNT = 0;
        SCHED();
      }
      if (ks == 0) {
        if (hp == 12) {
          WAITV_(6, "+v"(sf));
          if (__builtin_amdgcn_readfirstlane(sf.x) != (unsigned)tag || __builtin_amdgcn_readfirstlane(sf.y) != (unsigned)(tag >> 32))
            wait_tag(ws, WS_SF + (2 * rb + wt) * 8, tag);
        } else if (hp == 14) WAITV_(6, "+v"(st));
        else { STAMP(22 + (hp >> 1)); WAITV(6); }
        BARRIER();
        STAMP(14 + (hp >> 1));
        SCHED();
      }
    }
    BARRIER();
    STAMP(3);
    const f32x4 cg1 = __builtin_bit_cast(f32x4, rd128(L_W1 + (w ^ 4) * 128 + g * 16)), cu1 = __builtin_bit_cast(f32x4, rd128(L_W1 + (w ^ 4) * 128 + 64 + g * 16));
    if (w >= 4) {
      wait_tag(ws, WS_ZF + (lane & 31) * 8, tag);
      STAMP(4);
      u32x4 z0, z1, w0, w1;
      ald128(z0, rS, tid * 16, WS_Z + col * 8192 - 4096); ald128(z1, rS, tid * 16, WS_Z + col * 8192);
      ald128(w0, rS, tid * 16, WS_WT + col * 8192 - 4096); ald128(w1, rS, tid * 16, WS_WT + col * 8192);
      UNROLL for (int i = 0; i < 4; ++i) issueWd(10 + i, i);
      WAITV_(8, "+v"(z0), "+v"(z1), "+v"(w0), "+v"(w1));
      wr128(stZ(tid) - 4096, z0); wr128(stZ(tid), z1); wr128(L_W0 + stW(tid) - 4096, w0); wr128(L_W0 + stW(tid), w1);
      BARRIER();
      __builtin_amdgcn_s_setprio(1);
    }
    STAMP(5);
    UNROLL for (int t = 0; t < 4; ++t) {
      const float sd = hi16(st[t]), rs = __builtin_amdgcn_rcpf(sd);
      const f32x2 nm = sp(-lo16(st[t])), k1 = sp(-2.302208198f * rs), k3 = sp(-0.102943240f * rs * rs * rs), v2 = sp(sd * sd);
      u32x4 h;
      UNROLL for (int i = 0; i < 4; ++i) {
        const int m = i >> 1;
        const f32x4 ag = acc[m][t], au = acc[2 + m][t], sg = m ? cg1 : cs[0], su = m ? cu1 : cs[1];
        const f32x2 tg = fma2(nm, (i & 1) ? hi2(sg) : lo2(sg), (i & 1) ? hi2(ag) : lo2(ag)), tu = fma2(nm, (i & 1) ? hi2(su) : lo2(su), (i & 1) ? hi2(au) : lo2(au));
        const f32x2 ar = tg * fma2(k3, tg * tg, k1);
        const f32x2 dn = fma2(f32x2{__builtin_amdgcn_exp2f(ar.x), __builtin_amdgcn_exp2f(ar.y)}, v2, v2);
        h[i] = pk2(tg * tu * f32x2{__builtin_amdgcn_rcpf(dn.x), __builtin_amdgcn_rcpf(dn.y)});
      }
      st128(h, rS, lane * 16, WS_H + ((2 * rb + wt) * 56 + 4 * cb + wf) * 4096 + t * 1024);
    }
    STAMP(6);
    if (w < 4) {
      BARRIER();
      UNROLL for (int i = 0; i < 4; ++i) issueWd(10 + i, i);
    } else {
      __builtin_amdgcn_s_setprio(0);
    }
  } else {
    const int b = 4 * xcd + (idx - 28);
    touches(TOUCH | TOUCH2, L_BN + 20480);
    u32x4 xv[2][4];
    u32x4 tc[4], ts[4], t0[4], t2[4];
    const float ko = (float)(32 * w + 2 * c + 1), ke = (float)(32 * w + 4 * (c & 7)), ph = (c & 8) ? 0.25f : 0.f;
    const bool nyq = (w == 0 && c == 8);
    const unsigned vx = g * 32768 + c * 16, sx = (256 * w + b) * 1024;
    ald128p<0>(xv[0][0], rX, vx, sx); ald128p<256>(xv[0][1], rX, vx, sx); ald128p<512>(xv[0][2], rX, vx, sx); ald128p<768>(xv[0][3], rX, vx, sx);
    SCHED();
    dft_table(tc, ko, 0.f, g); dft_table(ts, ko, 0.25f, g);
    SCHED();
    ald128p<0>(xv[1][0], rX, vx, sx + 131072); ald128p<256>(xv[1][1], rX, vx, sx + 131072); ald128p<512>(xv[1][2], rX, vx, sx + 131072); ald128p<768>(xv[1][3], rX, vx, sx + 131072);
    ald64(ev, rS, 0, 0);
    SCHED();
    dft_table(t0, nyq ? 256.f : ke, nyq ? 0.f : ph, g); dft_table(t2, ke + 2.f, ph, g);
    SCHED();
    STAMP(1);
    UNROLL for (int tp = 0; tp < 2; ++tp) {
      UNROLL for (int i = 0; i < 4; ++i) dma(tp ? rI : rR, (tp ? L_WI : L_WR) + (4 * w + i) * 1024, lane * 16, b * 32896 + (4 * w + i) * 1024);
      if (tp == 0) WAITV_(9, "+v"(xv[0][0]), "+v"(xv[0][1]), "+v"(xv[0][2]), "+v"(xv[0][3]));
      else WAITV_(8, "+v"(xv[1][0]), "+v"(xv[1][1]), "+v"(xv[1][2]), "+v"(xv[1][3]), "+v"(ev));
      f32x2 o[4][4], sum = {0.f, 0.f};
      UNROLL for (int i = 0; i < 4; ++i) {
        f32x2 v[4];
        UNROLL for (int m = 0; m < 4; ++m) v[m] = f32x2{lo16(xv[tp][m][i]), hi16(xv[tp][m][i])};
        const f32x2 ua = v[0] + v[2], ub = v[1] + v[3];
        o[0][i] = v[0] - v[2]; o[1][i] = v[1] - v[3]; o[2][i] = ua + ub; o[3][i] = ua - ub;
        sum += o[2][i];
      }
      const float mean = row16_sum(sum.x + sum.y) * (1.f / 512.f);
      f32x2 q1 = {0.f, 0.f}, q2 = {0.f, 0.f};
      UNROLL for (int i = 0; i < 4; ++i) {
        o[2][i] -= sp(4.f * mean);
        q1 = fma2(o[0][i], o[0][i], q1); q1 = fma2(o[1][i], o[1][i], q1); q2 = fma2(o[2][i], o[2][i], q2); q2 = fma2(o[3][i], o[3][i], q2);
      }
      q1 = fma2(q2, sp(0.5f), q1);
      const float var = row16_sum(q1.x + q1.y) * (1.f / 1024.f);
      const float mb = rbf(mean), sd = rbf(__builtin_amdgcn_sqrtf(rbf(rbf(var) + 1e-5f)));
      const float rs = __builtin_amdgcn_rcpf(sd), dl = 4.f * (mean - mb) * rs;
      const int a = 8 * w + 4 * tp + g;
      if (c == 0) *(LDS unsigned*)(L_BN + 16384 + (((a & 31) >> 1) * 4 + 2 * (a >> 5) + (a & 1)) * 4) = pk(mb, sd);
      UNROLL for (int m = 0; m < 4; ++m) {
        u32x4 pv;
        UNROLL for (int i = 0; i < 4; ++i) pv[i] = pk2(m == 2 ? fma2(o[m][i], sp(rs), sp(dl)) : o[m][i] * sp(rs));
        wr128(m * 16384 + a * 256 + ((c ^ (a & 15)) * 16), pv);
      }
    }
    if (w == 0) dma(rR, L_WR + 32768, (lane & 7) * 16, b * 32896 + 32768);
    if (w == 1) dma(rI, L_WI + 32768, (lane & 7) * 16, b * 32896 + 32768);
    make_tag();
    STAMP(13);
    WAITV(0); BARRIER();
    STAMP(2);
    if (w == 3) __builtin_amdgcn_raw_buffer_store_b32(*(LDS unsigned*)(L_BN + 16384 + lane * 4), rS, lane * 4, WS_ST + b * 256, SC1);
    const unsigned bn = L_BN + w * 2048;
    auto putZ = [&](f32x4 (&d)[4], unsigned r0, unsigned r1) {
      wr128(bn + c * 128 + ((g ^ (c & 7)) * 16), pk8(d[0], d[1]));
      wr128(bn + c * 128 + (((4 + g) ^ (c & 7)) * 16), pk8(d[2], d[3]));
      const unsigned ra = bn + (lane >> 3) * 128 + (((lane & 7) ^ (lane >> 3)) * 16);
      const u32x4 p0 = rd128(ra), p1 = rd128(ra + 1024);
      st128(p0, rS, r0 * 4096 + (lane & 7) * 16, WS_Z + b * 128);
      st128(p1, rS, r1 * 4096 + (lane & 7) * 16, WS_Z + b * 128);
    };
    const int l8 = lane >> 3;
#pragma unroll 1
    for (int pass = 0; pass < 2; ++pass) {
      if (pass == (w >> 2)) {
        f32x4 d0[4], d2[4], dc[4], ds[4];
        const unsigned sg = (c & 1) ? 0x80008000u : 0u;
        UNROLL for (int t = 0; t < 4; ++t) d0[t] = d2[t] = dc[t] = ds[t] = f32x4{0, 0, 0, 0};
        UNROLL for (int s = 0; s < 4; ++s) {
          const u32x4 hc = ts[s] ^ sg, hs = tc[s] ^ (sg ^ 0x80008000u);
          UNROLL for (int t = 0; t < 4; ++t) {
            const unsigned a = c * 256 + (((4 * s + g) ^ c) * 16) + t * 4096;
            const u32x4 v0 = rd128(a), v1 = rd128(a + 16384);
            dc[t] = mfma(v0, tc[s], dc[t]); ds[t] = mfma(v0, ts[s], ds[t]);
            dc[t] = mfma(v1, hc, dc[t]); ds[t] = mfma(v1, hs, ds[t]);
          }
        }
        if (w == 3) { WAITV(0); put_tag(ws, WS_SF + b * 8, tag, 192); }
        putZ(dc, 2 * (32 * w + 2 * l8 + 1), 2 * (32 * w + 2 * l8 + 17));
        putZ(ds, 2 * (32 * w + 2 * l8 + 1) + 1, 2 * (32 * w + 2 * l8 + 17) + 1);
        UNROLL for (int s = 0; s < 4; ++s) UNROLL for (int t = 0; t < 4; ++t) {
          const unsigned a = c * 256 + (((4 * s + g) ^ c) * 16) + t * 4096;
          d0[t] = mfma(rd128(a + 32768), t0[s], d0[t]);
          d2[t] = mfma(rd128(a + 49152), t2[s], d2[t]);
        }
        putZ(d0, 2 * (32 * w + 4 * l8), 2 * (32 * w + 4 * l8) + 1);
        putZ(d2, 2 * (32 * w + 4 * l8 + 2), 2 * (32 * w + 4 * l8 + 2) + 1);
        STAMP(3);
      } else {
        const unsigned a = c * 2056 + 8 * w + 2 * g;
        u32x4 o[8];
        UNROLL for (int it = 0; it < 8; ++it) UNROLL for (int i = 0; i < 4; ++i)
          o[it][i] = (unsigned)*(LDS unsigned short*)(L_WR + a + i * 514 + it * 64) | ((unsigned)*(LDS unsigned short*)(L_WI + a + i * 514 + it * 64) << 16);
        UNROLL for (int it = 0; it < 8; ++it) st128(o[it], rS, g * 8192 + c * 16, WS_WT + b * 256 + (32 * it + 4 * w) * 8192);
        if (w == 7) {
          const unsigned v = (unsigned)*(LDS unsigned short*)(L_WR + lane * 514 + 512) | ((unsigned)*(LDS unsigned short*)(L_WI + lane * 514 + 512) << 16);
          __builtin_amdgcn_raw_buffer_store_b32(v, rS, lane * 4, WS_WT + b * 256 + 256 * 8192, SC1);
        }
        STAMP(14);
      }
    }
    WAITV(0); BARRIER();
    put_tag(ws, WS_ZF + b * 8, tag);
    STAMP(4);
    UNROLL for (int i = 0; i < 8; ++i) issueWd(i, i);
    fft_consts(fc, c, g, w & 3, yscale, true);
    WAITV(8); BARRIER();
    UNROLL for (int i = 0; i < 4; ++i) read_park(i);
    wait_tag(ws, WS_ZF + (lane & 31) * 8, tag);
    const u32x4 zr = __builtin_amdgcn_raw_buffer_load_b128(rS, tid * 16, WS_Z + col * 8192, SC1);
    const u32x4 w0 = __builtin_amdgcn_raw_buffer_load_b128(rS, tid * 16, WS_WT + col * 8192, SC1);
    if (special) wr128(L_W1 + stW(tid), __builtin_amdgcn_raw_buffer_load_b128(rS, tid * 16, WS_WT + 256 * 8192, SC1));
    wr128(stZ(tid), zr); wr128(L_W0 + stW(tid), w0);
    BARRIER();
    UNROLL for (int i = 0; i < 4; ++i) issueWd(10 + i, i);
    STAMP(6);
  }
  auto h_done = [&]() {
    WAITV(0);
    if (idx < 28 && lane == 0) {
      const unsigned n = __hip_atomic_fetch_add((LDS unsigned*)L_CNT, 1u, __ATOMIC_RELAXED, __HIP_MEMORY_SCOPE_WORKGROUP);
      if (n == 7) __hip_atomic_store((GLB u64*)(ws + WS_HF + (4 * (xcd >> 1) + (idx & 3)) * 128 + (7 * (xcd & 1) + (idx >> 2)) * 8), tag, __ATOMIC_RELAXED, __HIP_MEMORY_SCOPE_AGENT);
    }
  };

  const unsigned NEG = 0x80008000u, sgc = (c & 1) ? NEG : 0u;
  const u32x4 c64h = fc.c64 ^ sgc, s64h = fc.s64 ^ sgc, n64 = fc.s64 ^ NEG, n64h = s64h ^ NEG;
  if (w < 4 || special) {
    const int T = w & 3;
    u32x4 zf[2][2][2];
    const unsigned za = L_Z + c * 128 + ((g ^ (c >> 1)) * 16) + (w >> 2) * 4096;
    UNROLL for (int bt = 0; bt < 2; ++bt) UNROLL for (int hh = 0; hh < 2; ++hh) {
      zf[0][bt][hh] = rd128((za ^ (hh * 64)) + bt * 2048);
      zf[1][bt][hh] = rd128((za ^ (hh * 64)) + bt * 2048 + 4096);
    }
    unsigned wv[8];
    const unsigned wa = L_W0 + (w >> 2) * 16384 + g * 1024 + (((16 * T + c) ^ ((g & 1) * 16)) * 4);
    UNROLL for (int j = 0; j < 8; ++j) wv[j] = *(LDS unsigned*)(wa + (j >> 2) * 4096 + (j & 3) * 256);
    if (special) {
      UNROLL for (int bt = 0; bt < 2; ++bt) UNROLL for (int hh = 0; hh < 2; ++hh) zf[1][bt][hh] = u32x4{0, 0, 0, 0};
      BARRIER();
    }
    f32x4 re[2], im[2];
    UNROLL for (int bt = 0; bt < 2; ++bt) {
      re[bt] = mfma(zf[0][bt][0], fc.c64, f32x4{0, 0, 0, 0}); im[bt] = mfma(zf[0][bt][0], n64, f32x4{0, 0, 0, 0});
      re[bt] = mfma(zf[0][bt][1], c64h, re[bt]);             im[bt] = mfma(zf[0][bt][1], n64h, im[bt]);
      re[bt] = mfma(zf[1][bt][0], fc.s64, re[bt]);           im[bt] = mfma(zf[1][bt][0], fc.c64, im[bt]);
      re[bt] = mfma(zf[1][bt][1], s64h, re[bt]);             im[bt] = mfma(zf[1][bt][1], c64h, im[bt]);
    }
    UNROLL for (int j = 0; j < 8; ++j) {
      const float a = re[j >> 2][j & 3], b = im[j >> 2][j & 3];
      re[j >> 2][j & 3] = a * fc.t1c[j] + b * fc.t1s[j]; im[j >> 2][j & 3] = b * fc.t1c[j] - a * fc.t1s[j];
    }
    u32x4 pr = pk8(re[0], re[1]), pi = pk8(im[0], im[1]);
    h_done();
    const u32x4 c32h = fc.c32 ^ 0x80000000u, s32h = fc.s32 ^ 0x80000000u, n32 = fc.s32 ^ NEG, n32h = fc.s32 ^ 0x00008000u;
    re[0] = mfma(fc.c32, pr, f32x4{0, 0, 0, 0}); im[0] = mfma(fc.c32, pi, f32x4{0, 0, 0, 0});
    re[1] = mfma(c32h, pr, f32x4{0, 0, 0, 0});   im[1] = mfma(c32h, pi, f32x4{0, 0, 0, 0});
    re[0] = mfma(fc.s32, pi, re[0]); im[0] = mfma(n32, pr, im[0]);
    re[1] = mfma(s32h, pi, re[1]);   im[1] = mfma(n32h, pr, im[1]);
    UNROLL for (int j = 0; j < 8; ++j) {
      const float a = re[j >> 2][j & 3], b = im[j >> 2][j & 3], fr = lo16(wv[j]), fi = hi16(wv[j]);
      re[j >> 2][j & 3] = a * fr - b * fi; im[j >> 2][j & 3] = a * fi + b * fr;
    }
    pr = pk8(re[0], re[1]); pi = pk8(im[0], im[1]);
    re[0] = mfma(pr, fc.c32, f32x4{0, 0, 0, 0}); im[0] = mfma(pr, fc.s32, f32x4{0, 0, 0, 0});
    re[1] = mfma(pr, c32h, f32x4{0, 0, 0, 0});   im[1] = mfma(pr, s32h, f32x4{0, 0, 0, 0});
    re[0] = mfma(pi, n32, re[0]);  im[0] = mfma(pi, fc.c32, im[0]);
    re[1] = mfma(pi, n32h, re[1]); im[1] = mfma(pi, c32h, im[1]);
    const unsigned qa = ((w >> 2) ? L_Z : L_Q0) + (T >> 1) * 1024 + lane * 16 + (T & 1) * 8;
    UNROLL for (int bt = 0; bt < 2; ++bt) {
      float qr[4], qi[4];
      UNROLL for (int r = 0; r < 4; ++r) {
        const float a = re[bt][r], b = im[bt][r], tc_ = fc.t2c[4 * bt + r], ts_ = fc.t2s[4 * bt + r];
        qr[r] = a * tc_ - b * ts_; qi[r] = a * ts_ + b * tc_;
      }
      *(LDS u32x2*)(qa + bt * 2048) = u32x2{pk(qr[0], qr[1]), pk(qr[2], qr[3])};
      *(LDS u32x2*)(qa + bt * 2048 + 4096) = u32x2{pk(qi[0], qi[1]), pk(qi[2], qi[3])};
    }
  }
  else h_done();
  BARRIER();
  STAMP(7);
  u32x4 yout;
  {
    const unsigned qa = L_Q0 + (w >> 2) * 2048 + lane * 16;
    u32x4 r0 = rd128(qa), r1 = rd128(qa + 1024), i0 = rd128(qa + 4096), i1 = rd128(qa + 5120);
    f32x4 yr = mfma(fc.c64, r0, f32x4{0, 0, 0, 0}), yi;
    yr = mfma(c64h, r1, yr); yr = mfma(n64, i0, yr); yr = mfma(n64h, i1, yr);
    if (special) {
      const unsigned qb = qa - 16384;
      r0 = rd128(qb); r1 = rd128(qb + 1024); i0 = rd128(qb + 4096); i1 = rd128(qb + 5120);
      yi = mfma(fc.c64, r0, f32x4{0, 0, 0, 0}); yi = mfma(c64h, r1, yi); yi = mfma(n64, i0, yi); yi = mfma(n64h, i1, yi);
    } else {
      yi = mfma(fc.s64, r0, f32x4{0, 0, 0, 0}); yi = mfma(s64h, r1, yi); yi = mfma(fc.c64, i0, yi); yi = mfma(c64h, i1, yi);
    }
    yout = u32x4{pk(yr.x, yi.x), pk(yr.y, yi.y), pk(yr.z, yi.z), pk(yr.w, yi.w)};
  }
  if (w == 7) wait_tag(ws, WS_HF + (bo >> 1) * 128 + (c < 14 ? c : 13) * 8, tag);
  BARRIER();
  STAMP(8);
  {
    const unsigned sH = WS_H + (bo * 56 + 14 * kq) * 4096 + th * 2048;
    u32x4 hb[14][2];
    auto ldh = [&](int i) { ald128(hb[i][0], rS, lane * 16, sH + i * 4096); ald128(hb[i][1], rS, lane * 16, sH + i * 4096 + 1024); };
    ldh(0); ldh(1);
    st128(yout, rS, c * 65536 + g * 16, WS_Y + (w >> 2) * 1048576 + col * 256 + (w & 3) * 64);
    issueWd(8, 8); issueWd(9, 9);
    unsigned G[4][16];
    auto genG = [&](int ct) {
      const float st = (float)(64 * nb + 16 * ct + c) * (1.f / 512.f), a0 = __builtin_amdgcn_fractf(st * (float)(64 * kq + 16 * g));
      f32x2 p0 = {fcos(a0), fsin(-a0)}, p1 = {fcos(a0 + st), fsin(-a0 - st)};
      const f32x2 m = sp(2.f * fcos(st));
      G[ct][0] = (kq == 0 && g == 0) ? ((c & 1) ? 0xbf803f80u : 0x3f803f80u) : pk2(p0);
      G[ct][1] = pk2(p1);
      UNROLL for (int n = 2; n < 16; ++n) { const f32x2 p2 = fma2(m, p1, -p0); G[ct][n] = pk2(p2); p0 = p1; p1 = p2; }
      UNROLL for (int n = 0; n < 16; n += 4) asm volatile("" : "+v"(G[ct][n]), "+v"(G[ct][n + 1]), "+v"(G[ct][n + 2]), "+v"(G[ct][n + 3]));
    };
    genG(0); genG(1);
    SCHED();
    WAITV_(0, "+v"(hb[0][0]), "+v"(hb[0][1]), "+v"(hb[1][0]), "+v"(hb[1][1]));
    STAMP(9);
    ldh(2); ldh(3); ldh(4); ldh(5);
    f32x4 acc[4][2];
    UNROLL for (int ct = 0; ct < 4; ++ct) acc[ct][0] = acc[ct][1] = f32x4{0, 0, 0, 0};
    u32x2 yv[16], yf;
    u32x4 xres, A[2][4];
    UNROLL for (int i = 0; i < 14; ++i) {
      if (i >= 3 && i < 13) {
        const unsigned so = (i + 1 < 10 ? i + 1 : i - 9) * 16384;
        UNROLL for (int ct = 0; ct < 4; ++ct) A[(i + 1) & 1][ct] = rdtr(aD[ct] + so, aD[ct] + so + 2048);
      }
      if (i >= 2 && i <= 5) WAITV_(6, "+v"(hb[i][0]), "+v"(hb[i][1]));
      if (i >= 6 && i <= 8) WAITV_(7, "+v"(hb[i][0]), "+v"(hb[i][1]));
      if (i == 9) WAITV_(6, "+v"(hb[i][0]), "+v"(hb[i][1]), "+v"(yf));
      if (i == 10) WAITV_(23, "+v"(hb[i][0]), "+v"(hb[i][1]));
      if (i == 11) WAITV_(21, "+v"(hb[i][0]), "+v"(hb[i][1]));
      if (i == 12) WAITV_(19, "+v"(hb[i][0]), "+v"(hb[i][1]));
      if (i == 13) WAITV_(17, "+v"(hb[i][0]), "+v"(hb[i][1]));
      UNROLL for (int ct = 0; ct < 4; ++ct) {
        const u32x4 a = i < 4 ? park[i][ct] : A[i & 1][ct];
        acc[ct][0] = mfma(a, hb[i][0], acc[ct][0]); acc[ct][1] = mfma(a, hb[i][1], acc[ct][1]);
      }
      SCHED();
      if (i == 1) { BARRIER(); put_tag(ws, WS_YF + col * 8, tag); genG(2); }
      if (i == 2) genG(3);
      if (i == 8) BARRIER();
      if (i == 5) ald64(yf, rS, lane * 8, WS_YF + 512 * kq);
      if (i >= 2 && i + 4 < 14) ldh(i + 4);
      if (i == 9) {
        if (!__all(yf.x == (unsigned)tag && yf.y == (unsigned)(tag >> 32))) wait_tag(ws, WS_YF + (64 * kq + lane) * 8, tag);
        STAMP(10);
        UNROLL for (int n = 0; n < 16; ++n) ald64(yv[n], rS, g * 4096 + c * 8, WS_Y + bo * 65536 + kq * 16384 + th * 128 + n * 256);
        ald128p<0>(xres, rX, (tid >> 3) * 32768 + (tid & 7) * 16, bo * 1024 + nb * 128);
      }
      SCHED();
    }
    UNROLL for (int s = 0; s < 4; ++s) {
      if (s == 0) WAITV_(13, "+v"(yv[0]), "+v"(yv[1]), "+v"(yv[2]), "+v"(yv[3]));
      if (s == 1) WAITV_(9, "+v"(yv[4]), "+v"(yv[5]), "+v"(yv[6]), "+v"(yv[7]));
      if (s == 2) WAITV_(5, "+v"(yv[8]), "+v"(yv[9]), "+v"(yv[10]), "+v"(yv[11]));
      if (s == 3) WAITV_(0, "+v"(yv[12]), "+v"(yv[13]), "+v"(yv[14]), "+v"(yv[15]), "+v"(xres));
      const u32x4 y0 = {yv[4 * s].x, yv[4 * s + 1].x, yv[4 * s + 2].x, yv[4 * s + 3].x}, y1 = {yv[4 * s].y, yv[4 * s + 1].y, yv[4 * s + 2].y, yv[4 * s + 3].y};
      UNROLL for (int ct = 0; ct < 4; ++ct) {
        const u32x4 a = {G[ct][4 * s], G[ct][4 * s + 1], G[ct][4 * s + 2], G[ct][4 * s + 3]};
        acc[ct][0] = mfma(a, y0, acc[ct][0]); acc[ct][1] = mfma(a, y1, acc[ct][1]);
      }
    }
    STAMP(11);
    UNROLL for (int ct = 0; ct < 4; ++ct) UNROLL for (int t = 0; t < 2; ++t)
      wr128(65536 + kq * 16384 + (32 * th + 2 * c + t) * 256 + (((4 * ct + g) ^ (c & 7)) * 16), __builtin_bit_cast(u32x4, acc[ct][t]));
    BARRIER();
    const unsigned ra = 65536 + (tid >> 3) * 256 + (((2 * (tid & 7)) ^ ((tid >> 4) & 7)) * 16);
    f32x4 o0 = {lo16(xres.x), hi16(xres.x), lo16(xres.y), hi16(xres.y)}, o1 = {lo16(xres.z), hi16(xres.z), lo16(xres.w), hi16(xres.w)};
    UNROLL for (int k = 0; k < 4; ++k) {
      o0 += __builtin_bit_cast(f32x4, rd128(ra + k * 16384));
      o1 += __builtin_bit_cast(f32x4, rd128((ra ^ 16) + k * 16384));
    }
    *(GLB u32x4*)(out + (size_t)((tid >> 3) * 32 + bo) * 1024 + nb * 128 + (tid & 7) * 16) = pk8(o0, o1);
    if (bid == 0) put_tag(ws, 0, epoch + 1);
    STAMP(12); DIAG_END();
  }
}
}

namespace {
__global__ void k_pre(u64* p) { if (threadIdx.x == 0) p[512*32] = __builtin_amdgcn_s_memrealtime(); }
__global__ void k_probe2(const u64* p) { __shared__ u64 a[512], b[512]; __shared__ u64 r; const int t = threadIdx.x; u64 s0 = __builtin_amdgcn_s_memrealtime(); (void)a; (void)b; if(t<224) a[t]=(unsigned)(p[((0+t)*2+1)*32+29]-p[((0+t)*2+1)*32]); __syncthreads(); if(t<224){int c=0,e=0; for(int j=0;j<224;++j){c+=a[j]<a[t]; e+=a[j]==a[t];} if(c<=112&&112<c+e) r=a[t]*10;}
  __syncthreads(); u64 d = r;
  if (d > 1000000) d = 1000000; d += 10000;
  if (t < 64) while (__builtin_amdgcn_s_memrealtime() - s0 < d) __builtin_amdgcn_s_sleep(8); } }
extern "C" void kernel_launch(void* const* d_in, const int* in_sizes, int n_in, void* d_out, int out_size, void* d_ws, size_t ws_size,
                              hipStream_t stream) {
  (void)in_sizes; (void)n_in; (void)out_size; (void)ws_size;
  (void)hipFuncSetAttribute((const void*)fused_kernel, hipFuncAttributeMaxDynamicSharedMemorySize, (int)LDS_BYTES);
  fused_kernel<<<256, 512, LDS_BYTES, stream>>>((const char*)d_in[0], (const char*)d_in[1], (const char*)d_in[2], (const char*)d_in[4],
                                                (const char*)d_in[5], (const char*)d_in[6], (char*)d_out, (char*)d_ws);
  k_probe2<<<1,512,0,stream>>>((const u64*)((char*)d_ws+WS_END));
}
```

```cpp
#include <hip/hip_runtime.h>

namespace {
__global__ void __launch_bounds__(512, 2)
fused_kernel(const char* x, const char* wr, const char* wi, const char* wg, const char* wu, const char* wd, char* out, char* ws) {
#if __HIP_DEVICE_COMPILE__
  unsigned tid = threadIdx.x;
  asm volatile(
    "Lentry:\n"
    "s_load_dwordx2 s[4:5], s[0:1], 0x0\n"
    "s_lshr_b32 s34, s2, 3\n"
    "s_and_b32 s3, s2, 8\n"
    "s_add_u32 s60, s3, 0x18\n"
    "s_sub_u32 s61, 0x20, s3\n"
    "s_mov_b32 s10, 1835008\n"
    "s_cmp_ge_u32 s34, 28\n"
    "s_cselect_b32 s60, 8, s60\n"
    "s_cselect_b32 s61, 16, s61\n"
    "s_cselect_b32 s10, 1052672, s10\n"
    "s_load_dwordx2 s[8:9], s[0:1], s60\n"
    "s_load_dwordx2 s[12:13], s[0:1], s61\n"
    "s_load_dwordx2 s[20:21], s[0:1], 0x38\n"
    "s_load_dwordx2 s[16:17], s[0:1], 0x28\n"
    "s_load_dwordx2 s[24:25], s[0:1], 0x30\n"
    "v_readfirstlane_b32 s32, v0\n"
    "v_and_b32 v1, 63, v0\n"
    "v_lshlrev_b32 v250, 4, v1\n"
    "s_lshr_b32 s32, s32, 6\n"
    "s_and_b32 s33, s2, 7\n"
    "s_mov_b32 s6, 2097152\n"
    "s_mov_b32 s7, 0x20000\n"
    "s_mov_b32 s11, s7\n"
    "s_mov_b64 s[14:15], s[10:11]\n"
    "v_lshrrev_b32 v2, 4, v1\n"
    "s_cmp_ge_u32 s34, 28\n"
    "s_cbranch_scc1 Ldft0\n"
    "Lffn0:\n"
    "s_getpc_b64 s[28:29]\n"
    "Lpc:\n"
    "s_mov_b32 s30, Lend-Lpc\n"
    "s_mov_b32 s31, 0x20000\n"
    "s_and_b32 s60, s2, 56\n"
    "s_add_u32 s60, s60, s32\n"
    "s_lshl_b32 s60, s60, 10\n"
    "v_and_b32 v3, 7, v0\n"
    "v_lshlrev_b32 v3, 7, v3\n"
    "buffer_load_dword v255, v3, s[28:31], s60 offen\n"
    "s_lshl_b32 s3, s3, 11\n"
    "s_lshl_b32 s40, s32, 11\n"
    "s_add_u32 s41, s40, s3\n"
    "s_add_u32 s41, s41, 16384\n"
    "s_sub_u32 s42, s40, s3\n"
    "s_add_u32 s42, s42, 32768\n"
    "s_and_b32 s3, s33, 6\n"
    "s_lshl_b32 s38, s3, 7\n"
    "s_and_b32 s60, s34, 3\n"
    "s_lshl1_add_u32 s30, s3, s60\n"
    "s_and_b32 s3, s33, 1\n"
    "s_mul_i32 s3, s3, 7\n"
    "s_lshr_b32 s31, s34, 2\n"
    "s_add_u32 s31, s31, s3\n"
    "s_mul_i32 s39, s38, 1792\n"
    "s_and_b32 s61, s32, 1\n"
    "s_mul_i32 s60, s61, 0x100040\n"
    "s_lshr_b32 s3, s32, 1\n"
    "s_lshl_b32 s3, s3, 15\n"
    "s_or_b32 s60, s60, s3\n"
    "s_lshl_b32 s3, s30, 11\n"
    "s_or_b32 s60, s60, s3\n"
    "v_and_b32 v3, 7, v1\n"
    "v_xor_b32 v3, v3, v2\n"
    "v_lshlrev_b32 v3, 4, v3\n"
    "v_lshrrev_b32 v4, 3, v1\n"
    "v_lshl_or_b32 v3, v4, 17, v3\n"
    "v_xor_b32 v244, s60, v3\n"
    "s_lshl_b32 s3, s61, 2\n"
    "v_bfe_u32 v5, v1, 1, 3\n"
    "v_xor_b32 v3, v5, v2\n"
    "v_xor_b32 v4, s3, v3\n"
    "s_mul_i32 s60, s32, 28672\n"
    "s_lshl_b32 s3, s31, 7\n"
    "s_add_u32 s60, s60, s3\n"
    "v_lshrrev_b32 v6, 2, v4\n"
    "s_and_b32 s3, s3, 128\n"
    "s_lshl_b32 s3, s3, 1\n"
    "s_sub_u32 s3, 1920, s3\n"
    "v_mul_u32_u24 v7, 3584, v2\n"
    "v_mad_u32_u24 v6, v6, s3, v7\n"
    "v_and_b32 v4, 3, v4\n"
    "v_lshl_add_u32 v6, v4, 5, v6\n"
    "v_and_b32 v4, 16, v250\n"
    "v_add3_u32 v245, v6, v4, s60\n"
    "v_add_u32 v246, 13312, v245\n"
    "s_and_b32 s3, s34, 2\n"
    "s_lshl_b32 s3, s3, 9\n"
    "s_add_u32 s44, s41, s3\n"
    "s_mul_i32 s62, s3, 14\n"
    "s_add_u32 s48, s39, s62\n"
    "s_and_b32 s63, s34, 4\n"
    "s_lshl_b32 s63, s63, 8\n"
    "s_add_u32 s46, s40, s63\n"
    "s_add_u32 s55, s38, s63\n"
    "s_add_u32 s49, s48, 229376\n"
    "s_add_u32 s56, s55, 128\n"
    "s_add_u32 m0, s44, 65536\n"
    "s_waitcnt lgkmcnt(0)\n"
    "buffer_load_dwordx4 v245, s[8:11], s48 offen lds\n"
    "s_add_u32 m0, s46, 65536\n"
    "s_nop 0\n"
    "buffer_load_dwordx4 v244, s[4:7], s55 offen lds\n"
    "s_add_u32 m0, s44, 114688\n"
    "s_nop 0\n"
    "buffer_load_dwordx4 v245, s[8:11], s49 offen lds\n"
    "s_add_u32 m0, s46, 114688\n"
    "s_nop 0\n"
    "buffer_load_dwordx4 v244, s[4:7], s56 offen lds\n"
    "s_mov_b32 s22, 16777216\n"
    "s_mov_b32 s23, s7\n"
    "s_mov_b32 s18, 1835008\n"
    "s_mov_b32 s19, s7\n"
    "s_mov_b64 s[26:27], s[6:7]\n"
    "s_mul_i32 s3, s2, 3\n"
    "s_add_u32 s3, s3, s32\n"
    "s_lshl_b32 s3, s3, 7\n"
    "s_add_u32 s3, s3, 32768\n"
    "s_cmp_lt_u32 s32, 3\n"
    "s_cselect_b32 s3, s3, 0\n"
    "v_mul_u32_u24 v252, s3, v1\n"
    "s_mov_b64 exec, 3\n"
    "buffer_load_dwordx2 v[252:253], v252, s[20:23], 0 offen sc1\n"
    "s_mov_b64 exec, -1\n"
    "s_xor_b32 s45, s44, 1024\n"
    "s_xor_b32 s47, s46, 1024\n"
    "s_xor_b32 s57, s55, 1024\n"
    "s_xor_b32 s58, s56, 1024\n"
    "s_sub_u32 s62, 14336, s62\n"
    "s_add_u32 s50, s39, s62\n"
    "s_add_u32 s53, s50, 229376\n"
    "s_mov_b32 s59, s39\n"
    "s_add_u32 s38, s38, 128\n"
    "s_add_u32 s39, s39, 229376\n"
    "s_cmp_ge_u32 s32, 4\n"
    "s_cbranch_scc1 LfB\n"
    "s_branch LfA\n"
    "Ldft0:\n"
    "s_sub_u32 s61, s34, 28\n"
    "s_lshl2_add_u32 s30, s33, s61\n"
    "s_lshl_b32 s60, s32, 8\n"
    "s_add_u32 s60, s60, s30\n"
    "s_lshl_b32 s60, s60, 10\n"
    "v_and_b32 v3, 0x70, v250\n"
    "v_lshrrev_b32 v4, 3, v1\n"
    "v_lshl_or_b32 v233, v4, 15, v3\n"
    "s_waitcnt lgkmcnt(0)\n"
    "buffer_load_dwordx4 v[128:131], v233, s[4:7], s60 offen\n"
    "buffer_load_dwordx4 v[132:135], v233, s[4:7], s60 offen offset:256\n"
    "buffer_load_dwordx4 v[136:139], v233, s[4:7], s60 offen offset:512\n"
    "buffer_load_dwordx4 v[140:143], v233, s[4:7], s60 offen offset:768\n"
    "buffer_load_dwordx4 v[144:147], v233, s[4:7], s60 offen offset:128\n"
    "buffer_load_dwordx4 v[148:151], v233, s[4:7], s60 offen offset:384\n"
    "buffer_load_dwordx4 v[152:155], v233, s[4:7], s60 offen offset:640\n"
    "buffer_load_dwordx4 v[156:159], v233, s[4:7], s60 offen offset:896\n"
    "s_mov_b32 s22, 16777216\n"
    "s_mov_b32 s23, s7\n"
    "s_mov_b32 s18, 1835008\n"
    "s_mov_b32 s19, s7\n"
    "s_mov_b64 s[26:27], s[6:7]\n"
    "buffer_load_dwordx2 v[252:253], off, s[20:23], 0 sc1\n"
    "s_lshl4_add_u32 s61, s33, s61\n"
    "s_mul_i32 s31, s61, 8224\n"
    "s_lshl_b32 s3, s32, 1\n"
    "v_lshrrev_b32 v47, 5, v1\n"
    "v_add_u32 v47, s3, v47\n"
    "v_and_b32 v5, 31, v1\n"
    "v_xor_b32 v5, v5, v47\n"
    "v_mul_u32_u24 v47, 514, v47\n"
    "v_lshl_add_u32 v47, v5, 4, v47\n"
    "s_lshl_b32 s40, s32, 10\n"
    "s_lshl_b32 s3, s32, 5\n"
    "s_add_u32 s61, s3, 1\n"
    "s_and_b32 s41, s3, 0x60\n"
    "s_lshr_b32 s62, s32, 2\n"
    "s_lshl1_add_u32 s41, s62, s41\n"
    "v_and_b32 v6, 15, v1\n"
    "v_and_b32 v9, 7, v1\n"
    "v_and_b32 v10, 8, v1\n"
    "v_lshlrev_b32 v9, 2, v9\n"
    "v_lshl_or_b32 v9, v10, 5, v9\n"
    "v_cvt_f32_u32 v11, v10\n"
    "v_add_u32 v7, s61, v9\n"
    "v_add_u32 v8, s41, v9\n"
    "v_lshlrev_b32 v238, 3, v2\n"
    "v_cvt_f32_u32 v7, v7\n"
    "v_cvt_f32_u32 v8, v8\n"
    "v_cvt_f32_u32 v238, v238\n"
    "v_mul_f32 v9, 0x3dc00000, v11\n"
    "v_mul_f32 v11, 0x3e800000, v11\n"
    "v_sub_f32 v240, 1.0, v11\n"
    "v_mul_f32 v12, 0x3b000000, v7\n"
    "v_add_f32 v14, v12, v12\n"
    "v_mul_f32 v15, 0x42000000, v12\n"
    "v_fma_f32 v16, v12, v238, v9\n"
    "v_add_f32 v20, v16, v15\n"
    "v_add_f32 v18, v16, v14\n"
    "v_cos_f32 v14, v14\n"
    "v_cos_f32 v15, v15\n"
    "v_add_f32 v17, v16, v12\n"
    "v_add_f32 v19, v18, v12\n"
    "v_add_f32 v21, v20, v12\n"
    "v_fmamk_f32 v22, v12, 0x42080000, v16\n"
    "v_fmamk_f32 v23, v12, 0x420c0000, v16\n"
    "v_pk_add_f32 v[14:15], v[14:15], v[14:15]\n"
    "v_cos_f32 v24, v16\n"
    "v_cos_f32 v25, v17\n"
    "v_cos_f32 v26, v18\n"
    "v_cos_f32 v27, v19\n"
    "v_cos_f32 v32, v20\n"
    "v_cos_f32 v33, v21\n"
    "v_cos_f32 v34, v22\n"
    "v_cos_f32 v35, v23\n"
    "v_pk_fma_f32 v[28:29], v[14:15], v[26:27], v[24:25] op_sel_hi:[0,1,1] neg_lo:[0,0,1] neg_hi:[0,0,1]\n"
    "v_pk_fma_f32 v[36:37], v[14:15], v[34:35], v[32:33] op_sel_hi:[0,1,1] neg_lo:[0,0,1] neg_hi:[0,0,1]\n"
    "v_pk_fma_f32 v[30:31], v[14:15], v[28:29], v[26:27] op_sel_hi:[0,1,1] neg_lo:[0,0,1] neg_hi:[0,0,1]\n"
    "v_pk_fma_f32 v[38:39], v[14:15], v[36:37], v[34:35] op_sel_hi:[0,1,1] neg_lo:[0,0,1] neg_hi:[0,0,1]\n"
    "v_cvt_pk_bf16_f32 v64, v24, v26\n"
    "v_cvt_pk_bf16_f32 v65, v28, v30\n"
    "v_cvt_pk_bf16_f32 v72, v25, v27\n"
    "v_cvt_pk_bf16_f32 v73, v29, v31\n"
    "v_pk_fma_f32 v[24:25], v[14:15], v[32:33], v[24:25] op_sel:[1,0,0] neg_lo:[0,0,1] neg_hi:[0,0,1]\n"
    "v_pk_fma_f32 v[26:27], v[14:15], v[34:35], v[26:27] op_sel:[1,0,0] neg_lo:[0,0,1] neg_hi:[0,0,1]\n"
    "v_pk_fma_f32 v[28:29], v[14:15], v[36:37], v[28:29] op_sel:[1,0,0] neg_lo:[0,0,1] neg_hi:[0,0,1]\n"
    "v_pk_fma_f32 v[30:31], v[14:15], v[38:39], v[30:31] op_sel:[1,0,0] neg_lo:[0,0,1] neg_hi:[0,0,1]\n"
    "v_cvt_pk_bf16_f32 v68, v32, v34\n"
    "v_cvt_pk_bf16_f32 v69, v36, v38\n"
    "v_cvt_pk_bf16_f32 v76, v33, v35\n"
    "v_cvt_pk_bf16_f32 v77, v37, v39\n"
    "v_pk_fma_f32 v[32:33], v[14:15], v[24:25], v[32:33] op_sel:[1,0,0] neg_lo:[0,0,1] neg_hi:[0,0,1]\n"
    "v_pk_fma_f32 v[34:35], v[14:15], v[26:27], v[34:35] op_sel:[1,0,0] neg_lo:[0,0,1] neg_hi:[0,0,1]\n"
    "v_pk_fma_f32 v[36:37], v[14:15], v[28:29], v[36:37] op_sel:[1,0,0] neg_lo:[0,0,1] neg_hi:[0,0,1]\n"
    "v_pk_fma_f32 v[38:39], v[14:15], v[30:31], v[38:39] op_sel:[1,0,0] neg_lo:[0,0,1] neg_hi:[0,0,1]\n"
    "v_cvt_pk_bf16_f32 v66, v24, v26\n"
    "v_cvt_pk_bf16_f32 v67, v28, v30\n"
    "v_cvt_pk_bf16_f32 v74, v25, v27\n"
    "v_cvt_pk_bf16_f32 v75, v29, v31\n"
    "v_cvt_pk_bf16_f32 v70, v32, v34\n"
    "v_cvt_pk_bf16_f32 v71, v36, v38\n"
    "v_cvt_pk_bf16_f32 v78, v33, v35\n"
    "v_cvt_pk_bf16_f32 v79, v37, v39\n"
    "v_mov_b32 v242, v240\n"
    "v_mov_b32 v10, 1.0\n"
    "v_or_b32 v11, s3, v6\n"
    "v_cmp_eq_u32 vcc, 8, v11\n"
    "s_nop 0\n"
    "s_mov_b64 exec, vcc\n"
    "v_mov_b32 v8, 0x43000000\n"
    "v_mov_b32 v9, 0x3e000000\n"
    "v_mov_b32 v10, 0x3fb504f3\n"
    "v_mov_b32 v242, 0\n"
    "s_mov_b64 exec, -1\n"
    "v_mul_f32 v12, 0x3b000000, v8\n"
    "v_add_f32 v14, v12, v12\n"
    "v_mul_f32 v15, 0x42000000, v12\n"
    "v_fma_f32 v16, v12, v238, v9\n"
    "v_add_f32 v20, v16, v15\n"
    "v_add_f32 v18, v16, v14\n"
    "v_cos_f32 v14, v14\n"
    "v_cos_f32 v15, v15\n"
    "v_add_f32 v17, v16, v12\n"
    "v_add_f32 v19, v18, v12\n"
    "v_add_f32 v21, v20, v12\n"
    "v_fmamk_f32 v22, v12, 0x42080000, v16\n"
    "v_fmamk_f32 v23, v12, 0x420c0000, v16\n"
    "v_pk_add_f32 v[14:15], v[14:15], v[14:15]\n"
    "v_cos_f32 v24, v16\n"
    "v_cos_f32 v25, v17\n"
    "v_cos_f32 v26, v18\n"
    "v_cos_f32 v27, v19\n"
    "v_cos_f32 v32, v20\n"
    "v_cos_f32 v33, v21\n"
    "v_cos_f32 v34, v22\n"
    "v_cos_f32 v35, v23\n"
    "v_pk_mul_f32 v[24:25], v[24:25], v[10:11] op_sel_hi:[1,0]\n"
    "v_pk_mul_f32 v[26:27], v[26:27], v[10:11] op_sel_hi:[1,0]\n"
    "v_pk_mul_f32 v[32:33], v[32:33], v[10:11] op_sel_hi:[1,0]\n"
    "v_pk_mul_f32 v[34:35], v[34:35], v[10:11] op_sel_hi:[1,0]\n"
    "v_pk_fma_f32 v[28:29], v[14:15], v[26:27], v[24:25] op_sel_hi:[0,1,1] neg_lo:[0,0,1] neg_hi:[0,0,1]\n"
    "v_pk_fma_f32 v[36:37], v[14:15], v[34:35], v[32:33] op_sel_hi:[0,1,1] neg_lo:[0,0,1] neg_hi:[0,0,1]\n"
    "v_pk_fma_f32 v[30:31], v[14:15], v[28:29], v[26:27] op_sel_hi:[0,1,1] neg_lo:[0,0,1] neg_hi:[0,0,1]\n"
    "v_pk_fma_f32 v[38:39], v[14:15], v[36:37], v[34:35] op_sel_hi:[0,1,1] neg_lo:[0,0,1] neg_hi:[0,0,1]\n"
    "v_cvt_pk_bf16_f32 v96, v24, v26\n"
    "v_cvt_pk_bf16_f32 v97, v28, v30\n"
    "v_cvt_pk_bf16_f32 v104, v25, v27\n"
    "v_cvt_pk_bf16_f32 v105, v29, v31\n"
    "v_pk_fma_f32 v[24:25], v[14:15], v[32:33], v[24:25] op_sel:[1,0,0] neg_lo:[0,0,1] neg_hi:[0,0,1]\n"
    "v_pk_fma_f32 v[26:27], v[14:15], v[34:35], v[26:27] op_sel:[1,0,0] neg_lo:[0,0,1] neg_hi:[0,0,1]\n"
    "v_pk_fma_f32 v[28:29], v[14:15], v[36:37], v[28:29] op_sel:[1,0,0] neg_lo:[0,0,1] neg_hi:[0,0,1]\n"
    "v_pk_fma_f32 v[30:31], v[14:15], v[38:39], v[30:31] op_sel:[1,0,0] neg_lo:[0,0,1] neg_hi:[0,0,1]\n"
    "v_cvt_pk_bf16_f32 v100, v32, v34\n"
    "v_cvt_pk_bf16_f32 v101, v36, v38\n"
    "v_cvt_pk_bf16_f32 v108, v33, v35\n"
    "v_cvt_pk_bf16_f32 v109, v37, v39\n"
    "v_pk_fma_f32 v[32:33], v[14:15], v[24:25], v[32:33] op_sel:[1,0,0] neg_lo:[0,0,1] neg_hi:[0,0,1]\n"
    "v_pk_fma_f32 v[34:35], v[14:15], v[26:27], v[34:35] op_sel:[1,0,0] neg_lo:[0,0,1] neg_hi:[0,0,1]\n"
    "v_pk_fma_f32 v[36:37], v[14:15], v[28:29], v[36:37] op_sel:[1,0,0] neg_lo:[0,0,1] neg_hi:[0,0,1]\n"
    "v_pk_fma_f32 v[38:39], v[14:15], v[30:31], v[38:39] op_sel:[1,0,0] neg_lo:[0,0,1] neg_hi:[0,0,1]\n"
    "v_cvt_pk_bf16_f32 v98, v24, v26\n"
    "v_cvt_pk_bf16_f32 v99, v28, v30\n"
    "v_cvt_pk_bf16_f32 v106, v25, v27\n"
    "v_cvt_pk_bf16_f32 v107, v29, v31\n"
    "v_cvt_pk_bf16_f32 v102, v32, v34\n"
    "v_cvt_pk_bf16_f32 v103, v36, v38\n"
    "v_cvt_pk_bf16_f32 v110, v33, v35\n"
    "v_cvt_pk_bf16_f32 v111, v37, v39\n"
    "s_mov_b32 s63, 0xffff0000\n"
    "s_lshl_b32 s3, s32, 3\n"
    "v_add_u32 v44, s3, v4\n"
    "v_and_b32 v46, 7, v1\n"
    "v_xor_b32 v45, v44, v46\n"
    "v_and_b32 v45, 15, v45\n"
    "v_lshlrev_b32 v45, 4, v45\n"
    "v_lshl_or_b32 v44, v44, 8, v45\n"
    "v_xor_b32 v45, 0x80, v44\n"
    "v_lshlrev_b32 v46, 25, v46\n"
    "v_lshl_or_b32 v46, v4, 3, v46\n"
    "s_lshl_b32 s60, s30, 9\n"
    "s_lshl3_add_u32 s60, s3, s60\n"
    "s_add_u32 s60, s60, 16384\n"
    "v_lshrrev_b32 v228, 16, v240\n"
    "v_and_b32 v228, 0x8000, v228\n"
    "v_lshl_or_b32 v228, v228, 16, v228\n"
    "v_xor_b32 v229, 0x80008000, v228\n"
    "v_xor_b32 v5, v6, v2\n"
    "v_lshlrev_b32 v5, 4, v5\n"
    "v_lshl_or_b32 v224, v6, 8, v5\n"
    "v_xor_b32 v225, 64, v224\n"
    "v_xor_b32 v226, 0x80, v224\n"
    "v_xor_b32 v227, 0xc0, v224\n"
    "s_lshl_b32 s3, s32, 6\n"
    "s_add_u32 s3, s3, 65536\n"
    "v_lshl_or_b32 v230, v6, 9, v5\n"
    "v_xor_b32 v230, s3, v230\n"
    "v_mul_u32_u24 v231, 65792, v2\n"
    "v_lshl_add_u32 v231, v6, 4, v231\n"
    "s_mov_b32 s3, 32896\n"
    "v_xor_b32 v232, 7, v4\n"
    "v_mad_u32_u24 v233, v4, s3, v3\n"
    "v_mad_u32_u24 v232, v232, s3, v3\n"
    "v_add_u32 v237, 4096, v233\n"
    "v_add_u32 v239, 1875072, v232\n"
    "v_add_u32 v241, 1879168, v232\n"
    "s_cmp_eq_u32 s32, 0\n"
    "s_cselect_b64 exec, 0xff, 0\n"
    "v_add_u32 v237, 1056768, v233\n"
    "v_add_u32 v239, 4096, v233\n"
    "v_add_u32 v241, 1052672, v233\n"
    "s_mov_b64 exec, -1\n"
    "s_lshl_b32 s3, s32, 12\n"
    "s_add_u32 s3, s3, 131072\n"
    "v_and_b32 v5, 0x70, v5\n"
    "v_lshl_or_b32 v5, v6, 7, v5\n"
    "v_add_u32 v234, s3, v5\n"
    "v_xor_b32 v235, 64, v234\n"
    "v_lshlrev_b32 v8, 4, v4\n"
    "v_xor_b32 v8, v8, v250\n"
    "v_add_u32 v236, s3, v8\n"
    "s_mul_i32 s61, s32, 263168\n"
    "s_add_u32 s39, s61, 3670016\n"
    "s_lshl_b32 s3, s30, 7\n"
    "s_add_u32 s3, s3, 1048576\n"
    "s_add_u32 s38, s3, s61\n"
    "s_sub_u32 s44, s3, s61\n"
    "s_add_u32 s44, s44, 1866752\n"
    "s_and_b32 s61, s32, 3\n"
    "s_mul_i32 s61, s61, 263168\n"
    "s_add_u32 s45, s3, s61\n"
    "s_sub_u32 s46, s3, s61\n"
    "s_lshr_b32 s61, s32, 2\n"
    "s_mul_i32 s3, s61, 16384\n"
    "s_add_u32 s45, s45, s3\n"
    "s_mul_i32 s3, s61, 16512\n"
    "s_sub_u32 s46, s46, s3\n"
    "s_and_b32 s3, s34, 3\n"
    "s_lshl3_add_u32 s3, s3, s33\n"
    "s_lshl_b32 s3, s3, 8\n"
    "s_add_u32 s39, s39, s3\n"
    "s_and_b32 s47, s32, 4\n"
    "s_lshl_b32 s47, s47, 12\n"
    "s_mov_b32 s42, 0x05040100\n"
    "s_mov_b32 s43, 0x07060302\n"
    "v_add_u32 v224, s47, v224\n"
    "v_add_u32 v225, s47, v225\n"
    "v_add_u32 v226, s47, v226\n"
    "v_add_u32 v227, s47, v227\n"
    "s_waitcnt vmcnt(8)\n"
    "v_lshlrev_b32 v12, 16, v128\n"
    "v_and_b32 v13, s63, v128\n"
    "s_waitcnt vmcnt(6)\n"
    "v_lshlrev_b32 v16, 16, v136\n"
    "v_and_b32 v17, s63, v136\n"
    "v_lshlrev_b32 v14, 16, v132\n"
    "v_and_b32 v15, s63, v132\n"
    "s_waitcnt vmcnt(5)\n"
    "v_lshlrev_b32 v18, 16, v140\n"
    "v_and_b32 v19, s63, v140\n"
    "v_pk_add_f32 v[20:21], v[12:13], v[16:17]\n"
    "v_pk_add_f32 v[22:23], v[14:15], v[18:19]\n"
    "v_pk_add_f32 v[160:161], v[12:13], v[16:17] neg_lo:[0,1] neg_hi:[0,1]\n"
    "v_pk_add_f32 v[168:169], v[14:15], v[18:19] neg_lo:[0,1] neg_hi:[0,1]\n"
    "v_pk_add_f32 v[176:177], v[20:21], v[22:23]\n"
    "v_pk_add_f32 v[184:185], v[20:21], v[22:23] neg_lo:[0,1] neg_hi:[0,1]\n"
    "v_lshlrev_b32 v12, 16, v129\n"
    "v_and_b32 v13, s63, v129\n"
    "v_lshlrev_b32 v16, 16, v137\n"
    "v_and_b32 v17, s63, v137\n"
    "v_lshlrev_b32 v14, 16, v133\n"
    "v_and_b32 v15, s63, v133\n"
    "v_lshlrev_b32 v18, 16, v141\n"
    "v_and_b32 v19, s63, v141\n"
    "v_pk_add_f32 v[20:21], v[12:13], v[16:17]\n"
    "v_pk_add_f32 v[22:23], v[14:15], v[18:19]\n"
    "v_pk_add_f32 v[162:163], v[12:13], v[16:17] neg_lo:[0,1] neg_hi:[0,1]\n"
    "v_pk_add_f32 v[170:171], v[14:15], v[18:19] neg_lo:[0,1] neg_hi:[0,1]\n"
    "v_pk_add_f32 v[178:179], v[20:21], v[22:23]\n"
    "v_pk_add_f32 v[186:187], v[20:21], v[22:23] neg_lo:[0,1] neg_hi:[0,1]\n"
    "v_lshlrev_b32 v12, 16, v130\n"
    "v_and_b32 v13, s63, v130\n"
    "v_lshlrev_b32 v16, 16, v138\n"
    "v_and_b32 v17, s63, v138\n"
    "v_lshlrev_b32 v14, 16, v134\n"
    "v_and_b32 v15, s63, v134\n"
    "v_lshlrev_b32 v18, 16, v142\n"
    "v_and_b32 v19, s63, v142\n"
    "v_pk_add_f32 v[20:21], v[12:13], v[16:17]\n"
    "v_pk_add_f32 v[22:23], v[14:15], v[18:19]\n"
    "v_pk_add_f32 v[164:165], v[12:13], v[16:17] neg_lo:[0,1] neg_hi:[0,1]\n"
    "v_pk_add_f32 v[172:173], v[14:15], v[18:19] neg_lo:[0,1] neg_hi:[0,1]\n"
    "v_pk_add_f32 v[180:181], v[20:21], v[22:23]\n"
    "v_pk_add_f32 v[188:189], v[20:21], v[22:23] neg_lo:[0,1] neg_hi:[0,1]\n"
    "v_lshlrev_b32 v12, 16, v131\n"
    "v_and_b32 v13, s63, v131\n"
    "v_lshlrev_b32 v16, 16, v139\n"
    "v_and_b32 v17, s63, v139\n"
    "v_lshlrev_b32 v14, 16, v135\n"
    "v_and_b32 v15, s63, v135\n"
    "v_lshlrev_b32 v18, 16, v143\n"
    "v_and_b32 v19, s63, v143\n"
    "v_pk_add_f32 v[20:21], v[12:13], v[16:17]\n"
    "v_pk_add_f32 v[22:23], v[14:15], v[18:19]\n"
    "v_pk_add_f32 v[166:167], v[12:13], v[16:17] neg_lo:[0,1] neg_hi:[0,1]\n"
    "v_pk_add_f32 v[174:175], v[14:15], v[18:19] neg_lo:[0,1] neg_hi:[0,1]\n"
    "v_pk_add_f32 v[182:183], v[20:21], v[22:23]\n"
    "v_pk_add_f32 v[190:191], v[20:21], v[22:23] neg_lo:[0,1] neg_hi:[0,1]\n"
    "v_pk_add_f32 v[20:21], v[176:177], v[178:179]\n"
    "v_pk_add_f32 v[22:23], v[180:181], v[182:183]\n"
    "v_pk_add_f32 v[24:25], v[20:21], v[22:23]\n"
    "s_waitcnt vmcnt(4)\n"
    "v_lshlrev_b32 v12, 16, v144\n"
    "v_and_b32 v13, s63, v144\n"
    "s_waitcnt vmcnt(2)\n"
    "v_lshlrev_b32 v16, 16, v152\n"
    "v_and_b32 v17, s63, v152\n"
    "v_lshlrev_b32 v14, 16, v148\n"
    "v_and_b32 v15, s63, v148\n"
    "s_waitcnt vmcnt(1)\n"
    "v_lshlrev_b32 v18, 16, v156\n"
    "v_and_b32 v19, s63, v156\n"
    "v_pk_add_f32 v[20:21], v[12:13], v[16:17]\n"
    "v_pk_add_f32 v[22:23], v[14:15], v[18:19]\n"
    "v_pk_add_f32 v[192:193], v[12:13], v[16:17] neg_lo:[0,1] neg_hi:[0,1]\n"
    "v_pk_add_f32 v[200:201], v[14:15], v[18:19] neg_lo:[0,1] neg_hi:[0,1]\n"
    "v_pk_add_f32 v[208:209], v[20:21], v[22:23]\n"
    "v_pk_add_f32 v[216:217], v[20:21], v[22:23] neg_lo:[0,1] neg_hi:[0,1]\n"
    "v_lshlrev_b32 v12, 16, v145\n"
    "v_and_b32 v13, s63, v145\n"
    "v_lshlrev_b32 v16, 16, v153\n"
    "v_and_b32 v17, s63, v153\n"
    "v_lshlrev_b32 v14, 16, v149\n"
    "v_and_b32 v15, s63, v149\n"
    "v_lshlrev_b32 v18, 16, v157\n"
    "v_and_b32 v19, s63, v157\n"
    "v_pk_add_f32 v[20:21], v[12:13], v[16:17]\n"
    "v_pk_add_f32 v[22:23], v[14:15], v[18:19]\n"
    "v_pk_add_f32 v[194:195], v[12:13], v[16:17] neg_lo:[0,1] neg_hi:[0,1]\n"
    "v_pk_add_f32 v[202:203], v[14:15], v[18:19] neg_lo:[0,1] neg_hi:[0,1]\n"
    "v_pk_add_f32 v[210:211], v[20:21], v[22:23]\n"
    "v_pk_add_f32 v[218:219], v[20:21], v[22:23] neg_lo:[0,1] neg_hi:[0,1]\n"
    "v_lshlrev_b32 v12, 16, v146\n"
    "v_and_b32 v13, s63, v146\n"
    "v_lshlrev_b32 v16, 16, v154\n"
    "v_and_b32 v17, s63, v154\n"
    "v_lshlrev_b32 v14, 16, v150\n"
    "s_add_u32 m0, s40, 65536\n"
    "s_add_u32 s62, s31, 0\n"
    "v_and_b32 v15, s63, v150\n"
    "buffer_load_dwordx4 v47, s[8:11], s62 offen lds\n"
    "v_lshlrev_b32 v18, 16, v158\n"
    "v_and_b32 v19, s63, v158\n"
    "v_pk_add_f32 v[20:21], v[12:13], v[16:17]\n"
    "v_pk_add_f32 v[22:23], v[14:15], v[18:19]\n"
    "v_pk_add_f32 v[196:197], v[12:13], v[16:17] neg_lo:[0,1] neg_hi:[0,1]\n"
    "v_pk_add_f32 v[204:205], v[14:15], v[18:19] neg_lo:[0,1] neg_hi:[0,1]\n"
    "v_pk_add_f32 v[212:213], v[20:21], v[22:23]\n"
    "v_pk_add_f32 v[220:221], v[20:21], v[22:23] neg_lo:[0,1] neg_hi:[0,1]\n"
    "v_lshlrev_b32 v12, 16, v147\n"
    "v_and_b32 v13, s63, v147\n"
    "v_lshlrev_b32 v16, 16, v155\n"
    "v_and_b32 v17, s63, v155\n"
    "v_lshlrev_b32 v14, 16, v151\n"
    "v_and_b32 v15, s63, v151\n"
    "v_lshlrev_b32 v18, 16, v159\n"
    "v_and_b32 v19, s63, v159\n"
    "s_add_u32 m0, s40, 98304\n"
    "s_add_u32 s62, s31, 0\n"
    "v_pk_add_f32 v[20:21], v[12:13], v[16:17]\n"
    "buffer_load_dwordx4 v47, s[12:15], s62 offen lds\n"
    "v_pk_add_f32 v[22:23], v[14:15], v[18:19]\n"
    "v_pk_add_f32 v[198:199], v[12:13], v[16:17] neg_lo:[0,1] neg_hi:[0,1]\n"
    "v_pk_add_f32 v[206:207], v[14:15], v[18:19] neg_lo:[0,1] neg_hi:[0,1]\n"
    "v_pk_add_f32 v[214:215], v[20:21], v[22:23]\n"
    "v_pk_add_f32 v[222:223], v[20:21], v[22:23] neg_lo:[0,1] neg_hi:[0,1]\n"
    "v_pk_add_f32 v[20:21], v[208:209], v[210:211]\n"
    "v_pk_add_f32 v[22:23], v[212:213], v[214:215]\n"
    "v_pk_add_f32 v[26:27], v[20:21], v[22:23]\n"
    "s_waitcnt vmcnt(2)\n"
    "v_readfirstlane_b32 s35, v252\n"
    "v_readfirstlane_b32 s37, v253\n"
    "s_add_u32 s36, s35, 0x632be59b\n"
    "s_xor_b32 s37, s37, 0x9e3779b9\n"
    "s_add_u32 s35, s35, 1\n"
    "v_pk_add_f32 v[24:25], v[24:25], v[26:27]\n"
    "v_add_f32 v24, v24, v25\n"
    "v_pk_mul_f32 v[28:29], v[160:161], v[160:161]\n"
    "v_pk_mul_f32 v[32:33], v[184:185], v[184:185]\n"
    "s_add_u32 m0, s40, 73728\n"
    "s_add_u32 s62, s31, 32896\n"
    "v_add_f32_dpp v24, v24, v24 quad_perm:[1,0,3,2] row_mask:0xf bank_mask:0xf\n"
    "buffer_load_dwordx4 v47, s[8:11], s62 offen lds\n"
    "v_pk_fma_f32 v[28:29], v[162:163], v[162:163], v[28:29]\n"
    "v_pk_fma_f32 v[28:29], v[164:165], v[164:165], v[28:29]\n"
    "v_add_f32_dpp v24, v24, v24 quad_perm:[2,3,0,1] row_mask:0xf bank_mask:0xf\n"
    "v_pk_fma_f32 v[32:33], v[186:187], v[186:187], v[32:33]\n"
    "v_pk_fma_f32 v[28:29], v[166:167], v[166:167], v[28:29]\n"
    "v_add_f32_dpp v24, v24, v24 row_half_mirror row_mask:0xf bank_mask:0xf\n"
    "v_pk_fma_f32 v[28:29], v[192:193], v[192:193], v[28:29]\n"
    "v_pk_fma_f32 v[32:33], v[188:189], v[188:189], v[32:33]\n"
    "v_mul_f32 v25, 0x3c000000, v24\n"
    "v_mul_f32 v24, 0x3b000000, v24\n"
    "v_pk_fma_f32 v[28:29], v[194:195], v[194:195], v[28:29]\n"
    "v_pk_fma_f32 v[28:29], v[196:197], v[196:197], v[28:29]\n"
    "v_pk_fma_f32 v[32:33], v[190:191], v[190:191], v[32:33]\n"
    "v_pk_fma_f32 v[28:29], v[198:199], v[198:199], v[28:29]\n"
    "v_pk_fma_f32 v[28:29], v[168:169], v[168:169], v[28:29]\n"
    "v_pk_fma_f32 v[32:33], v[216:217], v[216:217], v[32:33]\n"
    "s_add_u32 m0, s40, 106496\n"
    "s_add_u32 s62, s31, 32896\n"
    "v_pk_fma_f32 v[28:29], v[170:171], v[170:171], v[28:29]\n"
    "buffer_load_dwordx4 v47, s[12:15], s62 offen lds\n"
    "v_pk_fma_f32 v[28:29], v[172:173], v[172:173], v[28:29]\n"
    "v_pk_fma_f32 v[32:33], v[218:219], v[218:219], v[32:33]\n"
    "v_pk_fma_f32 v[28:29], v[174:175], v[174:175], v[28:29]\n"
    "v_pk_fma_f32 v[28:29], v[200:201], v[200:201], v[28:29]\n"
    "v_pk_fma_f32 v[32:33], v[220:221], v[220:221], v[32:33]\n"
    "v_pk_fma_f32 v[28:29], v[202:203], v[202:203], v[28:29]\n"
    "v_pk_fma_f32 v[28:29], v[204:205], v[204:205], v[28:29]\n"
    "v_pk_fma_f32 v[32:33], v[222:223], v[222:223], v[32:33]\n"
    "v_pk_fma_f32 v[28:29], v[206:207], v[206:207], v[28:29]\n"
    "v_pk_add_f32 v[176:177], v[24:25], v[176:177] op_sel:[1,0] neg_lo:[1,0] neg_hi:[1,0]\n"
    "v_pk_add_f32 v[178:179], v[24:25], v[178:179] op_sel:[1,0] neg_lo:[1,0] neg_hi:[1,0]\n"
    "v_pk_add_f32 v[180:181], v[24:25], v[180:181] op_sel:[1,0] neg_lo:[1,0] neg_hi:[1,0]\n"
    "v_pk_add_f32 v[182:183], v[24:25], v[182:183] op_sel:[1,0] neg_lo:[1,0] neg_hi:[1,0]\n"
    "v_pk_add_f32 v[208:209], v[24:25], v[208:209] op_sel:[1,0] neg_lo:[1,0] neg_hi:[1,0]\n"
    "v_pk_add_f32 v[210:211], v[24:25], v[210:211] op_sel:[1,0] neg_lo:[1,0] neg_hi:[1,0]\n"
    "v_pk_add_f32 v[212:213], v[24:25], v[212:213] op_sel:[1,0] neg_lo:[1,0] neg_hi:[1,0]\n"
    "v_pk_add_f32 v[214:215], v[24:25], v[214:215] op_sel:[1,0] neg_lo:[1,0] neg_hi:[1,0]\n"
    "s_add_u32 m0, s40, 81920\n"
    "s_add_u32 s62, s31, 65792\n"
    "v_pk_fma_f32 v[32:33], v[176:177], v[176:177], v[32:33]\n"
    "buffer_load_dwordx4 v47, s[8:11], s62 offen lds\n"
    "v_pk_fma_f32 v[32:33], v[178:179], v[178:179], v[32:33]\n"
    "v_pk_fma_f32 v[32:33], v[180:181], v[180:181], v[32:33]\n"
    "v_pk_fma_f32 v[32:33], v[182:183], v[182:183], v[32:33]\n"
    "v_pk_fma_f32 v[32:33], v[208:209], v[208:209], v[32:33]\n"
    "v_pk_fma_f32 v[32:33], v[210:211], v[210:211], v[32:33]\n"
    "v_pk_fma_f32 v[32:33], v[212:213], v[212:213], v[32:33]\n"
    "v_pk_fma_f32 v[32:33], v[214:215], v[214:215], v[32:33]\n"
    "v_add_f32 v28, v28, v29\n"
    "v_add_f32 v32, v32, v33\n"
    "v_fmac_f32 v28, 0.5, v32\n"
    "v_xor_b32_dpp v80, v64, v229 row_ror:8 row_mask:0xf bank_mask:0xf\n"
    "v_xor_b32_dpp v81, v65, v229 row_ror:8 row_mask:0xf bank_mask:0xf\n"
    "v_add_f32_dpp v28, v28, v28 quad_perm:[1,0,3,2] row_mask:0xf bank_mask:0xf\n"
    "v_xor_b32_dpp v82, v66, v229 row_ror:8 row_mask:0xf bank_mask:0xf\n"
    "v_xor_b32_dpp v83, v67, v229 row_ror:8 row_mask:0xf bank_mask:0xf\n"
    "v_add_f32_dpp v28, v28, v28 quad_perm:[2,3,0,1] row_mask:0xf bank_mask:0xf\n"
    "s_add_u32 m0, s40, 114688\n"
    "s_add_u32 s62, s31, 65792\n"
    "v_xor_b32_dpp v84, v68, v229 row_ror:8 row_mask:0xf bank_mask:0xf\n"
    "buffer_load_dwordx4 v47, s[12:15], s62 offen lds\n"
    "v_xor_b32_dpp v85, v69, v229 row_ror:8 row_mask:0xf bank_mask:0xf\n"
    "v_add_f32_dpp v28, v28, v28 row_half_mirror row_mask:0xf bank_mask:0xf\n"
    "v_mul_f32 v28, 0x3a800000, v28\n"
    "v_xor_b32_dpp v86, v70, v229 row_ror:8 row_mask:0xf bank_mask:0xf\n"
    "v_cvt_pk_bf16_f32 v28, v28, v28\n"
    "v_xor_b32_dpp v87, v71, v229 row_ror:8 row_mask:0xf bank_mask:0xf\n"
    "v_lshlrev_b32 v28, 16, v28\n"
    "v_add_f32 v28, 0x3727c5ac, v28\n"
    "v_xor_b32_dpp v88, v72, v228 row_ror:8 row_mask:0xf bank_mask:0xf\n"
    "v_cvt_pk_bf16_f32 v28, v28, v28\n"
    "v_xor_b32_dpp v89, v73, v228 row_ror:8 row_mask:0xf bank_mask:0xf\n"
    "v_lshlrev_b32 v28, 16, v28\n"
    "v_sqrt_f32 v28, v28\n"
    "v_xor_b32_dpp v90, v74, v228 row_ror:8 row_mask:0xf bank_mask:0xf\n"
    "v_mov_b32 v37, s36\n"
    "v_xor_b32_dpp v91, v75, v228 row_ror:8 row_mask:0xf bank_mask:0xf\n"
    "v_cvt_pk_bf16_f32 v36, v24, v28\n"
    "s_add_u32 m0, s40, 90112\n"
    "s_add_u32 s62, s31, 98688\n"
    "v_and_b32 v40, s63, v36\n"
    "buffer_load_dwordx4 v47, s[8:11], s62 offen lds\n"
    "v_xor_b32_dpp v92, v76, v228 row_ror:8 row_mask:0xf bank_mask:0xf\n"
    "v_lshlrev_b32 v41, 16, v36\n"
    "v_xor_b32_dpp v93, v77, v228 row_ror:8 row_mask:0xf bank_mask:0xf\n"
    "v_rcp_f32 v40, v40\n"
    "v_sub_f32 v41, v24, v41\n"
    "v_xor_b32_dpp v94, v78, v228 row_ror:8 row_mask:0xf bank_mask:0xf\n"
    "v_mul_f32 v41, 4.0, v41\n"
    "v_xor_b32_dpp v95, v79, v228 row_ror:8 row_mask:0xf bank_mask:0xf\n"
    "v_mul_f32 v41, v41, v40\n"
    "v_pk_mul_f32 v[160:161], v[160:161], v[40:41] op_sel_hi:[1,0]\n"
    "v_pk_mul_f32 v[162:163], v[162:163], v[40:41] op_sel_hi:[1,0]\n"
    "v_pk_mul_f32 v[164:165], v[164:165], v[40:41] op_sel_hi:[1,0]\n"
    "v_pk_mul_f32 v[166:167], v[166:167], v[40:41] op_sel_hi:[1,0]\n"
    "v_pk_mul_f32 v[192:193], v[192:193], v[40:41] op_sel_hi:[1,0]\n"
    "v_pk_mul_f32 v[194:195], v[194:195], v[40:41] op_sel_hi:[1,0]\n"
    "v_pk_mul_f32 v[196:197], v[196:197], v[40:41] op_sel_hi:[1,0]\n"
    "s_add_u32 m0, s40, 122880\n"
    "s_add_u32 s62, s31, 98688\n"
    "v_pk_mul_f32 v[198:199], v[198:199], v[40:41] op_sel_hi:[1,0]\n"
    "buffer_load_dwordx4 v47, s[12:15], s62 offen lds\n"
    "v_cvt_pk_bf16_f32 v128, v160, v162\n"
    "v_cvt_pk_bf16_f32 v129, v164, v166\n"
    "v_cvt_pk_bf16_f32 v130, v192, v194\n"
    "v_cvt_pk_bf16_f32 v131, v196, v198\n"
    "ds_write_b128 v44, v[128:131]\n"
    "v_cvt_pk_bf16_f32 v132, v161, v163\n"
    "v_cvt_pk_bf16_f32 v133, v165, v167\n"
    "v_cvt_pk_bf16_f32 v134, v193, v195\n"
    "v_cvt_pk_bf16_f32 v135, v197, v199\n"
    "ds_write_b128 v45, v[132:135]\n"
    "v_pk_mul_f32 v[168:169], v[168:169], v[40:41] op_sel_hi:[1,0]\n"
    "v_pk_mul_f32 v[170:171], v[170:171], v[40:41] op_sel_hi:[1,0]\n"
    "v_pk_mul_f32 v[172:173], v[172:173], v[40:41] op_sel_hi:[1,0]\n"
    "v_pk_mul_f32 v[174:175], v[174:175], v[40:41] op_sel_hi:[1,0]\n"
    "v_pk_mul_f32 v[200:201], v[200:201], v[40:41] op_sel_hi:[1,0]\n"
    "v_pk_mul_f32 v[202:203], v[202:203], v[40:41] op_sel_hi:[1,0]\n"
    "v_pk_mul_f32 v[204:205], v[204:205], v[40:41] op_sel_hi:[1,0]\n"
    "v_pk_mul_f32 v[206:207], v[206:207], v[40:41] op_sel_hi:[1,0]\n"
    "v_cvt_pk_bf16_f32 v136, v168, v170\n"
    "v_cvt_pk_bf16_f32 v137, v172, v174\n"
    "v_cvt_pk_bf16_f32 v138, v200, v202\n"
    "v_cvt_pk_bf16_f32 v139, v204, v206\n"
    "ds_write_b128 v44, v[136:139] offset:16384\n"
    "v_cvt_pk_bf16_f32 v140, v169, v171\n"
    "v_cvt_pk_bf16_f32 v141, v173, v175\n"
    "v_cvt_pk_bf16_f32 v142, v201, v203\n"
    "v_cvt_pk_bf16_f32 v143, v205, v207\n"
    "ds_write_b128 v45, v[140:143] offset:16384\n"
    "v_pk_fma_f32 v[176:177], v[40:41], v[176:177], v[40:41] op_sel:[0,0,1] op_sel_hi:[0,1,1]\n"
    "v_pk_fma_f32 v[178:179], v[40:41], v[178:179], v[40:41] op_sel:[0,0,1] op_sel_hi:[0,1,1]\n"
    "v_pk_fma_f32 v[180:181], v[40:41], v[180:181], v[40:41] op_sel:[0,0,1] op_sel_hi:[0,1,1]\n"
    "v_pk_fma_f32 v[182:183], v[40:41], v[182:183], v[40:41] op_sel:[0,0,1] op_sel_hi:[0,1,1]\n"
    "v_pk_fma_f32 v[208:209], v[40:41], v[208:209], v[40:41] op_sel:[0,0,1] op_sel_hi:[0,1,1]\n"
    "v_pk_fma_f32 v[210:211], v[40:41], v[210:211], v[40:41] op_sel:[0,0,1] op_sel_hi:[0,1,1]\n"
    "v_pk_fma_f32 v[212:213], v[40:41], v[212:213], v[40:41] op_sel:[0,0,1] op_sel_hi:[0,1,1]\n"
    "v_pk_fma_f32 v[214:215], v[40:41], v[214:215], v[40:41] op_sel:[0,0,1] op_sel_hi:[0,1,1]\n"
    "v_cvt_pk_bf16_f32 v144, v176, v178\n"
    "v_cvt_pk_bf16_f32 v145, v180, v182\n"
    "v_cvt_pk_bf16_f32 v146, v208, v210\n"
    "v_cvt_pk_bf16_f32 v147, v212, v214\n"
    "ds_write_b128 v44, v[144:147] offset:32768\n"
    "v_cvt_pk_bf16_f32 v148, v177, v179\n"
    "v_cvt_pk_bf16_f32 v149, v181, v183\n"
    "v_cvt_pk_bf16_f32 v150, v209, v211\n"
    "v_cvt_pk_bf16_f32 v151, v213, v215\n"
    "ds_write_b128 v45, v[148:151] offset:32768\n"
    "v_pk_mul_f32 v[184:185], v[184:185], v[40:41] op_sel_hi:[1,0]\n"
    "v_pk_mul_f32 v[186:187], v[186:187], v[40:41] op_sel_hi:[1,0]\n"
    "v_pk_mul_f32 v[188:189], v[188:189], v[40:41] op_sel_hi:[1,0]\n"
    "v_pk_mul_f32 v[190:191], v[190:191], v[40:41] op_sel_hi:[1,0]\n"
    "v_pk_mul_f32 v[216:217], v[216:217], v[40:41] op_sel_hi:[1,0]\n"
    "v_pk_mul_f32 v[218:219], v[218:219], v[40:41] op_sel_hi:[1,0]\n"
    "v_pk_mul_f32 v[220:221], v[220:221], v[40:41] op_sel_hi:[1,0]\n"
    "v_pk_mul_f32 v[222:223], v[222:223], v[40:41] op_sel_hi:[1,0]\n"
    "v_cvt_pk_bf16_f32 v152, v184, v186\n"
    "v_cvt_pk_bf16_f32 v153, v188, v190\n"
    "v_cvt_pk_bf16_f32 v154, v216, v218\n"
    "v_cvt_pk_bf16_f32 v155, v220, v222\n"
    "ds_write_b128 v44, v[152:155] offset:49152\n"
    "v_cvt_pk_bf16_f32 v156, v185, v187\n"
    "v_cvt_pk_bf16_f32 v157, v189, v191\n"
    "v_cvt_pk_bf16_f32 v158, v217, v219\n"
    "v_cvt_pk_bf16_f32 v159, v221, v223\n"
    "ds_write_b128 v45, v[156:159] offset:49152\n"
    "buffer_store_dwordx2 v[36:37], v46, s[20:23], s60 offen sc1\n"
    "s_cmp_lg_u32 s32, 0\n"
    "s_cbranch_scc1 Ldft1\n"
    "Ldc1:\n"
    "v_and_b32 v5, 1, v2\n"
    "v_lshl_or_b32 v6, v5, 7, v6\n"
    "v_mul_u32_u24 v6, 514, v6\n"
    "s_add_u32 s62, s31, 32896\n"
    "buffer_load_ushort v244, v6, s[8:11], s31 offen offset:512\n"
    "buffer_load_ushort v245, v6, s[8:11], s62 offen offset:512\n"
    "buffer_load_ushort v246, v6, s[12:15], s31 offen offset:512\n"
    "buffer_load_ushort v247, v6, s[12:15], s62 offen offset:512\n"
    "Ldft1:\n"
    "s_waitcnt lgkmcnt(0)\n"
    "s_barrier\n"
    "ds_read_b128 v[128:131], v224 offset:32768\n"
    "ds_read_b128 v[132:135], v224 offset:36864\n"
    "ds_read_b128 v[136:139], v224 offset:40960\n"
    "ds_read_b128 v[140:143], v224 offset:45056\n"
    "ds_read_b128 v[144:147], v225 offset:32768\n"
    "ds_read_b128 v[148:151], v225 offset:36864\n"
    "ds_read_b128 v[152:155], v225 offset:40960\n"
    "ds_read_b128 v[156:159], v225 offset:45056\n"
    "s_waitcnt lgkmcnt(7)\n"
    "v_mfma_f32_16x16x32_bf16 v[32:35], v[128:131], v[96:99], 0\n"
    "ds_read_b128 v[128:131], v226 offset:32768\n"
    "s_waitcnt lgkmcnt(7)\n"
    "v_mfma_f32_16x16x32_bf16 v[36:39], v[132:135], v[96:99], 0\n"
    "ds_read_b128 v[132:135], v226 offset:36864\n"
    "s_waitcnt lgkmcnt(7)\n"
    "v_mfma_f32_16x16x32_bf16 v[40:43], v[136:139], v[96:99], 0\n"
    "ds_read_b128 v[136:139], v226 offset:40960\n"
    "s_waitcnt lgkmcnt(7)\n"
    "v_mfma_f32_16x16x32_bf16 v[44:47], v[140:143], v[96:99], 0\n"
    "ds_read_b128 v[140:143], v226 offset:45056\n"
    "s_waitcnt lgkmcnt(7)\n"
    "v_mfma_f32_16x16x32_bf16 v[32:35], v[144:147], v[100:103], v[32:35]\n"
    "ds_read_b128 v[144:147], v227 offset:32768\n"
    "s_waitcnt lgkmcnt(7)\n"
    "v_mfma_f32_16x16x32_bf16 v[36:39], v[148:151], v[100:103], v[36:39]\n"
    "ds_read_b128 v[148:151], v227 offset:36864\n"
    "s_waitcnt lgkmcnt(7)\n"
    "v_mfma_f32_16x16x32_bf16 v[40:43], v[152:155], v[100:103], v[40:43]\n"
    "ds_read_b128 v[152:155], v227 offset:40960\n"
    "s_waitcnt lgkmcnt(7)\n"
    "v_mfma_f32_16x16x32_bf16 v[44:47], v[156:159], v[100:103], v[44:47]\n"
    "ds_read_b128 v[156:159], v227 offset:45056\n"
    "s_waitcnt lgkmcnt(7)\n"
    "v_mfma_f32_16x16x32_bf16 v[48:51], v[128:131], v[104:107], 0\n"
    "v_subrev_u32 v224, s47, v224\n"
    "v_subrev_u32 v225, s47, v225\n"
    "v_subrev_u32 v226, s47, v226\n"
    "v_subrev_u32 v227, s47, v227\n"
    "ds_read_b128 v[128:131], v224\n"
    "s_waitcnt vmcnt(1)\n"
    "s_barrier\n"
    "ds_read_b128 v[176:179], v230\n"
    "ds_read_b128 v[180:183], v230 offset:8192\n"
    "s_waitcnt lgkmcnt(9)\n"
    "v_mfma_f32_16x16x32_bf16 v[52:55], v[132:135], v[104:107], 0\n"
    "ds_read_b128 v[132:135], v224 offset:4096\n"
    "ds_read_b128 v[184:187], v230 offset:16384\n"
    "ds_read_b128 v[188:191], v230 offset:24576\n"
    "s_waitcnt lgkmcnt(11)\n"
    "v_mfma_f32_16x16x32_bf16 v[56:59], v[136:139], v[104:107], 0\n"
    "ds_read_b128 v[136:139], v224 offset:8192\n"
    "ds_read_b128 v[192:195], v230 offset:32768\n"
    "ds_read_b128 v[196:199], v230 offset:40960\n"
    "s_waitcnt lgkmcnt(13)\n"
    "v_mfma_f32_16x16x32_bf16 v[60:63], v[140:143], v[104:107], 0\n"
    "ds_read_b128 v[140:143], v224 offset:12288\n"
    "ds_read_b128 v[200:203], v230 offset:49152\n"
    "ds_read_b128 v[204:207], v230 offset:57344\n"
    "s_waitcnt lgkmcnt(14)\n"
    "v_mfma_f32_16x16x32_bf16 v[48:51], v[144:147], v[108:111], v[48:51]\n"
    "ds_read_b128 v[144:147], v224 offset:16384\n"
    "v_mfma_f32_16x16x32_bf16 v[52:55], v[148:151], v[108:111], v[52:55]\n"
    "ds_read_b128 v[148:151], v224 offset:20480\n"
    "s_waitcnt lgkmcnt(14)\n"
    "v_mfma_f32_16x16x32_bf16 v[56:59], v[152:155], v[108:111], v[56:59]\n"
    "ds_read_b128 v[152:155], v224 offset:24576\n"
    "s_waitcnt lgkmcnt(12)\n"
    "v_perm_b32 v208, v180, v176, s42\n"
    "s_waitcnt lgkmcnt(9)\n"
    "v_perm_b32 v209, v188, v184, s42\n"
    "v_mfma_f32_16x16x32_bf16 v[60:63], v[156:159], v[108:111], v[60:63]\n"
    "ds_read_b128 v[156:159], v224 offset:28672\n"
    "s_waitcnt lgkmcnt(7)\n"
    "v_perm_b32 v210, v196, v192, s42\n"
    "s_waitcnt lgkmcnt(4)\n"
    "v_perm_b32 v211, v204, v200, s42\n"
    "v_mfma_f32_16x16x32_bf16 v[0:3], v[128:131], v[64:67], 0\n"
    "ds_read_b128 v[128:131], v225\n"
    "s_add_u32 s62, s39, 0\n"
    "buffer_store_dwordx4 v[208:211], v231, s[20:23], s62 offen sc1\n"
    "v_mfma_f32_16x16x32_bf16 v[4:7], v[132:135], v[64:67], 0\n"
    "ds_read_b128 v[132:135], v225 offset:4096\n"
    "v_pk_fma_f32 v[112:113], v[242:243], v[32:33], v[48:49] op_sel_hi:[0,1,1]\n"
    "v_pk_fma_f32 v[32:33], v[242:243], v[48:49], v[32:33] op_sel_hi:[0,1,1] neg_lo:[1,0,0] neg_hi:[1,0,0]\n"
    "v_mfma_f32_16x16x32_bf16 v[8:11], v[136:139], v[64:67], 0\n"
    "ds_read_b128 v[136:139], v225 offset:8192\n"
    "v_pk_fma_f32 v[114:115], v[242:243], v[34:35], v[50:51] op_sel_hi:[0,1,1]\n"
    "v_pk_fma_f32 v[34:35], v[242:243], v[50:51], v[34:35] op_sel_hi:[0,1,1] neg_lo:[1,0,0] neg_hi:[1,0,0]\n"
    "v_perm_b32 v212, v180, v176, s43\n"
    "v_perm_b32 v213, v188, v184, s43\n"
    "v_mfma_f32_16x16x32_bf16 v[12:15], v[140:143], v[64:67], 0\n"
    "ds_read_b128 v[140:143], v225 offset:12288\n"
    "v_pk_fma_f32 v[116:117], v[242:243], v[36:37], v[52:53] op_sel_hi:[0,1,1]\n"
    "v_pk_fma_f32 v[36:37], v[242:243], v[52:53], v[36:37] op_sel_hi:[0,1,1] neg_lo:[1,0,0] neg_hi:[1,0,0]\n"
    "v_perm_b32 v214, v196, v192, s43\n"
    "v_perm_b32 v215, v204, v200, s43\n"
    "s_waitcnt lgkmcnt(7)\n"
    "v_mfma_f32_16x16x32_bf16 v[0:3], v[144:147], v[80:83], v[0:3]\n"
    "ds_read_b128 v[144:147], v225 offset:16384\n"
    "v_pk_fma_f32 v[118:119], v[242:243], v[38:39], v[54:55] op_sel_hi:[0,1,1]\n"
    "v_pk_fma_f32 v[38:39], v[242:243], v[54:55], v[38:39] op_sel_hi:[0,1,1] neg_lo:[1,0,0] neg_hi:[1,0,0]\n"
    "s_add_u32 s62, s39, 8192\n"
    "buffer_store_dwordx4 v[212:215], v231, s[20:23], s62 offen sc1\n"
    "s_waitcnt lgkmcnt(7)\n"
    "v_mfma_f32_16x16x32_bf16 v[4:7], v[148:151], v[80:83], v[4:7]\n"
    "ds_read_b128 v[148:151], v225 offset:20480\n"
    "v_pk_fma_f32 v[120:121], v[242:243], v[40:41], v[56:57] op_sel_hi:[0,1,1]\n"
    "v_pk_fma_f32 v[40:41], v[242:243], v[56:57], v[40:41] op_sel_hi:[0,1,1] neg_lo:[1,0,0] neg_hi:[1,0,0]\n"
    "s_waitcnt lgkmcnt(7)\n"
    "v_mfma_f32_16x16x32_bf16 v[8:11], v[152:155], v[80:83], v[8:11]\n"
    "ds_read_b128 v[152:155], v225 offset:24576\n"
    "v_pk_fma_f32 v[122:123], v[242:243], v[42:43], v[58:59] op_sel_hi:[0,1,1]\n"
    "v_pk_fma_f32 v[42:43], v[242:243], v[58:59], v[42:43] op_sel_hi:[0,1,1] neg_lo:[1,0,0] neg_hi:[1,0,0]\n"
    "v_perm_b32 v216, v181, v177, s42\n"
    "v_perm_b32 v217, v189, v185, s42\n"
    "s_waitcnt lgkmcnt(7)\n"
    "v_mfma_f32_16x16x32_bf16 v[12:15], v[156:159], v[80:83], v[12:15]\n"
    "ds_read_b128 v[156:159], v225 offset:28672\n"
    "v_pk_fma_f32 v[124:125], v[242:243], v[44:45], v[60:61] op_sel_hi:[0,1,1]\n"
    "v_pk_fma_f32 v[44:45], v[242:243], v[60:61], v[44:45] op_sel_hi:[0,1,1] neg_lo:[1,0,0] neg_hi:[1,0,0]\n"
    "v_perm_b32 v218, v197, v193, s42\n"
    "v_perm_b32 v219, v205, v201, s42\n"
    "s_waitcnt lgkmcnt(7)\n"
    "v_mfma_f32_16x16x32_bf16 v[0:3], v[128:131], v[68:71], v[0:3]\n"
    "ds_read_b128 v[128:131], v226\n"
    "v_pk_fma_f32 v[126:127], v[242:243], v[46:47], v[62:63] op_sel_hi:[0,1,1]\n"
    "v_pk_fma_f32 v[46:47], v[242:243], v[62:63], v[46:47] op_sel_hi:[0,1,1] neg_lo:[1,0,0] neg_hi:[1,0,0]\n"
    "s_add_u32 s62, s39, 16384\n"
    "buffer_store_dwordx4 v[216:219], v231, s[20:23], s62 offen sc1\n"
    "s_waitcnt lgkmcnt(7)\n"
    "v_mfma_f32_16x16x32_bf16 v[4:7], v[132:135], v[68:71], v[4:7]\n"
    "ds_read_b128 v[132:135], v226 offset:4096\n"
    "v_cvt_pk_bf16_f32 v48, v112, v113\n"
    "v_cvt_pk_bf16_f32 v49, v114, v115\n"
    "s_waitcnt lgkmcnt(7)\n"
    "v_mfma_f32_16x16x32_bf16 v[8:11], v[136:139], v[68:71], v[8:11]\n"
    "ds_read_b128 v[136:139], v226 offset:8192\n"
    "v_cvt_pk_bf16_f32 v50, v116, v117\n"
    "v_cvt_pk_bf16_f32 v51, v118, v119\n"
    "ds_write_b128 v234, v[48:51]\n"
    "v_perm_b32 v220, v181, v177, s43\n"
    "v_perm_b32 v221, v189, v185, s43\n"
    "s_waitcnt lgkmcnt(8)\n"
    "v_mfma_f32_16x16x32_bf16 v[12:15], v[140:143], v[68:71], v[12:15]\n"
    "ds_read_b128 v[140:143], v226 offset:12288\n"
    "v_cvt_pk_bf16_f32 v52, v120, v121\n"
    "v_cvt_pk_bf16_f32 v53, v122, v123\n"
    "v_perm_b32 v222, v197, v193, s43\n"
    "v_perm_b32 v223, v205, v201, s43\n"
    "s_waitcnt lgkmcnt(8)\n"
    "v_mfma_f32_16x16x32_bf16 v[0:3], v[144:147], v[84:87], v[0:3]\n"
    "ds_read_b128 v[144:147], v226 offset:16384\n"
    "v_cvt_pk_bf16_f32 v54, v124, v125\n"
    "v_cvt_pk_bf16_f32 v55, v126, v127\n"
    "ds_write_b128 v235, v[52:55]\n"
    "s_add_u32 s62, s39, 24576\n"
    "buffer_store_dwordx4 v[220:223], v231, s[20:23], s62 offen sc1\n"
    "s_waitcnt lgkmcnt(9)\n"
    "v_mfma_f32_16x16x32_bf16 v[4:7], v[148:151], v[84:87], v[4:7]\n"
    "ds_read_b128 v[148:151], v226 offset:20480\n"
    "ds_read_b128 v[112:115], v236\n"
    "ds_read_b128 v[116:119], v236 offset:1024\n"
    "s_waitcnt lgkmcnt(11)\n"
    "v_mfma_f32_16x16x32_bf16 v[8:11], v[152:155], v[84:87], v[8:11]\n"
    "ds_read_b128 v[152:155], v226 offset:24576\n"
    "v_cvt_pk_bf16_f32 v56, v32, v33\n"
    "v_cvt_pk_bf16_f32 v57, v34, v35\n"
    "v_perm_b32 v208, v182, v178, s42\n"
    "v_perm_b32 v209, v190, v186, s42\n"
    "s_waitcnt lgkmcnt(11)\n"
    "v_mfma_f32_16x16x32_bf16 v[12:15], v[156:159], v[84:87], v[12:15]\n"
    "ds_read_b128 v[156:159], v226 offset:28672\n"
    "v_cvt_pk_bf16_f32 v58, v36, v37\n"
    "v_cvt_pk_bf16_f32 v59, v38, v39\n"
    "ds_write_b128 v234, v[56:59] offset:2048\n"
    "v_perm_b32 v210, v198, v194, s42\n"
    "v_perm_b32 v211, v206, v202, s42\n"
    "s_waitcnt lgkmcnt(12)\n"
    "v_mfma_f32_16x16x32_bf16 v[16:19], v[128:131], v[72:75], 0\n"
    "ds_read_b128 v[128:131], v227\n"
    "v_cvt_pk_bf16_f32 v60, v40, v41\n"
    "v_cvt_pk_bf16_f32 v61, v42, v43\n"
    "s_add_u32 s62, s39, 32896\n"
    "buffer_store_dwordx4 v[208:211], v231, s[20:23], s62 offen sc1\n"
    "s_waitcnt lgkmcnt(12)\n"
    "v_mfma_f32_16x16x32_bf16 v[20:23], v[132:135], v[72:75], 0\n"
    "ds_read_b128 v[132:135], v227 offset:4096\n"
    "v_cvt_pk_bf16_f32 v62, v44, v45\n"
    "v_cvt_pk_bf16_f32 v63, v46, v47\n"
    "ds_write_b128 v235, v[60:63] offset:2048\n"
    "s_add_u32 s62, s45, 0\n"
    "s_waitcnt lgkmcnt(7)\n"
    "buffer_store_dwordx4 v[112:115], v233, s[20:23], s62 offen sc1\n"
    "v_mfma_f32_16x16x32_bf16 v[24:27], v[136:139], v[72:75], 0\n"
    "ds_read_b128 v[136:139], v227 offset:8192\n"
    "ds_read_b128 v[120:123], v236 offset:2048\n"
    "ds_read_b128 v[124:127], v236 offset:3072\n"
    "s_add_u32 s62, s45, 0\n"
    "s_waitcnt lgkmcnt(9)\n"
    "buffer_store_dwordx4 v[116:119], v237, s[20:23], s62 offen sc1\n"
    "v_perm_b32 v212, v182, v178, s43\n"
    "v_perm_b32 v213, v190, v186, s43\n"
    "v_mfma_f32_16x16x32_bf16 v[28:31], v[140:143], v[72:75], 0\n"
    "ds_read_b128 v[140:143], v227 offset:12288\n"
    "v_perm_b32 v214, v198, v194, s43\n"
    "v_perm_b32 v215, v206, v202, s43\n"
    "v_mfma_f32_16x16x32_bf16 v[16:19], v[144:147], v[88:91], v[16:19]\n"
    "ds_read_b128 v[144:147], v227 offset:16384\n"
    "s_add_u32 s62, s46, 0\n"
    "s_waitcnt lgkmcnt(3)\n"
    "buffer_store_dwordx4 v[120:123], v239, s[20:23], s62 offen sc1\n"
    "s_add_u32 s62, s39, 41088\n"
    "buffer_store_dwordx4 v[212:215], v231, s[20:23], s62 offen sc1\n"
    "v_mfma_f32_16x16x32_bf16 v[20:23], v[148:151], v[88:91], v[20:23]\n"
    "ds_read_b128 v[148:151], v227 offset:20480\n"
    "s_add_u32 s62, s46, 0\n"
    "s_waitcnt lgkmcnt(3)\n"
    "buffer_store_dwordx4 v[124:127], v241, s[20:23], s62 offen sc1\n"
    "v_mfma_f32_16x16x32_bf16 v[24:27], v[152:155], v[88:91], v[24:27]\n"
    "ds_read_b128 v[152:155], v227 offset:24576\n"
    "v_perm_b32 v216, v183, v179, s42\n"
    "v_perm_b32 v217, v191, v187, s42\n"
    "v_mfma_f32_16x16x32_bf16 v[28:31], v[156:159], v[88:91], v[28:31]\n"
    "ds_read_b128 v[156:159], v227 offset:28672\n"
    "v_perm_b32 v218, v199, v195, s42\n"
    "v_perm_b32 v219, v207, v203, s42\n"
    "v_mfma_f32_16x16x32_bf16 v[16:19], v[128:131], v[76:79], v[16:19]\n"
    "s_add_u32 s62, s39, 49280\n"
    "buffer_store_dwordx4 v[216:219], v231, s[20:23], s62 offen sc1\n"
    "v_mfma_f32_16x16x32_bf16 v[20:23], v[132:135], v[76:79], v[20:23]\n"
    "v_mfma_f32_16x16x32_bf16 v[24:27], v[136:139], v[76:79], v[24:27]\n"
    "v_perm_b32 v220, v183, v179, s43\n"
    "v_perm_b32 v221, v191, v187, s43\n"
    "s_waitcnt lgkmcnt(4)\n"
    "v_mfma_f32_16x16x32_bf16 v[28:31], v[140:143], v[76:79], v[28:31]\n"
    "v_perm_b32 v222, v199, v195, s43\n"
    "v_perm_b32 v223, v207, v203, s43\n"
    "s_waitcnt lgkmcnt(3)\n"
    "v_mfma_f32_16x16x32_bf16 v[16:19], v[144:147], v[92:95], v[16:19]\n"
    "s_add_u32 s62, s39, 57472\n"
    "buffer_store_dwordx4 v[220:223], v231, s[20:23], s62 offen sc1\n"
    "s_waitcnt lgkmcnt(2)\n"
    "v_mfma_f32_16x16x32_bf16 v[20:23], v[148:151], v[92:95], v[20:23]\n"
    "s_waitcnt lgkmcnt(1)\n"
    "v_mfma_f32_16x16x32_bf16 v[24:27], v[152:155], v[92:95], v[24:27]\n"
    "s_waitcnt lgkmcnt(0)\n"
    "v_mfma_f32_16x16x32_bf16 v[28:31], v[156:159], v[92:95], v[28:31]\n"
    "v_pk_fma_f32 v[160:161], v[240:241], v[0:1], v[16:17] op_sel_hi:[0,1,1]\n"
    "v_pk_fma_f32 v[0:1], v[240:241], v[16:17], v[0:1] op_sel_hi:[0,1,1] neg_lo:[1,0,0] neg_hi:[1,0,0]\n"
    "v_pk_fma_f32 v[162:163], v[240:241], v[2:3], v[18:19] op_sel_hi:[0,1,1]\n"
    "v_pk_fma_f32 v[2:3], v[240:241], v[18:19], v[2:3] op_sel_hi:[0,1,1] neg_lo:[1,0,0] neg_hi:[1,0,0]\n"
    "v_pk_fma_f32 v[164:165], v[240:241], v[4:5], v[20:21] op_sel_hi:[0,1,1]\n"
    "v_pk_fma_f32 v[4:5], v[240:241], v[20:21], v[4:5] op_sel_hi:[0,1,1] neg_lo:[1,0,0] neg_hi:[1,0,0]\n"
    "v_pk_fma_f32 v[166:167], v[240:241], v[6:7], v[22:23] op_sel_hi:[0,1,1]\n"
    "v_pk_fma_f32 v[6:7], v[240:241], v[22:23], v[6:7] op_sel_hi:[0,1,1] neg_lo:[1,0,0] neg_hi:[1,0,0]\n"
    "v_pk_fma_f32 v[168:169], v[240:241], v[8:9], v[24:25] op_sel_hi:[0,1,1]\n"
    "v_pk_fma_f32 v[8:9], v[240:241], v[24:25], v[8:9] op_sel_hi:[0,1,1] neg_lo:[1,0,0] neg_hi:[1,0,0]\n"
    "v_pk_fma_f32 v[170:171], v[240:241], v[10:11], v[26:27] op_sel_hi:[0,1,1]\n"
    "v_pk_fma_f32 v[10:11], v[240:241], v[26:27], v[10:11] op_sel_hi:[0,1,1] neg_lo:[1,0,0] neg_hi:[1,0,0]\n"
    "v_pk_fma_f32 v[172:173], v[240:241], v[12:13], v[28:29] op_sel_hi:[0,1,1]\n"
    "v_pk_fma_f32 v[12:13], v[240:241], v[28:29], v[12:13] op_sel_hi:[0,1,1] neg_lo:[1,0,0] neg_hi:[1,0,0]\n"
    "v_pk_fma_f32 v[174:175], v[240:241], v[14:15], v[30:31] op_sel_hi:[0,1,1]\n"
    "v_pk_fma_f32 v[14:15], v[240:241], v[30:31], v[14:15] op_sel_hi:[0,1,1] neg_lo:[1,0,0] neg_hi:[1,0,0]\n"
    "v_cvt_pk_bf16_f32 v16, v160, v161\n"
    "v_cvt_pk_bf16_f32 v17, v162, v163\n"
    "v_cvt_pk_bf16_f32 v18, v164, v165\n"
    "v_cvt_pk_bf16_f32 v19, v166, v167\n"
    "ds_write_b128 v234, v[16:19]\n"
    "v_cvt_pk_bf16_f32 v20, v168, v169\n"
    "v_cvt_pk_bf16_f32 v21, v170, v171\n"
    "v_cvt_pk_bf16_f32 v22, v172, v173\n"
    "v_cvt_pk_bf16_f32 v23, v174, v175\n"
    "ds_write_b128 v235, v[20:23]\n"
    "ds_read_b128 v[160:163], v236\n"
    "ds_read_b128 v[164:167], v236 offset:1024\n"
    "v_cvt_pk_bf16_f32 v24, v0, v1\n"
    "v_cvt_pk_bf16_f32 v25, v2, v3\n"
    "v_cvt_pk_bf16_f32 v26, v4, v5\n"
    "v_cvt_pk_bf16_f32 v27, v6, v7\n"
    "ds_write_b128 v234, v[24:27] offset:2048\n"
    "v_cvt_pk_bf16_f32 v28, v8, v9\n"
    "v_cvt_pk_bf16_f32 v29, v10, v11\n"
    "v_cvt_pk_bf16_f32 v30, v12, v13\n"
    "v_cvt_pk_bf16_f32 v31, v14, v15\n"
    "ds_write_b128 v235, v[28:31] offset:2048\n"
    "ds_read_b128 v[168:171], v236 offset:2048\n"
    "ds_read_b128 v[172:175], v236 offset:3072\n"
    "s_add_u32 s62, s38, 8192\n"
    "s_waitcnt lgkmcnt(5)\n"
    "buffer_store_dwordx4 v[160:163], v233, s[20:23], s62 offen sc1\n"
    "s_add_u32 s62, s38, 12288\n"
    "s_waitcnt lgkmcnt(4)\n"
    "buffer_store_dwordx4 v[164:167], v233, s[20:23], s62 offen sc1\n"
    "s_add_u32 s62, s44, 0\n"
    "s_waitcnt lgkmcnt(1)\n"
    "buffer_store_dwordx4 v[168:171], v232, s[20:23], s62 offen sc1\n"
    "s_add_u32 s62, s44, 4096\n"
    "s_waitcnt lgkmcnt(0)\n"
    "buffer_store_dwordx4 v[172:175], v232, s[20:23], s62 offen sc1\n"
    "s_cmp_lg_u32 s32, 0\n"
    "s_cbranch_scc1 Ldft2\n"
    "Ldc2:\n"
    "s_waitcnt vmcnt(18)\n"
    "v_lshl_or_b32 v244, v245, 16, v244\n"
    "s_waitcnt vmcnt(16)\n"
    "v_lshl_or_b32 v246, v247, 16, v246\n"
    "v_mov_b32 v245, v244\n"
    "v_mov_b32 v247, v246\n"
    "s_nop 0\n"
    "v_permlane16_swap_b32 v244, v245\n"
    "v_permlane16_swap_b32 v246, v247\n"
    "s_add_u32 s62, s39, 2105344\n"
    "s_mov_b64 exec, 0xffff\n"
    "buffer_store_dwordx4 v[244:247], v250, s[20:23], s62 offen sc1\n"
    "s_mov_b64 exec, -1\n"
    "Ldft2:\n"
    "s_and_b32 s60, s32, 3\n"
    "s_lshr_b32 s61, s32, 2\n"
    "s_and_b32 s3, s34, 3\n"
    "s_lshl2_add_u32 s55, s33, s3\n"
    "s_lshl_b32 s43, s60, 12\n"
    "s_lshl_b32 s3, s61, 11\n"
    "s_add_u32 s43, s43, s3\n"
    "s_lshl_b32 s28, s34, 5\n"
    "s_or_b32 s28, s28, 0x60\n"
    "s_sub_u32 s28, s28, s16\n"
    "s_xor_b32 s28, s28, 0x200\n"
    "s_and_b32 s28, s28, 0x380\n"
    "s_lshr_b32 s31, s28, 1\n"
    "s_lshl_b32 s57, s55, 10\n"
    "s_add_u32 s57, s57, s28\n"
    "s_xor_b32 s3, s61, s55\n"
    "s_and_b32 s3, s3, 1\n"
    "s_lshl_b32 s3, s3, 14\n"
    "s_add_u32 s28, s28, s3\n"
    "s_mul_i32 s3, s60, 229376\n"
    "s_add_u32 s28, s28, s3\n"
    "v_lshrrev_b32 v2, 8, v250\n"
    "v_bfe_u32 v3, v250, 5, 2\n"
    "v_xor_b32 v3, v3, v2\n"
    "v_and_b32 v4, 16, v250\n"
    "v_lshl_or_b32 v3, v3, 5, v4\n"
    "v_lshrrev_b32 v4, 7, v250\n"
    "v_lshl_or_b32 v3, v4, 10, v3\n"
    "v_add_u32 v248, s28, v3\n"
    "v_add_u32 v249, 7168, v248\n"
    "s_cmp_eq_u32 s33, 7\n"
    "s_cselect_b32 s3, 0, s33\n"
    "s_lshl_b32 s44, s3, 12\n"
    "s_add_u32 s45, s44, 4096\n"
    "s_cmp_eq_u32 s45, 28672\n"
    "s_cselect_b32 s45, 0, s45\n"
    "s_add_u32 s46, s45, 4096\n"
    "s_cmp_eq_u32 s46, 28672\n"
    "s_cselect_b32 s46, 0, s46\n"
    "s_add_u32 s47, s46, 4096\n"
    "s_cmp_eq_u32 s47, 28672\n"
    "s_cselect_b32 s47, 0, s47\n"
    "s_add_u32 s48, s47, 4096\n"
    "s_cmp_eq_u32 s48, 28672\n"
    "s_cselect_b32 s48, 0, s48\n"
    "s_add_u32 s49, s48, 4096\n"
    "s_cmp_eq_u32 s49, 28672\n"
    "s_cselect_b32 s49, 0, s49\n"
    "s_add_u32 s50, s49, 4096\n"
    "s_cmp_eq_u32 s50, 28672\n"
    "s_cselect_b32 s50, 0, s50\n"
    "v_lshrrev_b32 v3, 6, v250\n"
    "v_bfe_u32 v4, v250, 7, 2\n"
    "v_lshlrev_b32 v4, 5, v4\n"
    "v_lshl_or_b32 v3, v3, 7, v4\n"
    "v_bfe_u32 v4, v250, 4, 2\n"
    "v_lshl_or_b32 v3, v4, 3, v3\n"
    "s_lshl_b32 s3, s60, 12\n"
    "v_add_u32 v236, s3, v3\n"
    "v_xor_b32 v237, 32, v236\n"
    "v_xor_b32 v238, 64, v236\n"
    "v_xor_b32 v239, 0x60, v236\n"
    "s_add_u32 s58, s2, 8\n"
    "s_and_b32 s58, s58, 255\n"
    "s_lshl_b32 s53, s58, 13\n"
    "s_lshr_b32 s3, s58, 2\n"
    "s_mul_i32 s3, s3, 128\n"
    "s_add_u32 s53, s53, s3\n"
    "s_lshl_b32 s3, s60, 11\n"
    "s_add_u32 s53, s53, s3\n"
    "s_add_u32 s53, s53, 1048576\n"
    "s_mul_i32 s56, s55, 56\n"
    "s_mul_i32 s3, s60, 7\n"
    "s_add_u32 s56, s56, s3\n"
    "s_mov_b32 s59, 0\n"
    "v_mov_b32 v208, s36\n"
    "v_mov_b32 v209, s37\n"
    "s_lshl_b32 s3, s32, 5\n"
    "s_add_u32 s3, s3, s30\n"
    "s_lshl_b32 s3, s3, 3\n"
    "s_add_u32 s3, s3, 32768\n"
    "s_mov_b32 s52, 2130706432\n"
    "v_mov_b32 v2, 0\n"
    "v_bfe_u32 v3, v250, 2, 6\n"
    "v_lshrrev_b32 v4, 8, v250\n"
    "v_lshl_or_b32 v3, v4, 12, v3\n"
    "s_waitcnt vmcnt(0)\n"
    "s_barrier\n"
    "s_mov_b64 exec, 1\n"
    "buffer_store_dwordx2 v[208:209], off, s[20:23], s3 sc1\n"
    "s_mov_b64 exec, -1\n"
    "ds_write_b32 v3, v2 offset:3584\n"
    "v_and_b32 v251, 0x1f0, v250\n"
    "v_lshrrev_b32 v251, 1, v251\n"
    "s_lshr_b32 s54, s58, 5\n"
    "s_lshl_b32 s54, s54, 8\n"
    "s_add_u32 s54, s54, 32768\n"
    "s_cmp_ge_u32 s32, 4\n"
    "s_cbranch_scc1 Ldg\n"
    "Ldf:\n"
    "s_mov_b32 s63, 0x36000000\n"
    "s_and_b32 s3, s32, 3\n"
    "s_lshl_b32 s3, s3, 4\n"
    "v_bfe_u32 v2, v250, 4, 4\n"
    "v_lshrrev_b32 v3, 6, v250\n"
    "s_lshl_b32 s62, s44, 3\n"
    "s_add_u32 m0, s43, 147456\n"
    "v_and_b32 v3, 12, v3\n"
    "buffer_load_dwordx4 v248, s[16:19], s62 offen lds\n"
    "v_add_u32 v4, s3, v2\n"
    "v_add_u32 v5, s3, v3\n"
    "v_cvt_f32_u32 v2, v2\n"
    "v_cvt_f32_u32 v10, v3\n"
    "v_cvt_f32_u32 v4, v4\n"
    "v_cvt_f32_u32 v42, v5\n"
    "s_cmp_eq_u32 s58, 0\n"
    "s_cselect_b32 s3, 0x35800000, s63\n"
    "v_mul_f32 v6, 0x3c800000, v4\n"
    "v_mul_f32 v7, 0x3a000000, v4\n"
    "v_mul_f32 v8, 0x3d000000, v2\n"
    "v_mul_f32 v4, 0x3a000000, v2\n"
    "buffer_load_dwordx4 v249, s[16:19], s62 offen offset:1024 lds\n"
    "v_add_f32 v5, 0x3c000000, v4\n"
    "v_mov_b32 v9, s3\n"
    "v_add_f32 v11, 0x3f800000, v10\n"
    "v_add_f32 v12, 0x40000000, v10\n"
    "v_add_f32 v13, 0x40400000, v10\n"
    "v_add_f32 v14, 0x41800000, v10\n"
    "v_add_f32 v15, 0x41880000, v10\n"
    "v_add_f32 v16, 0x41900000, v10\n"
    "v_add_f32 v17, 0x41980000, v10\n"
    "v_add_f32 v43, 0x3f800000, v42\n"
    "s_lshl_b32 s62, s45, 3\n"
    "s_add_u32 m0, s43, 16384\n"
    "v_add_f32 v44, 0x40000000, v42\n"
    "buffer_load_dwordx4 v248, s[16:19], s62 offen lds\n"
    "v_add_f32 v45, 0x40400000, v42\n"
    "v_pk_mul_f32 v[18:19], v[10:11], v[6:7] op_sel_hi:[1,0]\n"
    "v_pk_mul_f32 v[20:21], v[12:13], v[6:7] op_sel_hi:[1,0]\n"
    "v_pk_mul_f32 v[22:23], v[14:15], v[6:7] op_sel_hi:[1,0]\n"
    "v_pk_mul_f32 v[24:25], v[16:17], v[6:7] op_sel_hi:[1,0]\n"
    "v_cos_f32 v26, v18\n"
    "v_sin_f32 v34, v18\n"
    "v_cos_f32 v27, v19\n"
    "v_sin_f32 v35, v19\n"
    "v_cos_f32 v28, v20\n"
    "v_sin_f32 v36, v20\n"
    "buffer_load_dwordx4 v249, s[16:19], s62 offen offset:1024 lds\n"
    "v_cos_f32 v29, v21\n"
    "v_sin_f32 v37, v21\n"
    "v_cos_f32 v30, v22\n"
    "v_sin_f32 v38, v22\n"
    "v_cos_f32 v31, v23\n"
    "v_sin_f32 v39, v23\n"
    "v_cos_f32 v32, v24\n"
    "v_sin_f32 v40, v24\n"
    "v_cos_f32 v33, v25\n"
    "v_sin_f32 v41, v25\n"
    "s_lshl_b32 s62, s46, 3\n"
    "s_add_u32 m0, s43, 32768\n"
    "v_cvt_pk_bf16_f32 v200, v26, v27\n"
    "buffer_load_dwordx4 v248, s[16:19], s62 offen lds\n"
    "v_cvt_pk_bf16_f32 v201, v28, v29\n"
    "v_cvt_pk_bf16_f32 v202, v30, v31\n"
    "v_cvt_pk_bf16_f32 v203, v32, v33\n"
    "v_cvt_pk_bf16_f32 v204, v34, v35\n"
    "v_cvt_pk_bf16_f32 v205, v36, v37\n"
    "v_cvt_pk_bf16_f32 v206, v38, v39\n"
    "v_cvt_pk_bf16_f32 v207, v40, v41\n"
    "v_pk_mul_f32 v[18:19], v[10:11], v[8:9] op_sel_hi:[1,0]\n"
    "v_pk_mul_f32 v[20:21], v[12:13], v[8:9] op_sel_hi:[1,0]\n"
    "v_pk_mul_f32 v[22:23], v[14:15], v[8:9] op_sel_hi:[1,0]\n"
    "v_pk_mul_f32 v[24:25], v[16:17], v[8:9] op_sel_hi:[1,0]\n"
    "buffer_load_dwordx4 v249, s[16:19], s62 offen offset:1024 lds\n"
    "v_cos_f32 v26, v18\n"
    "v_sin_f32 v34, v18\n"
    "v_cos_f32 v27, v19\n"
    "v_sin_f32 v35, v19\n"
    "v_cos_f32 v28, v20\n"
    "v_sin_f32 v36, v20\n"
    "v_cos_f32 v29, v21\n"
    "v_sin_f32 v37, v21\n"
    "v_cos_f32 v30, v22\n"
    "v_sin_f32 v38, v22\n"
    "v_cos_f32 v31, v23\n"
    "s_lshl_b32 s62, s47, 3\n"
    "s_add_u32 m0, s43, 49152\n"
    "v_sin_f32 v39, v23\n"
    "buffer_load_dwordx4 v248, s[16:19], s62 offen lds\n"
    "v_cos_f32 v32, v24\n"
    "v_sin_f32 v40, v24\n"
    "v_cos_f32 v33, v25\n"
    "v_sin_f32 v41, v25\n"
    "v_cvt_pk_bf16_f32 v160, v26, v27\n"
    "v_cvt_pk_bf16_f32 v161, v28, v29\n"
    "v_cvt_pk_bf16_f32 v162, v30, v31\n"
    "v_cvt_pk_bf16_f32 v163, v32, v33\n"
    "v_cvt_pk_bf16_f32 v164, v34, v35\n"
    "v_cvt_pk_bf16_f32 v165, v36, v37\n"
    "v_cvt_pk_bf16_f32 v166, v38, v39\n"
    "buffer_load_dwordx4 v249, s[16:19], s62 offen offset:1024 lds\n"
    "v_cvt_pk_bf16_f32 v167, v40, v41\n"
    "v_pk_mul_f32 v[18:19], v[6:7], v[10:11] op_sel:[1,0]\n"
    "v_pk_mul_f32 v[20:21], v[6:7], v[12:13] op_sel:[1,0]\n"
    "v_pk_mul_f32 v[22:23], v[6:7], v[14:15] op_sel:[1,0]\n"
    "v_pk_mul_f32 v[24:25], v[6:7], v[16:17] op_sel:[1,0]\n"
    "v_cos_f32 v168, v18\n"
    "v_sin_f32 v176, v18\n"
    "v_cos_f32 v169, v19\n"
    "v_sin_f32 v177, v19\n"
    "v_cos_f32 v170, v20\n"
    "s_lshl3_add_u32 s62, s44, 917504\n"
    "s_add_u32 m0, s43, 131072\n"
    "v_sin_f32 v178, v20\n"
    "buffer_load_dwordx4 v248, s[16:19], s62 offen lds\n"
    "v_cos_f32 v171, v21\n"
    "v_sin_f32 v179, v21\n"
    "v_cos_f32 v172, v22\n"
    "v_sin_f32 v180, v22\n"
    "v_cos_f32 v173, v23\n"
    "v_sin_f32 v181, v23\n"
    "v_cos_f32 v174, v24\n"
    "v_sin_f32 v182, v24\n"
    "v_cos_f32 v175, v25\n"
    "v_sin_f32 v183, v25\n"
    "v_pk_mul_f32 v[18:19], v[42:43], v[4:5] op_sel_hi:[1,0]\n"
    "buffer_load_dwordx4 v249, s[16:19], s62 offen offset:1024 lds\n"
    "v_pk_mul_f32 v[20:21], v[44:45], v[4:5] op_sel_hi:[1,0]\n"
    "v_pk_mul_f32 v[22:23], v[4:5], v[42:43] op_sel:[1,0]\n"
    "v_pk_mul_f32 v[24:25], v[4:5], v[44:45] op_sel:[1,0]\n"
    "v_cos_f32 v184, v18\n"
    "v_sin_f32 v192, v18\n"
    "v_cos_f32 v185, v19\n"
    "v_sin_f32 v193, v19\n"
    "v_cos_f32 v186, v20\n"
    "v_sin_f32 v194, v20\n"
    "v_cos_f32 v187, v21\n"
    "s_lshl_b32 s62, s48, 3\n"
    "s_add_u32 m0, s43, 65536\n"
    "v_sin_f32 v195, v21\n"
    "buffer_load_dwordx4 v248, s[16:19], s62 offen lds\n"
    "v_cos_f32 v188, v22\n"
    "v_sin_f32 v196, v22\n"
    "v_cos_f32 v189, v23\n"
    "v_sin_f32 v197, v23\n"
    "v_cos_f32 v190, v24\n"
    "v_sin_f32 v198, v24\n"
    "v_cos_f32 v191, v25\n"
    "v_sin_f32 v199, v25\n"
    "v_pk_mul_f32 v[184:185], v[8:9], v[184:185] op_sel:[1,0]\n"
    "v_pk_mul_f32 v[186:187], v[8:9], v[186:187] op_sel:[1,0]\n"
    "v_pk_mul_f32 v[188:189], v[8:9], v[188:189] op_sel:[1,0]\n"
    "buffer_load_dwordx4 v249, s[16:19], s62 offen offset:1024 lds\n"
    "v_pk_mul_f32 v[190:191], v[8:9], v[190:191] op_sel:[1,0]\n"
    "v_pk_mul_f32 v[192:193], v[8:9], v[192:193] op_sel:[1,0]\n"
    "v_pk_mul_f32 v[194:195], v[8:9], v[194:195] op_sel:[1,0]\n"
    "v_pk_mul_f32 v[196:197], v[8:9], v[196:197] op_sel:[1,0]\n"
    "v_pk_mul_f32 v[198:199], v[8:9], v[198:199] op_sel:[1,0]\n"
    "s_branch Ldft3\n"
    "Ldg:\n"
    "s_and_b32 s60, s32, 3\n"
    "s_lshl_b32 s3, s60, 6\n"
    "v_lshrrev_b32 v2, 4, v250\n"
    "v_and_b32 v3, 48, v2\n"
    "v_add_u32 v3, s3, v3\n"
    "v_and_b32 v2, 15, v2\n"
    "v_add_u32 v2, s31, v2\n"
    "s_lshl_b32 s62, s44, 3\n"
    "s_add_u32 m0, s43, 147456\n"
    "v_cvt_f32_u32 v3, v3\n"
    "buffer_load_dwordx4 v248, s[16:19], s62 offen lds\n"
    "v_cvt_f32_u32 v2, v2\n"
    "v_lshlrev_b32 v4, 27, v250\n"
    "v_xor_b32 v4, 0x3f803f80, v4\n"
    "s_cmp_eq_u32 s60, 0\n"
    "s_cselect_b32 s28, 0xffff, 0\n"
    "s_mov_b32 s29, 0\n"
    "v_mul_f32 v2, 0x3b000000, v2\n"
    "v_add_f32 v10, 0x00000000, v2\n"
    "v_add_f32 v18, 0x3d000000, v2\n"
    "v_add_f32 v26, 0x3d800000, v2\n"
    "v_add_f32 v34, 0x3dc00000, v2\n"
    "v_mul_f32 v11, v10, v3\n"
    "v_mul_f32 v19, v18, v3\n"
    "v_mul_f32 v27, v26, v3\n"
    "v_mul_f32 v35, v34, v3\n"
    "v_cos_f32 v12, v10\n"
    "buffer_load_dwordx4 v249, s[16:19], s62 offen offset:1024 lds\n"
    "v_cos_f32 v20, v18\n"
    "v_cos_f32 v28, v26\n"
    "v_cos_f32 v36, v34\n"
    "v_fract_f32 v11, v11\n"
    "v_fract_f32 v19, v19\n"
    "v_fract_f32 v27, v27\n"
    "v_fract_f32 v35, v35\n"
    "v_add_f32 v10, v10, v11\n"
    "v_add_f32 v18, v18, v19\n"
    "v_add_f32 v26, v26, v27\n"
    "v_add_f32 v34, v34, v35\n"
    "v_cos_f32 v6, v11\n"
    "v_cos_f32 v14, v19\n"
    "v_cos_f32 v22, v27\n"
    "s_lshl_b32 s62, s45, 3\n"
    "s_add_u32 m0, s43, 16384\n"
    "v_cos_f32 v30, v35\n"
    "buffer_load_dwordx4 v248, s[16:19], s62 offen lds\n"
    "v_sin_f32_e64 v7, -v11\n"
    "v_sin_f32_e64 v15, -v19\n"
    "v_sin_f32_e64 v23, -v27\n"
    "v_sin_f32_e64 v31, -v35\n"
    "v_cos_f32 v8, v10\n"
    "v_cos_f32 v16, v18\n"
    "v_cos_f32 v24, v26\n"
    "v_cos_f32 v32, v34\n"
    "v_sin_f32_e64 v9, -v10\n"
    "v_sin_f32_e64 v17, -v18\n"
    "v_sin_f32_e64 v25, -v26\n"
    "v_sin_f32_e64 v33, -v34\n"
    "v_add_f32 v12, v12, v12\n"
    "v_add_f32 v20, v20, v20\n"
    "v_add_f32 v28, v28, v28\n"
    "buffer_load_dwordx4 v249, s[16:19], s62 offen offset:1024 lds\n"
    "v_add_f32 v36, v36, v36\n"
    "v_cvt_pk_bf16_f32 v160, v6, v7\n"
    "v_cvt_pk_bf16_f32 v176, v14, v15\n"
    "v_cvt_pk_bf16_f32 v192, v22, v23\n"
    "v_cvt_pk_bf16_f32 v96, v30, v31\n"
    "v_pk_fma_f32 v[6:7], v[12:13], v[8:9], v[6:7] op_sel_hi:[0,1,1] neg_lo:[0,0,1] neg_hi:[0,0,1]\n"
    "v_pk_fma_f32 v[14:15], v[20:21], v[16:17], v[14:15] op_sel_hi:[0,1,1] neg_lo:[0,0,1] neg_hi:[0,0,1]\n"
    "v_pk_fma_f32 v[22:23], v[28:29], v[24:25], v[22:23] op_sel_hi:[0,1,1] neg_lo:[0,0,1] neg_hi:[0,0,1]\n"
    "v_pk_fma_f32 v[30:31], v[36:37], v[32:33], v[30:31] op_sel_hi:[0,1,1] neg_lo:[0,0,1] neg_hi:[0,0,1]\n"
    "v_cvt_pk_bf16_f32 v161, v8, v9\n"
    "v_cvt_pk_bf16_f32 v177, v16, v17\n"
    "v_cvt_pk_bf16_f32 v193, v24, v25\n"
    "v_cvt_pk_bf16_f32 v97, v32, v33\n"
    "v_pk_fma_f32 v[8:9], v[12:13], v[6:7], v[8:9] op_sel_hi:[0,1,1] neg_lo:[0,0,1] neg_hi:[0,0,1]\n"
    "s_lshl_b32 s62, s46, 3\n"
    "s_add_u32 m0, s43, 32768\n"
    "v_pk_fma_f32 v[16:17], v[20:21], v[14:15], v[16:17] op_sel_hi:[0,1,1] neg_lo:[0,0,1] neg_hi:[0,0,1]\n"
    "buffer_load_dwordx4 v248, s[16:19], s62 offen lds\n"
    "v_pk_fma_f32 v[24:25], v[28:29], v[22:23], v[24:25] op_sel_hi:[0,1,1] neg_lo:[0,0,1] neg_hi:[0,0,1]\n"
    "v_pk_fma_f32 v[32:33], v[36:37], v[30:31], v[32:33] op_sel_hi:[0,1,1] neg_lo:[0,0,1] neg_hi:[0,0,1]\n"
    "v_cvt_pk_bf16_f32 v162, v6, v7\n"
    "v_cvt_pk_bf16_f32 v178, v14, v15\n"
    "v_cvt_pk_bf16_f32 v194, v22, v23\n"
    "v_cvt_pk_bf16_f32 v98, v30, v31\n"
    "v_pk_fma_f32 v[6:7], v[12:13], v[8:9], v[6:7] op_sel_hi:[0,1,1] neg_lo:[0,0,1] neg_hi:[0,0,1]\n"
    "v_pk_fma_f32 v[14:15], v[20:21], v[16:17], v[14:15] op_sel_hi:[0,1,1] neg_lo:[0,0,1] neg_hi:[0,0,1]\n"
    "v_pk_fma_f32 v[22:23], v[28:29], v[24:25], v[22:23] op_sel_hi:[0,1,1] neg_lo:[0,0,1] neg_hi:[0,0,1]\n"
    "v_pk_fma_f32 v[30:31], v[36:37], v[32:33], v[30:31] op_sel_hi:[0,1,1] neg_lo:[0,0,1] neg_hi:[0,0,1]\n"
    "v_cvt_pk_bf16_f32 v163, v8, v9\n"
    "v_cvt_pk_bf16_f32 v179, v16, v17\n"
    "v_cvt_pk_bf16_f32 v195, v24, v25\n"
    "v_cvt_pk_bf16_f32 v99, v32, v33\n"
    "v_pk_fma_f32 v[8:9], v[12:13], v[6:7], v[8:9] op_sel_hi:[0,1,1] neg_lo:[0,0,1] neg_hi:[0,0,1]\n"
    "buffer_load_dwordx4 v249, s[16:19], s62 offen offset:1024 lds\n"
    "v_pk_fma_f32 v[16:17], v[20:21], v[14:15], v[16:17] op_sel_hi:[0,1,1] neg_lo:[0,0,1] neg_hi:[0,0,1]\n"
    "v_pk_fma_f32 v[24:25], v[28:29], v[22:23], v[24:25] op_sel_hi:[0,1,1] neg_lo:[0,0,1] neg_hi:[0,0,1]\n"
    "v_pk_fma_f32 v[32:33], v[36:37], v[30:31], v[32:33] op_sel_hi:[0,1,1] neg_lo:[0,0,1] neg_hi:[0,0,1]\n"
    "v_cvt_pk_bf16_f32 v164, v6, v7\n"
    "v_cvt_pk_bf16_f32 v180, v14, v15\n"
    "v_cvt_pk_bf16_f32 v196, v22, v23\n"
    "v_cvt_pk_bf16_f32 v100, v30, v31\n"
    "v_pk_fma_f32 v[6:7], v[12:13], v[8:9], v[6:7] op_sel_hi:[0,1,1] neg_lo:[0,0,1] neg_hi:[0,0,1]\n"
    "v_pk_fma_f32 v[14:15], v[20:21], v[16:17], v[14:15] op_sel_hi:[0,1,1] neg_lo:[0,0,1] neg_hi:[0,0,1]\n"
    "v_pk_fma_f32 v[22:23], v[28:29], v[24:25], v[22:23] op_sel_hi:[0,1,1] neg_lo:[0,0,1] neg_hi:[0,0,1]\n"
    "v_pk_fma_f32 v[30:31], v[36:37], v[32:33], v[30:31] op_sel_hi:[0,1,1] neg_lo:[0,0,1] neg_hi:[0,0,1]\n"
    "v_cvt_pk_bf16_f32 v165, v8, v9\n"
    "v_cvt_pk_bf16_f32 v181, v16, v17\n"
    "s_lshl_b32 s62, s47, 3\n"
    "s_add_u32 m0, s43, 49152\n"
    "v_cvt_pk_bf16_f32 v197, v24, v25\n"
    "buffer_load_dwordx4 v248, s[16:19], s62 offen lds\n"
    "v_cvt_pk_bf16_f32 v101, v32, v33\n"
    "v_pk_fma_f32 v[8:9], v[12:13], v[6:7], v[8:9] op_sel_hi:[0,1,1] neg_lo:[0,0,1] neg_hi:[0,0,1]\n"
    "v_pk_fma_f32 v[16:17], v[20:21], v[14:15], v[16:17] op_sel_hi:[0,1,1] neg_lo:[0,0,1] neg_hi:[0,0,1]\n"
    "v_pk_fma_f32 v[24:25], v[28:29], v[22:23], v[24:25] op_sel_hi:[0,1,1] neg_lo:[0,0,1] neg_hi:[0,0,1]\n"
    "v_pk_fma_f32 v[32:33], v[36:37], v[30:31], v[32:33] op_sel_hi:[0,1,1] neg_lo:[0,0,1] neg_hi:[0,0,1]\n"
    "v_cvt_pk_bf16_f32 v166, v6, v7\n"
    "v_cvt_pk_bf16_f32 v182, v14, v15\n"
    "v_cvt_pk_bf16_f32 v198, v22, v23\n"
    "v_cvt_pk_bf16_f32 v102, v30, v31\n"
    "v_pk_fma_f32 v[6:7], v[12:13], v[8:9], v[6:7] op_sel_hi:[0,1,1] neg_lo:[0,0,1] neg_hi:[0,0,1]\n"
    "v_pk_fma_f32 v[14:15], v[20:21], v[16:17], v[14:15] op_sel_hi:[0,1,1] neg_lo:[0,0,1] neg_hi:[0,0,1]\n"
    "v_pk_fma_f32 v[22:23], v[28:29], v[24:25], v[22:23] op_sel_hi:[0,1,1] neg_lo:[0,0,1] neg_hi:[0,0,1]\n"
    "v_pk_fma_f32 v[30:31], v[36:37], v[32:33], v[30:31] op_sel_hi:[0,1,1] neg_lo:[0,0,1] neg_hi:[0,0,1]\n"
    "v_cvt_pk_bf16_f32 v167, v8, v9\n"
    "v_cvt_pk_bf16_f32 v183, v16, v17\n"
    "buffer_load_dwordx4 v249, s[16:19], s62 offen offset:1024 lds\n"
    "v_cvt_pk_bf16_f32 v199, v24, v25\n"
    "v_cvt_pk_bf16_f32 v103, v32, v33\n"
    "v_pk_fma_f32 v[8:9], v[12:13], v[6:7], v[8:9] op_sel_hi:[0,1,1] neg_lo:[0,0,1] neg_hi:[0,0,1]\n"
    "v_pk_fma_f32 v[16:17], v[20:21], v[14:15], v[16:17] op_sel_hi:[0,1,1] neg_lo:[0,0,1] neg_hi:[0,0,1]\n"
    "v_pk_fma_f32 v[24:25], v[28:29], v[22:23], v[24:25] op_sel_hi:[0,1,1] neg_lo:[0,0,1] neg_hi:[0,0,1]\n"
    "v_pk_fma_f32 v[32:33], v[36:37], v[30:31], v[32:33] op_sel_hi:[0,1,1] neg_lo:[0,0,1] neg_hi:[0,0,1]\n"
    "v_cvt_pk_bf16_f32 v168, v6, v7\n"
    "v_cvt_pk_bf16_f32 v184, v14, v15\n"
    "v_cvt_pk_bf16_f32 v200, v22, v23\n"
    "v_cvt_pk_bf16_f32 v104, v30, v31\n"
    "v_pk_fma_f32 v[6:7], v[12:13], v[8:9], v[6:7] op_sel_hi:[0,1,1] neg_lo:[0,0,1] neg_hi:[0,0,1]\n"
    "v_pk_fma_f32 v[14:15], v[20:21], v[16:17], v[14:15] op_sel_hi:[0,1,1] neg_lo:[0,0,1] neg_hi:[0,0,1]\n"
    "v_pk_fma_f32 v[22:23], v[28:29], v[24:25], v[22:23] op_sel_hi:[0,1,1] neg_lo:[0,0,1] neg_hi:[0,0,1]\n"
    "v_pk_fma_f32 v[30:31], v[36:37], v[32:33], v[30:31] op_sel_hi:[0,1,1] neg_lo:[0,0,1] neg_hi:[0,0,1]\n"
    "s_lshl3_add_u32 s62, s44, 917504\n"
    "s_add_u32 m0, s43, 131072\n"
    "v_cvt_pk_bf16_f32 v169, v8, v9\n"
    "buffer_load_dwordx4 v248, s[16:19], s62 offen lds\n"
    "v_cvt_pk_bf16_f32 v185, v16, v17\n"
    "v_cvt_pk_bf16_f32 v201, v24, v25\n"
    "v_cvt_pk_bf16_f32 v105, v32, v33\n"
    "v_pk_fma_f32 v[8:9], v[12:13], v[6:7], v[8:9] op_sel_hi:[0,1,1] neg_lo:[0,0,1] neg_hi:[0,0,1]\n"
    "v_pk_fma_f32 v[16:17], v[20:21], v[14:15], v[16:17] op_sel_hi:[0,1,1] neg_lo:[0,0,1] neg_hi:[0,0,1]\n"
    "v_pk_fma_f32 v[24:25], v[28:29], v[22:23], v[24:25] op_sel_hi:[0,1,1] neg_lo:[0,0,1] neg_hi:[0,0,1]\n"
    "v_pk_fma_f32 v[32:33], v[36:37], v[30:31], v[32:33] op_sel_hi:[0,1,1] neg_lo:[0,0,1] neg_hi:[0,0,1]\n"
    "v_cvt_pk_bf16_f32 v170, v6, v7\n"
    "v_cvt_pk_bf16_f32 v186, v14, v15\n"
    "v_cvt_pk_bf16_f32 v202, v22, v23\n"
    "v_cvt_pk_bf16_f32 v106, v30, v31\n"
    "v_pk_fma_f32 v[6:7], v[12:13], v[8:9], v[6:7] op_sel_hi:[0,1,1] neg_lo:[0,0,1] neg_hi:[0,0,1]\n"
    "v_pk_fma_f32 v[14:15], v[20:21], v[16:17], v[14:15] op_sel_hi:[0,1,1] neg_lo:[0,0,1] neg_hi:[0,0,1]\n"
    "v_pk_fma_f32 v[22:23], v[28:29], v[24:25], v[22:23] op_sel_hi:[0,1,1] neg_lo:[0,0,1] neg_hi:[0,0,1]\n"
    "v_pk_fma_f32 v[30:31], v[36:37], v[32:33], v[30:31] op_sel_hi:[0,1,1] neg_lo:[0,0,1] neg_hi:[0,0,1]\n"
    "buffer_load_dwordx4 v249, s[16:19], s62 offen offset:1024 lds\n"
    "v_cvt_pk_bf16_f32 v171, v8, v9\n"
    "v_cvt_pk_bf16_f32 v187, v16, v17\n"
    "v_cvt_pk_bf16_f32 v203, v24, v25\n"
    "v_cvt_pk_bf16_f32 v107, v32, v33\n"
    "v_pk_fma_f32 v[8:9], v[12:13], v[6:7], v[8:9] op_sel_hi:[0,1,1] neg_lo:[0,0,1] neg_hi:[0,0,1]\n"
    "v_pk_fma_f32 v[16:17], v[20:21], v[14:15], v[16:17] op_sel_hi:[0,1,1] neg_lo:[0,0,1] neg_hi:[0,0,1]\n"
    "v_pk_fma_f32 v[24:25], v[28:29], v[22:23], v[24:25] op_sel_hi:[0,1,1] neg_lo:[0,0,1] neg_hi:[0,0,1]\n"
    "v_pk_fma_f32 v[32:33], v[36:37], v[30:31], v[32:33] op_sel_hi:[0,1,1] neg_lo:[0,0,1] neg_hi:[0,0,1]\n"
    "v_cvt_pk_bf16_f32 v172, v6, v7\n"
    "v_cvt_pk_bf16_f32 v188, v14, v15\n"
    "v_cvt_pk_bf16_f32 v204, v22, v23\n"
    "v_cvt_pk_bf16_f32 v108, v30, v31\n"
    "v_pk_fma_f32 v[6:7], v[12:13], v[8:9], v[6:7] op_sel_hi:[0,1,1] neg_lo:[0,0,1] neg_hi:[0,0,1]\n"
    "v_pk_fma_f32 v[14:15], v[20:21], v[16:17], v[14:15] op_sel_hi:[0,1,1] neg_lo:[0,0,1] neg_hi:[0,0,1]\n"
    "s_lshl_b32 s62, s48, 3\n"
    "s_add_u32 m0, s43, 65536\n"
    "v_pk_fma_f32 v[22:23], v[28:29], v[24:25], v[22:23] op_sel_hi:[0,1,1] neg_lo:[0,0,1] neg_hi:[0,0,1]\n"
    "buffer_load_dwordx4 v248, s[16:19], s62 offen lds\n"
    "v_pk_fma_f32 v[30:31], v[36:37], v[32:33], v[30:31] op_sel_hi:[0,1,1] neg_lo:[0,0,1] neg_hi:[0,0,1]\n"
    "v_cvt_pk_bf16_f32 v173, v8, v9\n"
    "v_cvt_pk_bf16_f32 v189, v16, v17\n"
    "v_cvt_pk_bf16_f32 v205, v24, v25\n"
    "v_cvt_pk_bf16_f32 v109, v32, v33\n"
    "v_pk_fma_f32 v[8:9], v[12:13], v[6:7], v[8:9] op_sel_hi:[0,1,1] neg_lo:[0,0,1] neg_hi:[0,0,1]\n"
    "v_pk_fma_f32 v[16:17], v[20:21], v[14:15], v[16:17] op_sel_hi:[0,1,1] neg_lo:[0,0,1] neg_hi:[0,0,1]\n"
    "v_pk_fma_f32 v[24:25], v[28:29], v[22:23], v[24:25] op_sel_hi:[0,1,1] neg_lo:[0,0,1] neg_hi:[0,0,1]\n"
    "v_pk_fma_f32 v[32:33], v[36:37], v[30:31], v[32:33] op_sel_hi:[0,1,1] neg_lo:[0,0,1] neg_hi:[0,0,1]\n"
    "v_cvt_pk_bf16_f32 v174, v6, v7\n"
    "v_cvt_pk_bf16_f32 v190, v14, v15\n"
    "v_cvt_pk_bf16_f32 v206, v22, v23\n"
    "v_cvt_pk_bf16_f32 v110, v30, v31\n"
    "v_cndmask_b32 v160, v160, v4, s[28:29]\n"
    "v_cndmask_b32 v176, v176, v4, s[28:29]\n"
    "buffer_load_dwordx4 v249, s[16:19], s62 offen offset:1024 lds\n"
    "v_cndmask_b32 v192, v192, v4, s[28:29]\n"
    "v_cndmask_b32 v96, v96, v4, s[28:29]\n"
    "v_cvt_pk_bf16_f32 v175, v8, v9\n"
    "v_cvt_pk_bf16_f32 v191, v16, v17\n"
    "v_cvt_pk_bf16_f32 v207, v24, v25\n"
    "v_cvt_pk_bf16_f32 v111, v32, v33\n"
    "s_nop 0\n"
    "Ldft3:\n"
    "s_waitcnt vmcnt(2) lgkmcnt(0)\n"
    "buffer_load_dwordx2 v[254:255], v251, s[20:23], s54 offen sc1\n"
    "s_barrier\n"
    "ds_read_b64_tr_b16 v[64:65], v236 offset:16384\n"
    "ds_read_b64_tr_b16 v[66:67], v236 offset:18432\n"
    "ds_read_b64_tr_b16 v[68:69], v237 offset:16384\n"
    "ds_read_b64_tr_b16 v[70:71], v237 offset:18432\n"
    "ds_read_b64_tr_b16 v[72:73], v238 offset:16384\n"
    "ds_read_b64_tr_b16 v[74:75], v238 offset:18432\n"
    "ds_read_b64_tr_b16 v[76:77], v239 offset:16384\n"
    "ds_read_b64_tr_b16 v[78:79], v239 offset:18432\n"
    "ds_read_b64_tr_b16 v[80:81], v236 offset:32768\n"
    "ds_read_b64_tr_b16 v[82:83], v236 offset:34816\n"
    "ds_read_b64_tr_b16 v[84:85], v237 offset:32768\n"
    "ds_read_b64_tr_b16 v[86:87], v237 offset:34816\n"
    "ds_read_b64_tr_b16 v[88:89], v238 offset:32768\n"
    "ds_read_b64_tr_b16 v[90:91], v238 offset:34816\n"
    "ds_read_b64_tr_b16 v[92:93], v239 offset:32768\n"
    "ds_read_b64_tr_b16 v[94:95], v239 offset:34816\n"
    "ds_read_b64_tr_b16 v[220:221], v236 offset:49152\n"
    "ds_read_b64_tr_b16 v[222:223], v236 offset:51200\n"
    "ds_read_b64_tr_b16 v[240:241], v237 offset:49152\n"
    "ds_read_b64_tr_b16 v[242:243], v237 offset:51200\n"
    "ds_read_b64_tr_b16 v[244:245], v238 offset:49152\n"
    "ds_read_b64_tr_b16 v[246:247], v238 offset:51200\n"
    "ds_read_b64_tr_b16 v[232:233], v239 offset:49152\n"
    "ds_read_b64_tr_b16 v[234:235], v239 offset:51200\n"
    "v_add_u32 v236, 0x20000, v236\n"
    "v_add_u32 v237, 0x20000, v237\n"
    "v_add_u32 v238, 0x20000, v238\n"
    "v_add_u32 v239, 0x20000, v239\n"
    "ds_read_b64_tr_b16 v[144:145], v236\n"
    "ds_read_b64_tr_b16 v[146:147], v236 offset:2048\n"
    "ds_read_b64_tr_b16 v[148:149], v237\n"
    "ds_read_b64_tr_b16 v[150:151], v237 offset:2048\n"
    "ds_read_b64_tr_b16 v[152:153], v238\n"
    "ds_read_b64_tr_b16 v[154:155], v238 offset:2048\n"
    "ds_read_b64_tr_b16 v[156:157], v239\n"
    "ds_read_b64_tr_b16 v[158:159], v239 offset:2048\n"
    "ds_read_b64_tr_b16 v[128:129], v236 offset:16384\n"
    "ds_read_b64_tr_b16 v[130:131], v236 offset:18432\n"
    "ds_read_b64_tr_b16 v[132:133], v237 offset:16384\n"
    "ds_read_b64_tr_b16 v[134:135], v237 offset:18432\n"
    "ds_read_b64_tr_b16 v[136:137], v238 offset:16384\n"
    "ds_read_b64_tr_b16 v[138:139], v238 offset:18432\n"
    "ds_read_b64_tr_b16 v[140:141], v239 offset:16384\n"
    "ds_read_b64_tr_b16 v[142:143], v239 offset:18432\n"
    "v_subrev_u32 v236, 0x20000, v236\n"
    "v_subrev_u32 v237, 0x20000, v237\n"
    "v_subrev_u32 v238, 0x20000, v238\n"
    "v_subrev_u32 v239, 0x20000, v239\n"
    "s_waitcnt vmcnt(0)\n"
    "v_cmp_ne_u64 vcc, s[36:37], v[254:255]\n"
    "s_nop 0\n"
    "s_cbranch_vccnz Lsp0_dz\n"
    "Lret_dz:\n"
    "s_waitcnt lgkmcnt(0)\n"
    "s_barrier\n"
    "s_cmp_ge_u32 s32, 4\n"
    "s_cbranch_scc1 LdB\n"
    "LdA:\n"
    "v_lshrrev_b32 v254, 4, v250\n"
    "v_and_b32 v254, 48, v254\n"
    "v_xor_b32 v254, v254, v250\n"
    "v_xor_b32 v255, 64, v254\n"
    "s_lshl_b32 s3, s32, 11\n"
    "s_add_u32 m0, s3, 147456\n"
    "s_add_u32 s62, s53, 2621440\n"
    "buffer_load_dwordx4 v254, s[20:23], s53 offen sc1 lds\n"
    "buffer_load_dwordx4 v255, s[20:23], s53 offen offset:1024 sc1 lds\n"
    "buffer_load_dwordx4 v[8:11], v250, s[20:23], s62 offen sc1\n"
    "buffer_load_dwordx4 v[12:15], v250, s[20:23], s62 offen offset:1024 sc1\n"
    "s_cmp_eq_u32 s58, 0\n"
    "s_cbranch_scc1 Lsp\n"
    "s_branch Lafft\n"
    "LdB:\n"
    "v_xor_b32 v252, 0x4000, v248\n"
    "v_add_u32 v253, 7168, v252\n"
    "s_sleep 2\n"
    "s_lshl_b32 s62, s49, 3\n"
    "s_add_u32 m0, s43, 79872\n"
    "s_nop 0\n"
    "buffer_load_dwordx4 v252, s[16:19], s62 offen lds\n"
    "buffer_load_dwordx4 v253, s[16:19], s62 offen offset:1024 lds\n"
    "s_add_u32 m0, s43, 81920\n"
    "s_nop 0\n"
    "buffer_load_dwordx4 v248, s[16:19], s62 offen lds\n"
    "buffer_load_dwordx4 v249, s[16:19], s62 offen offset:1024 lds\n"
    "s_lshl_b32 s62, s50, 3\n"
    "s_add_u32 m0, s43, 96256\n"
    "s_nop 0\n"
    "buffer_load_dwordx4 v252, s[16:19], s62 offen lds\n"
    "buffer_load_dwordx4 v253, s[16:19], s62 offen offset:1024 lds\n"
    "s_add_u32 m0, s43, 98304\n"
    "s_nop 0\n"
    "buffer_load_dwordx4 v248, s[16:19], s62 offen lds\n"
    "buffer_load_dwordx4 v249, s[16:19], s62 offen offset:1024 lds\n"
    "s_lshl3_add_u32 s62, s45, 917504\n"
    "s_add_u32 m0, s43, 112640\n"
    "s_nop 0\n"
    "buffer_load_dwordx4 v252, s[16:19], s62 offen lds\n"
    "buffer_load_dwordx4 v253, s[16:19], s62 offen offset:1024 lds\n"
    "s_add_u32 m0, s43, 114688\n"
    "s_nop 0\n"
    "buffer_load_dwordx4 v248, s[16:19], s62 offen lds\n"
    "buffer_load_dwordx4 v249, s[16:19], s62 offen offset:1024 lds\n"
    "s_lshl3_add_u32 s62, s46, 917504\n"
    "s_add_u32 m0, s43, 129024\n"
    "s_nop 0\n"
    "buffer_load_dwordx4 v252, s[16:19], s62 offen lds\n"
    "buffer_load_dwordx4 v253, s[16:19], s62 offen offset:1024 lds\n"
    "s_add_u32 m0, s43, 131072\n"
    "s_nop 0\n"
    "buffer_load_dwordx4 v248, s[16:19], s62 offen lds\n"
    "buffer_load_dwordx4 v249, s[16:19], s62 offen offset:1024 lds\n"
    "s_lshl3_add_u32 s62, s47, 917504\n"
    "s_add_u32 m0, s43, 14336\n"
    "s_nop 0\n"
    "buffer_load_dwordx4 v252, s[16:19], s62 offen lds\n"
    "buffer_load_dwordx4 v253, s[16:19], s62 offen offset:1024 lds\n"
    "s_add_u32 m0, s43, 16384\n"
    "s_nop 0\n"
    "buffer_load_dwordx4 v248, s[16:19], s62 offen lds\n"
    "buffer_load_dwordx4 v249, s[16:19], s62 offen offset:1024 lds\n"
    "s_lshl3_add_u32 s62, s48, 917504\n"
    "s_add_u32 m0, s43, 30720\n"
    "s_nop 0\n"
    "buffer_load_dwordx4 v252, s[16:19], s62 offen lds\n"
    "buffer_load_dwordx4 v253, s[16:19], s62 offen offset:1024 lds\n"
    "s_add_u32 m0, s43, 32768\n"
    "s_nop 0\n"
    "buffer_load_dwordx4 v248, s[16:19], s62 offen lds\n"
    "buffer_load_dwordx4 v249, s[16:19], s62 offen offset:1024 lds\n"
    "v_and_b32 v211, 0x70, v250\n"
    "v_min_u32 v211, 0x60, v211\n"
    "v_lshlrev_b32 v211, 1, v211\n"
    "s_lshl_b32 s3, s56, 12\n"
    "s_lshr_b32 s60, s32, 2\n"
    "s_lshl_b32 s60, s60, 11\n"
    "s_add_u32 s3, s3, s60\n"
    "s_add_u32 s3, s3, 8388608\n"
    "v_add_u32 v210, s3, v250\n"
    "s_lshl_b32 s56, s56, 5\n"
    "s_add_u32 s56, s56, 43008\n"
    "s_mov_b32 s38, 1\n"
    "s_branch LbC\n"
    "Lsp:\n"
    "s_setprio 2\n"
    "s_add_u32 s62, s53, 4726784\n"
    "buffer_load_dwordx4 v[212:215], v250, s[20:23], s62 offen sc1\n"
    "buffer_load_dwordx4 v[216:219], v250, s[20:23], s62 offen offset:1024 sc1\n"
    "v_bfe_u32 v0, v250, 4, 1\n"
    "v_mul_u32_u24 v0, 0x8000, v0\n"
    "v_lshl_or_b32 v0, v0, 16, v0\n"
    "v_xor_b32 v1, 0x80008000, v0\n"
    "s_mov_b32 s63, 0x80008000\n"
    "v_xor_b32 v96, v0, v200\n"
    "v_xor_b32 v100, v0, v204\n"
    "v_xor_b32 v104, s63, v204\n"
    "v_xor_b32 v108, v1, v204\n"
    "v_xor_b32 v97, v0, v201\n"
    "v_xor_b32 v101, v0, v205\n"
    "v_xor_b32 v105, s63, v205\n"
    "v_xor_b32 v109, v1, v205\n"
    "v_xor_b32 v98, v0, v202\n"
    "v_xor_b32 v102, v0, v206\n"
    "v_xor_b32 v106, s63, v206\n"
    "v_xor_b32 v110, v1, v206\n"
    "v_xor_b32 v99, v0, v203\n"
    "v_xor_b32 v103, v0, v207\n"
    "v_xor_b32 v107, s63, v207\n"
    "v_xor_b32 v111, v1, v207\n"
    "v_bfe_u32 v2, v250, 5, 3\n"
    "v_lshrrev_b32 v3, 8, v250\n"
    "v_xor_b32 v2, v2, v3\n"
    "v_and_b32 v3, 0xf0, v250\n"
    "v_lshlrev_b32 v3, 3, v3\n"
    "v_lshl_or_b32 v2, v2, 4, v3\n"
    "v_add_u32 v224, 147456, v2\n"
    "v_xor_b32 v225, 64, v224\n"
    "s_and_b32 s3, s32, 2\n"
    "s_lshl_b32 s60, s3, 10\n"
    "s_add_u32 s60, s60, 155648\n"
    "v_add_u32 v227, s60, v250\n"
    "s_lshl_b32 s3, s3, 9\n"
    "s_and_b32 s60, s32, 1\n"
    "s_lshl3_add_u32 s3, s60, s3\n"
    "s_add_u32 s3, s3, 155648\n"
    "v_add_u32 v226, s3, v250\n"
    "v_mov_b32 v228, 3584\n"
    "v_mov_b32 v229, 1\n"
    "s_brev_b32 s61, 1\n"
    "s_waitcnt vmcnt(4)\n"
    "s_mov_b64 exec, 1\n"
    "ds_add_u32 v228, v229 offset:0\n"
    "s_mov_b64 exec, -1\n"
    "LpS:\n"
    "ds_read_b32 v230, v228 offset:0\n"
    "ds_read_b128 v[32:35], v224\n"
    "ds_read_b128 v[40:43], v224 offset:2048\n"
    "ds_read_b128 v[36:39], v225\n"
    "ds_read_b128 v[44:47], v225 offset:2048\n"
    "s_waitcnt lgkmcnt(4)\n"
    "v_readfirstlane_b32 s3, v230\n"
    "s_add_u32 s59, s59, 0x1000\n"
    "s_cselect_b32 s3, 4, s3\n"
    "s_cmp_lt_u32 s3, 4\n"
    "s_cbranch_scc1 LpS\n"
    "s_waitcnt lgkmcnt(3)\n"
    "v_mfma_f32_16x16x32_bf16 v[16:19], v[32:35], v[200:203], 0\n"
    "v_xor_b32 v112, s61, v160\n"
    "v_xor_b32 v116, s61, v164\n"
    "v_mfma_f32_16x16x32_bf16 v[24:27], v[32:35], v[104:107], 0\n"
    "v_xor_b32 v120, s63, v164\n"
    "v_xor_b32 v124, 0x8000, v164\n"
    "s_waitcnt lgkmcnt(2)\n"
    "v_mfma_f32_16x16x32_bf16 v[20:23], v[40:43], v[200:203], 0\n"
    "v_xor_b32 v113, s61, v161\n"
    "v_xor_b32 v117, s61, v165\n"
    "v_mfma_f32_16x16x32_bf16 v[28:31], v[40:43], v[104:107], 0\n"
    "v_xor_b32 v121, s63, v165\n"
    "v_xor_b32 v125, 0x8000, v165\n"
    "s_waitcnt lgkmcnt(1)\n"
    "v_mfma_f32_16x16x32_bf16 v[16:19], v[36:39], v[96:99], v[16:19]\n"
    "v_xor_b32 v114, s61, v162\n"
    "v_xor_b32 v118, s61, v166\n"
    "v_mfma_f32_16x16x32_bf16 v[24:27], v[36:39], v[108:111], v[24:27]\n"
    "v_xor_b32 v122, s63, v166\n"
    "v_xor_b32 v126, 0x8000, v166\n"
    "s_waitcnt lgkmcnt(0)\n"
    "v_mfma_f32_16x16x32_bf16 v[20:23], v[44:47], v[96:99], v[20:23]\n"
    "v_xor_b32 v115, s61, v163\n"
    "v_xor_b32 v119, s61, v167\n"
    "v_mfma_f32_16x16x32_bf16 v[28:31], v[44:47], v[108:111], v[28:31]\n"
    "v_xor_b32 v123, s63, v167\n"
    "v_xor_b32 v127, 0x8000, v167\n"
    "s_waitcnt vmcnt(3)\n"
    "v_lshlrev_b32 v32, 16, v8\n"
    "v_and_b32 v33, 0xffff0000, v8\n"
    "v_lshlrev_b32 v34, 16, v9\n"
    "v_and_b32 v35, 0xffff0000, v9\n"
    "v_lshlrev_b32 v40, 16, v10\n"
    "v_and_b32 v41, 0xffff0000, v10\n"
    "v_lshlrev_b32 v42, 16, v11\n"
    "v_and_b32 v43, 0xffff0000, v11\n"
    "s_waitcnt vmcnt(2)\n"
    "v_lshlrev_b32 v36, 16, v12\n"
    "v_and_b32 v37, 0xffff0000, v12\n"
    "v_lshlrev_b32 v38, 16, v13\n"
    "v_and_b32 v39, 0xffff0000, v13\n"
    "v_lshlrev_b32 v44, 16, v14\n"
    "v_and_b32 v45, 0xffff0000, v14\n"
    "v_lshlrev_b32 v46, 16, v15\n"
    "v_and_b32 v47, 0xffff0000, v15\n"
    "v_pk_mul_f32 v[48:49], v[24:25], v[176:177]\n"
    "v_pk_mul_f32 v[50:51], v[26:27], v[178:179]\n"
    "v_pk_mul_f32 v[52:53], v[28:29], v[180:181]\n"
    "v_pk_mul_f32 v[54:55], v[30:31], v[182:183]\n"
    "v_pk_mul_f32 v[56:57], v[16:17], v[176:177]\n"
    "v_pk_mul_f32 v[58:59], v[18:19], v[178:179]\n"
    "v_pk_mul_f32 v[60:61], v[20:21], v[180:181]\n"
    "v_pk_mul_f32 v[62:63], v[22:23], v[182:183]\n"
    "v_pk_fma_f32 v[16:17], v[16:17], v[168:169], v[48:49]\n"
    "v_pk_fma_f32 v[18:19], v[18:19], v[170:171], v[50:51]\n"
    "v_pk_fma_f32 v[20:21], v[20:21], v[172:173], v[52:53]\n"
    "v_pk_fma_f32 v[22:23], v[22:23], v[174:175], v[54:55]\n"
    "v_pk_fma_f32 v[24:25], v[24:25], v[168:169], v[56:57] neg_lo:[0,0,1] neg_hi:[0,0,1]\n"
    "v_pk_fma_f32 v[26:27], v[26:27], v[170:171], v[58:59] neg_lo:[0,0,1] neg_hi:[0,0,1]\n"
    "v_pk_fma_f32 v[28:29], v[28:29], v[172:173], v[60:61] neg_lo:[0,0,1] neg_hi:[0,0,1]\n"
    "v_pk_fma_f32 v[30:31], v[30:31], v[174:175], v[62:63] neg_lo:[0,0,1] neg_hi:[0,0,1]\n"
    "v_cvt_pk_bf16_f32 v0, v16, v17\n"
    "v_cvt_pk_bf16_f32 v1, v18, v19\n"
    "v_cvt_pk_bf16_f32 v2, v20, v21\n"
    "v_cvt_pk_bf16_f32 v3, v22, v23\n"
    "v_cvt_pk_bf16_f32 v4, v24, v25\n"
    "v_cvt_pk_bf16_f32 v5, v26, v27\n"
    "v_cvt_pk_bf16_f32 v6, v28, v29\n"
    "v_cvt_pk_bf16_f32 v7, v30, v31\n"
    "v_mfma_f32_16x16x32_bf16 v[16:19], v[160:163], v[0:3], 0\n"
    "s_nop 0\n"
    "v_mfma_f32_16x16x32_bf16 v[24:27], v[160:163], v[4:7], 0\n"
    "v_mfma_f32_16x16x32_bf16 v[20:23], v[112:115], v[0:3], 0\n"
    "v_mfma_f32_16x16x32_bf16 v[28:31], v[112:115], v[4:7], 0\n"
    "v_mfma_f32_16x16x32_bf16 v[16:19], v[164:167], v[4:7], v[16:19]\n"
    "v_mfma_f32_16x16x32_bf16 v[24:27], v[120:123], v[0:3], v[24:27]\n"
    "v_mfma_f32_16x16x32_bf16 v[20:23], v[116:119], v[4:7], v[20:23]\n"
    "v_mfma_f32_16x16x32_bf16 v[28:31], v[124:127], v[0:3], v[28:31]\n"
    "s_nop 5\n"
    "v_pk_mul_f32 v[48:49], v[24:25], v[40:41]\n"
    "v_pk_mul_f32 v[50:51], v[26:27], v[42:43]\n"
    "v_pk_mul_f32 v[52:53], v[28:29], v[44:45]\n"
    "v_pk_mul_f32 v[54:55], v[30:31], v[46:47]\n"
    "v_pk_mul_f32 v[56:57], v[16:17], v[40:41]\n"
    "v_pk_mul_f32 v[58:59], v[18:19], v[42:43]\n"
    "v_pk_mul_f32 v[60:61], v[20:21], v[44:45]\n"
    "v_pk_mul_f32 v[62:63], v[22:23], v[46:47]\n"
    "v_pk_fma_f32 v[16:17], v[16:17], v[32:33], v[48:49] neg_lo:[0,0,1] neg_hi:[0,0,1]\n"
    "v_pk_fma_f32 v[18:19], v[18:19], v[34:35], v[50:51] neg_lo:[0,0,1] neg_hi:[0,0,1]\n"
    "v_pk_fma_f32 v[20:21], v[20:21], v[36:37], v[52:53] neg_lo:[0,0,1] neg_hi:[0,0,1]\n"
    "v_pk_fma_f32 v[22:23], v[22:23], v[38:39], v[54:55] neg_lo:[0,0,1] neg_hi:[0,0,1]\n"
    "v_pk_fma_f32 v[24:25], v[24:25], v[32:33], v[56:57]\n"
    "v_pk_fma_f32 v[26:27], v[26:27], v[34:35], v[58:59]\n"
    "v_pk_fma_f32 v[28:29], v[28:29], v[36:37], v[60:61]\n"
    "v_pk_fma_f32 v[30:31], v[30:31], v[38:39], v[62:63]\n"
    "v_cvt_pk_bf16_f32 v0, v16, v17\n"
    "v_cvt_pk_bf16_f32 v1, v18, v19\n"
    "v_cvt_pk_bf16_f32 v2, v20, v21\n"
    "v_cvt_pk_bf16_f32 v3, v22, v23\n"
    "v_cvt_pk_bf16_f32 v4, v24, v25\n"
    "v_cvt_pk_bf16_f32 v5, v26, v27\n"
    "v_cvt_pk_bf16_f32 v6, v28, v29\n"
    "v_cvt_pk_bf16_f32 v7, v30, v31\n"
    "v_mfma_f32_16x16x32_bf16 v[16:19], v[0:3], v[160:163], 0\n"
    "v_mfma_f32_16x16x32_bf16 v[24:27], v[0:3], v[164:167], 0\n"
    "v_mfma_f32_16x16x32_bf16 v[20:23], v[0:3], v[112:115], 0\n"
    "v_mfma_f32_16x16x32_bf16 v[28:31], v[0:3], v[116:119], 0\n"
    "v_mfma_f32_16x16x32_bf16 v[16:19], v[4:7], v[120:123], v[16:19]\n"
    "v_mfma_f32_16x16x32_bf16 v[24:27], v[4:7], v[160:163], v[24:27]\n"
    "v_mfma_f32_16x16x32_bf16 v[20:23], v[4:7], v[124:127], v[20:23]\n"
    "v_mfma_f32_16x16x32_bf16 v[28:31], v[4:7], v[112:115], v[28:31]\n"
    "s_nop 5\n"
    "v_pk_mul_f32 v[48:49], v[24:25], v[192:193]\n"
    "v_pk_mul_f32 v[50:51], v[26:27], v[194:195]\n"
    "v_pk_mul_f32 v[52:53], v[28:29], v[196:197]\n"
    "v_pk_mul_f32 v[54:55], v[30:31], v[198:199]\n"
    "v_pk_mul_f32 v[56:57], v[16:17], v[192:193]\n"
    "v_pk_mul_f32 v[58:59], v[18:19], v[194:195]\n"
    "v_pk_mul_f32 v[60:61], v[20:21], v[196:197]\n"
    "v_pk_mul_f32 v[62:63], v[22:23], v[198:199]\n"
    "v_pk_fma_f32 v[16:17], v[16:17], v[184:185], v[48:49] neg_lo:[0,0,1] neg_hi:[0,0,1]\n"
    "v_pk_fma_f32 v[18:19], v[18:19], v[186:187], v[50:51] neg_lo:[0,0,1] neg_hi:[0,0,1]\n"
    "v_pk_fma_f32 v[20:21], v[20:21], v[188:189], v[52:53] neg_lo:[0,0,1] neg_hi:[0,0,1]\n"
    "v_pk_fma_f32 v[22:23], v[22:23], v[190:191], v[54:55] neg_lo:[0,0,1] neg_hi:[0,0,1]\n"
    "v_pk_fma_f32 v[24:25], v[24:25], v[184:185], v[56:57]\n"
    "v_pk_fma_f32 v[26:27], v[26:27], v[186:187], v[58:59]\n"
    "v_pk_fma_f32 v[28:29], v[28:29], v[188:189], v[60:61]\n"
    "v_pk_fma_f32 v[30:31], v[30:31], v[190:191], v[62:63]\n"
    "v_cvt_pk_bf16_f32 v0, v16, v17\n"
    "v_cvt_pk_bf16_f32 v1, v18, v19\n"
    "v_cvt_pk_bf16_f32 v2, v20, v21\n"
    "v_cvt_pk_bf16_f32 v3, v22, v23\n"
    "v_cvt_pk_bf16_f32 v4, v24, v25\n"
    "v_cvt_pk_bf16_f32 v5, v26, v27\n"
    "v_cvt_pk_bf16_f32 v6, v28, v29\n"
    "v_cvt_pk_bf16_f32 v7, v30, v31\n"
    "ds_write_b64 v226, v[0:1] offset:0\n"
    "ds_write_b64 v226, v[2:3] offset:2048\n"
    "ds_write_b64 v226, v[4:5] offset:4096\n"
    "ds_write_b64 v226, v[6:7] offset:6144\n"
    "ds_read_b128 v[32:35], v224 offset:4096\n"
    "ds_read_b128 v[40:43], v224 offset:6144\n"
    "ds_read_b128 v[36:39], v225 offset:4096\n"
    "ds_read_b128 v[44:47], v225 offset:6144\n"
    "v_and_b32 v211, 0x70, v250\n"
    "v_min_u32 v211, 0x60, v211\n"
    "v_lshlrev_b32 v211, 1, v211\n"
    "s_lshl_b32 s3, s56, 12\n"
    "s_lshr_b32 s60, s32, 2\n"
    "s_lshl_b32 s60, s60, 11\n"
    "s_add_u32 s3, s3, s60\n"
    "s_add_u32 s3, s3, 8388608\n"
    "v_add_u32 v210, s3, v250\n"
    "s_lshl_b32 s56, s56, 5\n"
    "s_add_u32 s56, s56, 43008\n"
    "buffer_load_dwordx2 v[252:253], v211, s[20:23], s56 offen sc1\n"
    "s_brev_b32 s61, 1\n"
    "s_waitcnt vmcnt(2)\n"
    "v_mov_b32 v8, v212\n"
    "v_mov_b32 v9, v213\n"
    "v_mov_b32 v10, v214\n"
    "v_mov_b32 v11, v215\n"
    "s_waitcnt vmcnt(1)\n"
    "v_mov_b32 v12, v216\n"
    "v_mov_b32 v13, v217\n"
    "v_mov_b32 v14, v218\n"
    "v_mov_b32 v15, v219\n"
    "v_subrev_u32 v226, 0x2000, v226\n"
    "v_subrev_u32 v227, 0x2000, v227\n"
    "s_waitcnt lgkmcnt(0)\n"
    "s_mov_b64 exec, 1\n"
    "ds_add_u32 v228, v229 offset:36\n"
    "s_mov_b64 exec, -1\n"
    "v_mfma_f32_16x16x32_bf16 v[16:19], v[32:35], v[200:203], 0\n"
    "v_mfma_f32_16x16x32_bf16 v[24:27], v[32:35], v[104:107], 0\n"
    "v_mfma_f32_16x16x32_bf16 v[20:23], v[40:43], v[200:203], 0\n"
    "v_mfma_f32_16x16x32_bf16 v[28:31], v[40:43], v[104:107], 0\n"
    "v_mfma_f32_16x16x32_bf16 v[16:19], v[36:39], v[96:99], v[16:19]\n"
    "v_mfma_f32_16x16x32_bf16 v[24:27], v[36:39], v[108:111], v[24:27]\n"
    "v_mfma_f32_16x16x32_bf16 v[20:23], v[44:47], v[96:99], v[20:23]\n"
    "v_mfma_f32_16x16x32_bf16 v[28:31], v[44:47], v[108:111], v[28:31]\n"
    "v_lshlrev_b32 v32, 16, v8\n"
    "v_and_b32 v33, 0xffff0000, v8\n"
    "v_lshlrev_b32 v34, 16, v9\n"
    "v_and_b32 v35, 0xffff0000, v9\n"
    "v_lshlrev_b32 v40, 16, v10\n"
    "v_and_b32 v41, 0xffff0000, v10\n"
    "v_lshlrev_b32 v42, 16, v11\n"
    "v_and_b32 v43, 0xffff0000, v11\n"
    "v_lshlrev_b32 v36, 16, v12\n"
    "v_and_b32 v37, 0xffff0000, v12\n"
    "v_lshlrev_b32 v38, 16, v13\n"
    "v_and_b32 v39, 0xffff0000, v13\n"
    "v_lshlrev_b32 v44, 16, v14\n"
    "v_and_b32 v45, 0xffff0000, v14\n"
    "v_lshlrev_b32 v46, 16, v15\n"
    "v_and_b32 v47, 0xffff0000, v15\n"
    "v_pk_mul_f32 v[48:49], v[24:25], v[176:177]\n"
    "v_pk_mul_f32 v[50:51], v[26:27], v[178:179]\n"
    "v_pk_mul_f32 v[52:53], v[28:29], v[180:181]\n"
    "v_pk_mul_f32 v[54:55], v[30:31], v[182:183]\n"
    "v_pk_mul_f32 v[56:57], v[16:17], v[176:177]\n"
    "v_pk_mul_f32 v[58:59], v[18:19], v[178:179]\n"
    "v_pk_mul_f32 v[60:61], v[20:21], v[180:181]\n"
    "v_pk_mul_f32 v[62:63], v[22:23], v[182:183]\n"
    "v_pk_fma_f32 v[16:17], v[16:17], v[168:169], v[48:49]\n"
    "v_pk_fma_f32 v[18:19], v[18:19], v[170:171], v[50:51]\n"
    "v_pk_fma_f32 v[20:21], v[20:21], v[172:173], v[52:53]\n"
    "v_pk_fma_f32 v[22:23], v[22:23], v[174:175], v[54:55]\n"
    "v_pk_fma_f32 v[24:25], v[24:25], v[168:169], v[56:57] neg_lo:[0,0,1] neg_hi:[0,0,1]\n"
    "v_pk_fma_f32 v[26:27], v[26:27], v[170:171], v[58:59] neg_lo:[0,0,1] neg_hi:[0,0,1]\n"
    "v_pk_fma_f32 v[28:29], v[28:29], v[172:173], v[60:61] neg_lo:[0,0,1] neg_hi:[0,0,1]\n"
    "v_pk_fma_f32 v[30:31], v[30:31], v[174:175], v[62:63] neg_lo:[0,0,1] neg_hi:[0,0,1]\n"
    "v_cvt_pk_bf16_f32 v0, v16, v17\n"
    "v_cvt_pk_bf16_f32 v1, v18, v19\n"
    "v_cvt_pk_bf16_f32 v2, v20, v21\n"
    "v_cvt_pk_bf16_f32 v3, v22, v23\n"
    "v_cvt_pk_bf16_f32 v4, v24, v25\n"
    "v_cvt_pk_bf16_f32 v5, v26, v27\n"
    "v_cvt_pk_bf16_f32 v6, v28, v29\n"
    "v_cvt_pk_bf16_f32 v7, v30, v31\n"
    "v_mfma_f32_16x16x32_bf16 v[16:19], v[160:163], v[0:3], 0\n"
    "s_nop 0\n"
    "v_mfma_f32_16x16x32_bf16 v[24:27], v[160:163], v[4:7], 0\n"
    "v_mfma_f32_16x16x32_bf16 v[20:23], v[112:115], v[0:3], 0\n"
    "v_mfma_f32_16x16x32_bf16 v[28:31], v[112:115], v[4:7], 0\n"
    "v_mfma_f32_16x16x32_bf16 v[16:19], v[164:167], v[4:7], v[16:19]\n"
    "v_mfma_f32_16x16x32_bf16 v[24:27], v[120:123], v[0:3], v[24:27]\n"
    "v_mfma_f32_16x16x32_bf16 v[20:23], v[116:119], v[4:7], v[20:23]\n"
    "v_mfma_f32_16x16x32_bf16 v[28:31], v[124:127], v[0:3], v[28:31]\n"
    "s_nop 5\n"
    "v_pk_mul_f32 v[48:49], v[24:25], v[40:41]\n"
    "v_pk_mul_f32 v[50:51], v[26:27], v[42:43]\n"
    "v_pk_mul_f32 v[52:53], v[28:29], v[44:45]\n"
    "v_pk_mul_f32 v[54:55], v[30:31], v[46:47]\n"
    "v_pk_mul_f32 v[56:57], v[16:17], v[40:41]\n"
    "v_pk_mul_f32 v[58:59], v[18:19], v[42:43]\n"
    "v_pk_mul_f32 v[60:61], v[20:21], v[44:45]\n"
    "v_pk_mul_f32 v[62:63], v[22:23], v[46:47]\n"
    "v_pk_fma_f32 v[16:17], v[16:17], v[32:33], v[48:49] neg_lo:[0,0,1] neg_hi:[0,0,1]\n"
    "v_pk_fma_f32 v[18:19], v[18:19], v[34:35], v[50:51] neg_lo:[0,0,1] neg_hi:[0,0,1]\n"
    "v_pk_fma_f32 v[20:21], v[20:21], v[36:37], v[52:53] neg_lo:[0,0,1] neg_hi:[0,0,1]\n"
    "v_pk_fma_f32 v[22:23], v[22:23], v[38:39], v[54:55] neg_lo:[0,0,1] neg_hi:[0,0,1]\n"
    "v_pk_fma_f32 v[24:25], v[24:25], v[32:33], v[56:57]\n"
    "v_pk_fma_f32 v[26:27], v[26:27], v[34:35], v[58:59]\n"
    "v_pk_fma_f32 v[28:29], v[28:29], v[36:37], v[60:61]\n"
    "v_pk_fma_f32 v[30:31], v[30:31], v[38:39], v[62:63]\n"
    "v_cvt_pk_bf16_f32 v0, v16, v17\n"
    "v_cvt_pk_bf16_f32 v1, v18, v19\n"
    "v_cvt_pk_bf16_f32 v2, v20, v21\n"
    "v_cvt_pk_bf16_f32 v3, v22, v23\n"
    "v_cvt_pk_bf16_f32 v4, v24, v25\n"
    "v_cvt_pk_bf16_f32 v5, v26, v27\n"
    "v_cvt_pk_bf16_f32 v6, v28, v29\n"
    "v_cvt_pk_bf16_f32 v7, v30, v31\n"
    "v_mfma_f32_16x16x32_bf16 v[16:19], v[0:3], v[160:163], 0\n"
    "v_mfma_f32_16x16x32_bf16 v[24:27], v[0:3], v[164:167], 0\n"
    "v_mfma_f32_16x16x32_bf16 v[20:23], v[0:3], v[112:115], 0\n"
    "v_mfma_f32_16x16x32_bf16 v[28:31], v[0:3], v[116:119], 0\n"
    "v_mfma_f32_16x16x32_bf16 v[16:19], v[4:7], v[120:123], v[16:19]\n"
    "v_mfma_f32_16x16x32_bf16 v[24:27], v[4:7], v[160:163], v[24:27]\n"
    "v_mfma_f32_16x16x32_bf16 v[20:23], v[4:7], v[124:127], v[20:23]\n"
    "v_mfma_f32_16x16x32_bf16 v[28:31], v[4:7], v[112:115], v[28:31]\n"
    "s_nop 5\n"
    "v_pk_mul_f32 v[48:49], v[24:25], v[192:193]\n"
    "v_pk_mul_f32 v[50:51], v[26:27], v[194:195]\n"
    "v_pk_mul_f32 v[52:53], v[28:29], v[196:197]\n"
    "v_pk_mul_f32 v[54:55], v[30:31], v[198:199]\n"
    "v_pk_mul_f32 v[56:57], v[16:17], v[192:193]\n"
    "v_pk_mul_f32 v[58:59], v[18:19], v[194:195]\n"
    "v_pk_mul_f32 v[60:61], v[20:21], v[196:197]\n"
    "v_pk_mul_f32 v[62:63], v[22:23], v[198:199]\n"
    "v_pk_fma_f32 v[16:17], v[16:17], v[184:185], v[48:49] neg_lo:[0,0,1] neg_hi:[0,0,1]\n"
    "v_pk_fma_f32 v[18:19], v[18:19], v[186:187], v[50:51] neg_lo:[0,0,1] neg_hi:[0,0,1]\n"
    "v_pk_fma_f32 v[20:21], v[20:21], v[188:189], v[52:53] neg_lo:[0,0,1] neg_hi:[0,0,1]\n"
    "v_pk_fma_f32 v[22:23], v[22:23], v[190:191], v[54:55] neg_lo:[0,0,1] neg_hi:[0,0,1]\n"
    "v_pk_fma_f32 v[24:25], v[24:25], v[184:185], v[56:57]\n"
    "v_pk_fma_f32 v[26:27], v[26:27], v[186:187], v[58:59]\n"
    "v_pk_fma_f32 v[28:29], v[28:29], v[188:189], v[60:61]\n"
    "v_pk_fma_f32 v[30:31], v[30:31], v[190:191], v[62:63]\n"
    "v_cvt_pk_bf16_f32 v0, v16, v17\n"
    "v_cvt_pk_bf16_f32 v1, v18, v19\n"
    "v_cvt_pk_bf16_f32 v2, v20, v21\n"
    "v_cvt_pk_bf16_f32 v3, v22, v23\n"
    "v_cvt_pk_bf16_f32 v4, v24, v25\n"
    "v_cvt_pk_bf16_f32 v5, v26, v27\n"
    "v_cvt_pk_bf16_f32 v6, v28, v29\n"
    "v_cvt_pk_bf16_f32 v7, v30, v31\n"
    "v_mov_b32 v254, s43\n"
    "v_mov_b32 v255, 1\n"
    "s_waitcnt vmcnt(0)\n"
    "v_cmp_ne_u64 vcc, s[36:37], v[252:253]\n"
    "s_nop 0\n"
    "s_cbranch_vccnz LpH\n"
    "s_mov_b64 exec, 1\n"
    "ds_write_b32 v254, v255 offset:3612\n"
    "s_mov_b64 exec, -1\n"
    "LpH:\n"
    "LpZ:\n"
    "ds_read_b32 v230, v228 offset:36\n"
    "s_waitcnt lgkmcnt(0)\n"
    "v_readfirstlane_b32 s3, v230\n"
    "s_add_u32 s59, s59, 0x1000\n"
    "s_cselect_b32 s3, 4, s3\n"
    "s_cmp_lt_u32 s3, 4\n"
    "s_cbranch_scc1 LpZ\n"
    "ds_write_b64 v226, v[0:1] offset:0\n"
    "ds_write_b64 v226, v[2:3] offset:2048\n"
    "ds_write_b64 v226, v[4:5] offset:4096\n"
    "ds_write_b64 v226, v[6:7] offset:6144\n"
    "s_mov_b64 exec, 1\n"
    "ds_add_u32 v228, v229 offset:4\n"
    "s_mov_b64 exec, -1\n"
    "LpQ:\n"
    "ds_read_b32 v230, v228 offset:4\n"
    "ds_read_b128 v[32:35], v227 offset:8192\n"
    "ds_read_b128 v[36:39], v227 offset:9216\n"
    "ds_read_b128 v[40:43], v227 offset:12288\n"
    "ds_read_b128 v[44:47], v227 offset:13312\n"
    "ds_read_b128 v[48:51], v227\n"
    "ds_read_b128 v[52:55], v227 offset:1024\n"
    "ds_read_b128 v[56:59], v227 offset:4096\n"
    "ds_read_b128 v[60:63], v227 offset:5120\n"
    "s_waitcnt lgkmcnt(8)\n"
    "v_readfirstlane_b32 s3, v230\n"
    "s_add_u32 s59, s59, 0x1000\n"
    "s_cselect_b32 s3, 4, s3\n"
    "s_cmp_lt_u32 s3, 4\n"
    "s_cbranch_scc1 LpQ\n"
    "s_waitcnt lgkmcnt(0)\n"
    "s_mov_b64 exec, 1\n"
    "ds_add_u32 v228, v229 offset:40\n"
    "s_mov_b64 exec, -1\n"
    "v_mfma_f32_16x16x32_bf16 v[16:19], v[200:203], v[32:35], 0\n"
    "v_mfma_f32_16x16x32_bf16 v[24:27], v[200:203], v[48:51], 0\n"
    "v_mfma_f32_16x16x32_bf16 v[16:19], v[96:99], v[36:39], v[16:19]\n"
    "v_mfma_f32_16x16x32_bf16 v[24:27], v[96:99], v[52:55], v[24:27]\n"
    "v_mfma_f32_16x16x32_bf16 v[16:19], v[104:107], v[40:43], v[16:19]\n"
    "v_mfma_f32_16x16x32_bf16 v[24:27], v[104:107], v[56:59], v[24:27]\n"
    "v_mfma_f32_16x16x32_bf16 v[16:19], v[108:111], v[44:47], v[16:19]\n"
    "v_mfma_f32_16x16x32_bf16 v[24:27], v[108:111], v[60:63], v[24:27]\n"
    "v_xor_b32 v32, s61, v32\n"
    "v_xor_b32 v33, s61, v33\n"
    "v_xor_b32 v34, s61, v34\n"
    "v_xor_b32 v35, s61, v35\n"
    "v_xor_b32 v36, s61, v36\n"
    "v_xor_b32 v37, s61, v37\n"
    "v_xor_b32 v38, s61, v38\n"
    "v_xor_b32 v39, s61, v39\n"
    "v_xor_b32 v40, s61, v40\n"
    "v_xor_b32 v41, s61, v41\n"
    "v_xor_b32 v42, s61, v42\n"
    "v_xor_b32 v43, s61, v43\n"
    "v_xor_b32 v44, s61, v44\n"
    "v_xor_b32 v45, s61, v45\n"
    "v_xor_b32 v46, s61, v46\n"
    "v_xor_b32 v47, s61, v47\n"
    "v_xor_b32 v48, s61, v48\n"
    "v_xor_b32 v49, s61, v49\n"
    "v_xor_b32 v50, s61, v50\n"
    "v_xor_b32 v51, s61, v51\n"
    "v_xor_b32 v52, s61, v52\n"
    "v_xor_b32 v53, s61, v53\n"
    "v_xor_b32 v54, s61, v54\n"
    "v_xor_b32 v55, s61, v55\n"
    "v_xor_b32 v56, s61, v56\n"
    "v_xor_b32 v57, s61, v57\n"
    "v_xor_b32 v58, s61, v58\n"
    "v_xor_b32 v59, s61, v59\n"
    "v_xor_b32 v60, s61, v60\n"
    "v_xor_b32 v61, s61, v61\n"
    "v_xor_b32 v62, s61, v62\n"
    "v_xor_b32 v63, s61, v63\n"
    "v_mfma_f32_16x16x32_bf16 v[20:23], v[200:203], v[32:35], 0\n"
    "v_lshrrev_b32 v8, 4, v250\n"
    "v_and_b32 v9, 48, v8\n"
    "v_xor_b32 v9, v9, v250\n"
    "v_mfma_f32_16x16x32_bf16 v[28:31], v[200:203], v[48:51], 0\n"
    "v_and_b32 v9, 0x70, v9\n"
    "v_and_b32 v8, 15, v8\n"
    "v_lshl_or_b32 v9, v8, 7, v9\n"
    "v_mfma_f32_16x16x32_bf16 v[20:23], v[96:99], v[36:39], v[20:23]\n"
    "s_lshl_b32 s3, s32, 11\n"
    "s_add_u32 s3, s3, 147456\n"
    "s_and_b32 s60, s32, 2\n"
    "v_mfma_f32_16x16x32_bf16 v[28:31], v[96:99], v[52:55], v[28:31]\n"
    "s_lshl4_add_u32 s62, s60, 0\n"
    "s_lshl1_add_u32 s62, s62, s3\n"
    "v_xor_b32 v9, s62, v9\n"
    "v_mfma_f32_16x16x32_bf16 v[20:23], v[104:107], v[40:43], v[20:23]\n"
    "v_xor_b32 v10, 64, v9\n"
    "v_lshrrev_b32 v8, 7, v250\n"
    "v_lshlrev_b32 v11, 4, v8\n"
    "v_mfma_f32_16x16x32_bf16 v[28:31], v[104:107], v[56:59], v[28:31]\n"
    "v_xor_b32 v11, v11, v250\n"
    "v_add_u32 v11, s3, v11\n"
    "v_and_b32 v12, 0x70, v250\n"
    "v_mfma_f32_16x16x32_bf16 v[20:23], v[108:111], v[44:47], v[20:23]\n"
    "v_lshl_or_b32 v12, v8, 16, v12\n"
    "v_mfma_f32_16x16x32_bf16 v[28:31], v[108:111], v[60:63], v[28:31]\n"
    "LpY:\n"
    "ds_read_b32 v230, v228 offset:40\n"
    "s_waitcnt lgkmcnt(0)\n"
    "v_readfirstlane_b32 s3, v230\n"
    "s_add_u32 s59, s59, 0x1000\n"
    "s_cselect_b32 s3, 4, s3\n"
    "s_cmp_lt_u32 s3, 4\n"
    "s_cbranch_scc1 LpY\n"
    "s_branch Lay\n"
    "LfB:\n"
    "s_and_b32 s61, s32, 1\n"
    "v_xor_b32 v3, v5, v2\n"
    "v_lshlrev_b32 v3, 4, v3\n"
    "v_and_b32 v4, 7, v1\n"
    "v_lshl_or_b32 v3, v4, 7, v3\n"
    "v_bfe_u32 v4, v1, 3, 1\n"
    "v_lshl_or_b32 v3, v4, 11, v3\n"
    "s_lshl_b32 s3, s61, 10\n"
    "s_add_u32 s3, s3, 65536\n"
    "v_add_u32 v240, s3, v3\n"
    "v_xor_b32 v241, 64, v240\n"
    "v_bfe_u32 v3, v1, 2, 3\n"
    "v_xor_b32 v3, s32, v3\n"
    "v_and_b32 v4, 3, v1\n"
    "v_lshlrev_b32 v4, 3, v4\n"
    "v_lshl_or_b32 v3, v3, 5, v4\n"
    "v_bfe_u32 v4, v1, 2, 2\n"
    "v_lshl_or_b32 v3, v4, 8, v3\n"
    "s_add_u32 m0, s42, 65536\n"
    "s_nop 0\n"
    "buffer_load_dwordx4 v245, s[12:15], s59 offen lds\n"
    "buffer_load_dwordx4 v246, s[12:15], s59 offen offset:1024 lds\n"
    "s_add_u32 m0, s45, 65536\n"
    "s_nop 0\n"
    "buffer_load_dwordx4 v245, s[8:11], s50 offen lds\n"
    "s_add_u32 m0, s47, 65536\n"
    "s_nop 0\n"
    "buffer_load_dwordx4 v244, s[4:7], s57 offen lds\n"
    "v_lshl_or_b32 v3, v2, 11, v3\n"
    "v_add_u32 v242, 81920, v3\n"
    "s_add_u32 m0, s42, 114688\n"
    "v_xor_b32 v243, 32, v242\n"
    "buffer_load_dwordx4 v245, s[12:15], s39 offen lds\n"
    "v_lshrrev_b32 v3, 5, v1\n"
    "v_xor_b32 v3, v3, v5\n"
    "v_and_b32 v3, 1, v3\n"
    "v_add_u32 v3, -1, v3\n"
    "v_and_b32 v220, 0x3f803f80, v3\n"
    "buffer_load_dwordx4 v246, s[12:15], s39 offen offset:1024 lds\n"
    "v_and_b32 v3, 1, v1\n"
    "v_mul_u32_u24 v3, 0xaaaa, v3\n"
    "v_add_u32 v3, 0x4444, v3\n"
    "s_add_u32 m0, s45, 114688\n"
    "v_lshl_or_b32 v247, v3, 16, v3\n"
    "buffer_load_dwordx4 v245, s[8:11], s53 offen lds\n"
    "v_mov_b32 v221, v220\n"
    "v_mov_b32 v222, v220\n"
    "v_mov_b32 v223, v220\n"
    "v_mov_b32 v216, 0\n"
    "s_add_u32 m0, s47, 114688\n"
    "v_mov_b32 v217, 0\n"
    "buffer_load_dwordx4 v244, s[4:7], s58 offen lds\n"
    "v_mov_b32 v218, 0\n"
    "v_mov_b32 v219, 0\n"
    "s_waitcnt vmcnt(4)\n"
    "s_barrier\n"
    "ds_read_b64_tr_b16 v[64:65], v242\n"
    "ds_read_b64_tr_b16 v[66:67], v242 offset:1024\n"
    "ds_read_b128 v[96:99], v240\n"
    "ds_read_b128 v[100:103], v241\n"
    "ds_read_b64_tr_b16 v[72:73], v242 offset:8192\n"
    "ds_read_b64_tr_b16 v[74:75], v242 offset:9216\n"
    "ds_read_b64_tr_b16 v[76:77], v242 offset:24576\n"
    "ds_read_b64_tr_b16 v[78:79], v242 offset:25600\n"
    "ds_read_b64_tr_b16 v[68:69], v242 offset:16384\n"
    "ds_read_b64_tr_b16 v[70:71], v242 offset:17408\n"
    "ds_read_b128 v[104:107], v240 offset:4096\n"
    "ds_read_b128 v[108:111], v241 offset:4096\n"
    "ds_read_b128 v[80:83], v240 offset:8192\n"
    "ds_read_b128 v[84:87], v241 offset:8192\n"
    "s_waitcnt lgkmcnt(8)\n"
    "v_mfma_f32_16x16x32_bf16 v[0:3], v[64:67], v[96:99], 0\n"
    "ds_read_b128 v[88:91], v240 offset:12288\n"
    "s_add_u32 s38, s38, 128\n"
    "s_and_b32 s38, s38, 1023\n"
    "s_mul_i32 s39, s38, 1792\n"
    "s_add_u32 m0, s41, 16384\n"
    "v_mfma_f32_16x16x32_bf16 v[0:3], v[72:75], v[100:103], v[0:3]\n"
    "ds_read_b128 v[92:95], v241 offset:12288\n"
    "buffer_load_dwordx4 v245, s[8:11], s39 offen lds\n"
    "s_waitcnt lgkmcnt(6)\n"
    "v_mfma_f32_16x16x32_bf16 v[8:11], v[76:79], v[100:103], 0\n"
    "ds_read_b64_tr_b16 v[112:113], v243\n"
    "v_mfma_f32_16x16x32_bf16 v[8:11], v[68:71], v[96:99], v[8:11]\n"
    "ds_read_b64_tr_b16 v[114:115], v243 offset:1024\n"
    "buffer_load_dwordx4 v246, s[8:11], s39 offen offset:1024 lds\n"
    "v_smfmac_f32_16x16x64_bf16 v[216:219], v[220:223], v[64:71], v247\n"
    "ds_read_b64_tr_b16 v[120:121], v243 offset:8192\n"
    "s_add_u32 m0, s40, 16384\n"
    "v_smfmac_f32_16x16x64_bf16 v[216:219], v[220:223], v[72:79], v247\n"
    "ds_read_b64_tr_b16 v[122:123], v243 offset:9216\n"
    "buffer_load_dwordx4 v244, s[4:7], s38 offen lds\n"
    "s_waitcnt lgkmcnt(8)\n"
    "v_mfma_f32_16x16x32_bf16 v[24:27], v[68:71], v[104:107], 0\n"
    "ds_read_b64_tr_b16 v[124:125], v243 offset:24576\n"
    "v_mfma_f32_16x16x32_bf16 v[24:27], v[76:79], v[108:111], v[24:27]\n"
    "ds_read_b64_tr_b16 v[126:127], v243 offset:25600\n"
    "v_mfma_f32_16x16x32_bf16 v[16:19], v[72:75], v[108:111], 0\n"
    "ds_read_b64_tr_b16 v[116:117], v243 offset:16384\n"
    "buffer_load_dwordx4 v244, s[4:7], s38 offen offset:1024 lds\n"
    "v_mfma_f32_16x16x32_bf16 v[16:19], v[64:67], v[104:107], v[16:19]\n"
    "ds_read_b64_tr_b16 v[118:119], v243 offset:17408\n"
    "s_add_u32 m0, s42, 16384\n"
    "s_waitcnt lgkmcnt(10)\n"
    "v_mfma_f32_16x16x32_bf16 v[32:35], v[64:67], v[80:83], 0\n"
    "buffer_load_dwordx4 v245, s[12:15], s39 offen lds\n"
    "v_mfma_f32_16x16x32_bf16 v[32:35], v[72:75], v[84:87], v[32:35]\n"
    "v_mfma_f32_16x16x32_bf16 v[40:43], v[76:79], v[84:87], 0\n"
    "buffer_load_dwordx4 v246, s[12:15], s39 offen offset:1024 lds\n"
    "v_add_u32 v240, 0xc000, v240\n"
    "v_mfma_f32_16x16x32_bf16 v[40:43], v[68:71], v[80:83], v[40:43]\n"
    "v_add_u32 v241, 0xc000, v241\n"
    "s_waitcnt lgkmcnt(8)\n"
    "v_mfma_f32_16x16x32_bf16 v[56:59], v[68:71], v[88:91], 0\n"
    "s_lshl3_add_u32 s3, s2, s32\n"
    "s_lshl_b32 s3, s3, 10\n"
    "v_lshlrev_b32 v233, 3, v250\n"
    "v_add_u32 v242, 0xc000, v242\n"
    "v_mfma_f32_16x16x32_bf16 v[56:59], v[76:79], v[92:95], v[56:59]\n"
    "s_mov_b64 exec, 255\n"
    "buffer_load_dword v233, v233, s[16:19], s3 offen\n"
    "s_mov_b64 exec, -1\n"
    "v_add_u32 v243, 0xc000, v243\n"
    "v_mfma_f32_16x16x32_bf16 v[48:51], v[72:75], v[92:95], 0\n"
    "v_mfma_f32_16x16x32_bf16 v[48:51], v[64:67], v[88:91], v[48:51]\n"
    "s_waitcnt vmcnt(7) lgkmcnt(0)\n"
    "s_barrier\n"
    "ds_read_b64_tr_b16 v[64:65], v242\n"
    "v_mfma_f32_16x16x32_bf16 v[4:7], v[112:115], v[96:99], 0\n"
    "ds_read_b64_tr_b16 v[66:67], v242 offset:1024\n"
    "s_lshl1_add_u32 s30, s30, s61\n"
    "s_lshl_b32 s54, s30, 9\n"
    "s_add_u32 s54, s54, 16384\n"
    "v_mfma_f32_16x16x32_bf16 v[4:7], v[120:123], v[100:103], v[4:7]\n"
    "ds_read_b64_tr_b16 v[72:73], v242 offset:8192\n"
    "s_mul_i32 s30, s30, 56\n"
    "s_xor_b32 s31, s31, 1\n"
    "v_mfma_f32_16x16x32_bf16 v[12:15], v[124:127], v[100:103], 0\n"
    "ds_read_b64_tr_b16 v[74:75], v242 offset:9216\n"
    "s_add_u32 s38, s38, 128\n"
    "s_and_b32 s38, s38, 1023\n"
    "s_mul_i32 s39, s38, 1792\n"
    "s_add_u32 m0, s41, 65536\n"
    "s_lshl1_add_u32 s30, s31, s30\n"
    "s_bfe_u32 s3, s32, 0x10001\n"
    "s_add_u32 s30, s30, s3\n"
    "v_mfma_f32_16x16x32_bf16 v[12:15], v[116:119], v[96:99], v[12:15]\n"
    "ds_read_b128 v[96:99], v240\n"
    "buffer_load_dwordx4 v245, s[8:11], s39 offen lds\n"
    "s_lshl_b32 s51, s30, 12\n"
    "s_add_u32 s51, s51, 8503296\n"
    "v_mfma_f32_16x16x32_bf16 v[28:31], v[116:119], v[104:107], 0\n"
    "ds_read_b128 v[100:103], v241\n"
    "s_lshl_b32 s52, s30, 5\n"
    "s_add_u32 s52, s52, 43904\n"
    "v_and_b32 v251, 0xf0, v250\n"
    "v_mfma_f32_16x16x32_bf16 v[28:31], v[124:127], v[108:111], v[28:31]\n"
    "ds_read_b64_tr_b16 v[76:77], v242 offset:24576\n"
    "v_lshlrev_b32 v251, 1, v251\n"
    "s_and_b32 s60, s32, 3\n"
    "v_mfma_f32_16x16x32_bf16 v[20:23], v[120:123], v[108:111], 0\n"
    "ds_read_b64_tr_b16 v[78:79], v242 offset:25600\n"
    "s_lshr_b32 s61, s32, 2\n"
    "s_and_b32 s3, s34, 3\n"
    "v_mfma_f32_16x16x32_bf16 v[20:23], v[112:115], v[104:107], v[20:23]\n"
    "ds_read_b64_tr_b16 v[68:69], v242 offset:16384\n"
    "s_lshl2_add_u32 s55, s33, s3\n"
    "s_lshl_b32 s43, s60, 12\n"
    "s_lshl_b32 s3, s61, 11\n"
    "v_mfma_f32_16x16x32_bf16 v[36:39], v[112:115], v[80:83], 0\n"
    "ds_read_b64_tr_b16 v[70:71], v242 offset:17408\n"
    "buffer_load_dwordx4 v246, s[8:11], s39 offen offset:1024 lds\n"
    "s_add_u32 s43, s43, s3\n"
    "s_lshl_b32 s28, s34, 5\n"
    "v_mfma_f32_16x16x32_bf16 v[36:39], v[120:123], v[84:87], v[36:39]\n"
    "ds_read_b128 v[104:107], v240 offset:4096\n"
    "s_or_b32 s28, s28, 0x60\n"
    "s_sub_u32 s28, s28, s16\n"
    "s_xor_b32 s28, s28, 0x200\n"
    "v_mfma_f32_16x16x32_bf16 v[44:47], v[124:127], v[84:87], 0\n"
    "ds_read_b128 v[108:111], v241 offset:4096\n"
    "s_and_b32 s28, s28, 0x380\n"
    "s_lshr_b32 s31, s28, 1\n"
    "v_mfma_f32_16x16x32_bf16 v[44:47], v[116:119], v[80:83], v[44:47]\n"
    "ds_read_b128 v[80:83], v240 offset:8192\n"
    "s_lshl_b32 s57, s55, 10\n"
    "s_add_u32 s57, s57, s28\n"
    "v_mfma_f32_16x16x32_bf16 v[60:63], v[116:119], v[88:91], 0\n"
    "ds_read_b128 v[84:87], v241 offset:8192\n"
    "s_add_u32 m0, s40, 65536\n"
    "s_xor_b32 s3, s61, s55\n"
    "s_and_b32 s3, s3, 1\n"
    "s_lshl_b32 s3, s3, 14\n"
    "v_mfma_f32_16x16x32_bf16 v[60:63], v[124:127], v[92:95], v[60:63]\n"
    "buffer_load_dwordx4 v244, s[4:7], s38 offen lds\n"
    "s_add_u32 s28, s28, s3\n"
    "s_mul_i32 s3, s60, 229376\n"
    "v_mfma_f32_16x16x32_bf16 v[52:55], v[120:123], v[92:95], 0\n"
    "s_add_u32 s28, s28, s3\n"
    "v_lshrrev_b32 v208, 8, v250\n"
    "v_bfe_u32 v209, v250, 5, 2\n"
    "v_mfma_f32_16x16x32_bf16 v[52:55], v[112:115], v[88:91], v[52:55]\n"
    "v_xor_b32 v209, v209, v208\n"
    "v_and_b32 v210, 16, v250\n"
    "v_lshl_or_b32 v209, v209, 5, v210\n"
    "v_lshrrev_b32 v210, 7, v250\n"
    "s_waitcnt lgkmcnt(4)\n"
    "v_mfma_f32_16x16x32_bf16 v[0:3], v[64:67], v[96:99], v[0:3]\n"
    "ds_read_b128 v[88:91], v240 offset:12288\n"
    "v_lshl_or_b32 v209, v210, 10, v209\n"
    "v_add_u32 v248, s28, v209\n"
    "v_add_u32 v249, 7168, v248\n"
    "v_mfma_f32_16x16x32_bf16 v[0:3], v[72:75], v[100:103], v[0:3]\n"
    "ds_read_b128 v[92:95], v241 offset:12288\n"
    "s_cmp_eq_u32 s33, 7\n"
    "s_cselect_b32 s3, 0, s33\n"
    "s_lshl_b32 s44, s3, 12\n"
    "v_mfma_f32_16x16x32_bf16 v[8:11], v[76:79], v[100:103], v[8:11]\n"
    "ds_read_b64_tr_b16 v[112:113], v243\n"
    "s_add_u32 s45, s44, 4096\n"
    "s_cmp_eq_u32 s45, 28672\n"
    "s_cselect_b32 s45, 0, s45\n"
    "s_add_u32 s46, s45, 4096\n"
    "v_mfma_f32_16x16x32_bf16 v[8:11], v[68:71], v[96:99], v[8:11]\n"
    "ds_read_b64_tr_b16 v[114:115], v243 offset:1024\n"
    "buffer_load_dwordx4 v244, s[4:7], s38 offen offset:1024 lds\n"
    "s_cmp_eq_u32 s46, 28672\n"
    "s_cselect_b32 s46, 0, s46\n"
    "s_add_u32 s47, s46, 4096\n"
    "v_smfmac_f32_16x16x64_bf16 v[216:219], v[220:223], v[64:71], v247\n"
    "ds_read_b64_tr_b16 v[120:121], v243 offset:8192\n"
    "s_cmp_eq_u32 s47, 28672\n"
    "s_cselect_b32 s47, 0, s47\n"
    "s_add_u32 s48, s47, 4096\n"
    "s_cmp_eq_u32 s48, 28672\n"
    "s_cselect_b32 s48, 0, s48\n"
    "v_smfmac_f32_16x16x64_bf16 v[216:219], v[220:223], v[72:79], v247\n"
    "ds_read_b64_tr_b16 v[122:123], v243 offset:9216\n"
    "s_add_u32 s49, s48, 4096\n"
    "s_cmp_eq_u32 s49, 28672\n"
    "s_cselect_b32 s49, 0, s49\n"
    "s_waitcnt lgkmcnt(8)\n"
    "v_mfma_f32_16x16x32_bf16 v[24:27], v[68:71], v[104:107], v[24:27]\n"
    "ds_read_b64_tr_b16 v[124:125], v243 offset:24576\n"
    "s_add_u32 s50, s49, 4096\n"
    "s_cmp_eq_u32 s50, 28672\n"
    "s_cselect_b32 s50, 0, s50\n"
    "v_mfma_f32_16x16x32_bf16 v[24:27], v[76:79], v[108:111], v[24:27]\n"
    "ds_read_b64_tr_b16 v[126:127], v243 offset:25600\n"
    "s_add_u32 m0, s42, 65536\n"
    "v_lshrrev_b32 v209, 6, v250\n"
    "v_bfe_u32 v210, v250, 7, 2\n"
    "v_lshlrev_b32 v210, 5, v210\n"
    "v_mfma_f32_16x16x32_bf16 v[16:19], v[72:75], v[108:111], v[16:19]\n"
    "ds_read_b64_tr_b16 v[116:117], v243 offset:16384\n"
    "buffer_load_dwordx4 v245, s[12:15], s39 offen lds\n"
    "v_lshl_or_b32 v209, v209, 7, v210\n"
    "v_bfe_u32 v210, v250, 4, 2\n"
    "v_mfma_f32_16x16x32_bf16 v[16:19], v[64:67], v[104:107], v[16:19]\n"
    "ds_read_b64_tr_b16 v[118:119], v243 offset:17408\n"
    "v_lshl_or_b32 v209, v210, 3, v209\n"
    "s_lshl_b32 s3, s60, 12\n"
    "v_add_u32 v236, s3, v209\n"
    "s_waitcnt lgkmcnt(10)\n"
    "v_mfma_f32_16x16x32_bf16 v[32:35], v[64:67], v[80:83], v[32:35]\n"
    "v_xor_b32 v237, 32, v236\n"
    "v_xor_b32 v238, 64, v236\n"
    "v_mfma_f32_16x16x32_bf16 v[32:35], v[72:75], v[84:87], v[32:35]\n"
    "v_xor_b32 v239, 0x60, v236\n"
    "s_add_u32 s58, s2, 8\n"
    "v_mfma_f32_16x16x32_bf16 v[40:43], v[76:79], v[84:87], v[40:43]\n"
    "s_and_b32 s58, s58, 255\n"
    "s_lshl_b32 s53, s58, 13\n"
    "s_lshr_b32 s3, s58, 2\n"
    "v_add_u32 v240, 0xfffe8000, v240\n"
    "v_mfma_f32_16x16x32_bf16 v[40:43], v[68:71], v[80:83], v[40:43]\n"
    "buffer_load_dwordx4 v246, s[12:15], s39 offen offset:1024 lds\n"
    "s_mul_i32 s3, s3, 128\n"
    "s_add_u32 s53, s53, s3\n"
    "v_add_u32 v241, 0xfffe8000, v241\n"
    "s_waitcnt lgkmcnt(8)\n"
    "v_mfma_f32_16x16x32_bf16 v[56:59], v[68:71], v[88:91], v[56:59]\n"
    "s_lshl_b32 s3, s60, 11\n"
    "s_add_u32 s53, s53, s3\n"
    "s_add_u32 s53, s53, 1048576\n"
    "v_add_u32 v242, 0xfffe8000, v242\n"
    "v_mfma_f32_16x16x32_bf16 v[56:59], v[76:79], v[92:95], v[56:59]\n"
    "s_mul_i32 s56, s55, 56\n"
    "s_mul_i32 s3, s60, 7\n"
    "v_add_u32 v243, 0xfffe8000, v243\n"
    "v_mfma_f32_16x16x32_bf16 v[48:51], v[72:75], v[92:95], v[48:51]\n"
    "s_add_u32 s56, s56, s3\n"
    "s_mov_b32 s59, 0\n"
    "v_mfma_f32_16x16x32_bf16 v[48:51], v[64:67], v[88:91], v[48:51]\n"
    "v_readfirstlane_b32 s35, v252\n"
    "v_readfirstlane_b32 s37, v253\n"
    "s_add_u32 s36, s35, 0x632be59b\n"
    "s_xor_b32 s37, s37, 0x9e3779b9\n"
    "s_add_u32 s35, s35, 1\n"
    "s_sleep 5\n"
    "s_waitcnt vmcnt(7) lgkmcnt(0)\n"
    "s_barrier\n"
    "ds_read_b64_tr_b16 v[64:65], v242\n"
    "v_mfma_f32_16x16x32_bf16 v[4:7], v[112:115], v[96:99], v[4:7]\n"
    "ds_read_b64_tr_b16 v[66:67], v242 offset:1024\n"
    "s_lshl_b32 s62, s44, 3\n"
    "s_mov_b32 m0, s43\n"
    "v_mfma_f32_16x16x32_bf16 v[4:7], v[120:123], v[100:103], v[4:7]\n"
    "ds_read_b64_tr_b16 v[72:73], v242 offset:8192\n"
    "buffer_load_dwordx4 v248, s[16:19], s62 offen lds\n"
    "v_mfma_f32_16x16x32_bf16 v[12:15], v[124:127], v[100:103], v[12:15]\n"
    "ds_read_b64_tr_b16 v[74:75], v242 offset:9216\n"
    "v_mfma_f32_16x16x32_bf16 v[12:15], v[116:119], v[96:99], v[12:15]\n"
    "ds_read_b128 v[96:99], v240\n"
    "v_mfma_f32_16x16x32_bf16 v[28:31], v[116:119], v[104:107], v[28:31]\n"
    "ds_read_b128 v[100:103], v241\n"
    "buffer_load_dwordx4 v249, s[16:19], s62 offen offset:1024 lds\n"
    "v_mfma_f32_16x16x32_bf16 v[28:31], v[124:127], v[108:111], v[28:31]\n"
    "ds_read_b64_tr_b16 v[76:77], v242 offset:24576\n"
    "v_mfma_f32_16x16x32_bf16 v[20:23], v[120:123], v[108:111], v[20:23]\n"
    "ds_read_b64_tr_b16 v[78:79], v242 offset:25600\n"
    "v_mfma_f32_16x16x32_bf16 v[20:23], v[112:115], v[104:107], v[20:23]\n"
    "ds_read_b64_tr_b16 v[68:69], v242 offset:16384\n"
    "s_add_u32 s38, s38, 128\n"
    "s_and_b32 s38, s38, 1023\n"
    "s_mul_i32 s39, s38, 1792\n"
    "s_add_u32 m0, s41, 114688\n"
    "v_mfma_f32_16x16x32_bf16 v[36:39], v[112:115], v[80:83], v[36:39]\n"
    "ds_read_b64_tr_b16 v[70:71], v242 offset:17408\n"
    "buffer_load_dwordx4 v245, s[8:11], s39 offen lds\n"
    "v_mfma_f32_16x16x32_bf16 v[36:39], v[120:123], v[84:87], v[36:39]\n"
    "ds_read_b128 v[104:107], v240 offset:4096\n"
    "v_add_f32 v216, v216, v217\n"
    "v_add_f32 v218, v218, v219\n"
    "v_mfma_f32_16x16x32_bf16 v[44:47], v[124:127], v[84:87], v[44:47]\n"
    "ds_read_b128 v[108:111], v241 offset:4096\n"
    "s_lshl3_add_u32 s28, s2, s32\n"
    "s_lshl_b32 s28, s28, 8\n"
    "s_add_u32 s28, s28, 15728640\n"
    "v_mfma_f32_16x16x32_bf16 v[44:47], v[116:119], v[80:83], v[44:47]\n"
    "ds_read_b128 v[80:83], v240 offset:8192\n"
    "buffer_load_dwordx4 v246, s[8:11], s39 offen offset:1024 lds\n"
    "v_mfma_f32_16x16x32_bf16 v[60:63], v[116:119], v[88:91], v[60:63]\n"
    "ds_read_b128 v[84:87], v241 offset:8192\n"
    "v_mov_b32 v217, s36\n"
    "v_mov_b32 v219, s36\n"
    "v_mfma_f32_16x16x32_bf16 v[60:63], v[124:127], v[92:95], v[60:63]\n"
    "v_mfma_f32_16x16x32_bf16 v[52:55], v[120:123], v[92:95], v[52:55]\n"
    "s_mov_b64 exec, 0xffff\n"
    "buffer_store_dwordx4 v[216:219], v250, s[20:23], s28 offen sc1\n"
    "s_mov_b64 exec, -1\n"
    "s_add_u32 m0, s40, 114688\n"
    "v_mfma_f32_16x16x32_bf16 v[52:55], v[112:115], v[88:91], v[52:55]\n"
    "v_lshrrev_b32 v247, 8, v250\n"
    "v_and_b32 v220, 0xf0, v250\n"
    "v_lshl_or_b32 v247, v247, 12, v220\n"
    "s_andn2_b32 s28, s28, 0x3000\n"
    "buffer_load_dwordx4 v244, s[4:7], s38 offen lds\n"
    "s_waitcnt lgkmcnt(4)\n"
    "v_mfma_f32_16x16x32_bf16 v[0:3], v[64:67], v[96:99], v[0:3]\n"
    "ds_read_b128 v[88:91], v240 offset:12288\n"
    "v_mfma_f32_16x16x32_bf16 v[0:3], v[72:75], v[100:103], v[0:3]\n"
    "ds_read_b128 v[92:95], v241 offset:12288\n"
    "v_mfma_f32_16x16x32_bf16 v[8:11], v[76:79], v[100:103], v[8:11]\n"
    "ds_read_b64_tr_b16 v[112:113], v243\n"
    "v_mfma_f32_16x16x32_bf16 v[8:11], v[68:71], v[96:99], v[8:11]\n"
    "ds_read_b64_tr_b16 v[114:115], v243 offset:1024\n"
    "buffer_load_dwordx4 v244, s[4:7], s38 offen offset:1024 lds\n"
    "s_waitcnt lgkmcnt(6)\n"
    "v_mfma_f32_16x16x32_bf16 v[24:27], v[68:71], v[104:107], v[24:27]\n"
    "ds_read_b64_tr_b16 v[120:121], v243 offset:8192\n"
    "v_mfma_f32_16x16x32_bf16 v[24:27], v[76:79], v[108:111], v[24:27]\n"
    "ds_read_b64_tr_b16 v[122:123], v243 offset:9216\n"
    "v_mfma_f32_16x16x32_bf16 v[16:19], v[72:75], v[108:111], v[16:19]\n"
    "ds_read_b64_tr_b16 v[124:125], v243 offset:24576\n"
    "v_mfma_f32_16x16x32_bf16 v[16:19], v[64:67], v[104:107], v[16:19]\n"
    "ds_read_b64_tr_b16 v[126:127], v243 offset:25600\n"
    "s_add_u32 m0, s42, 114688\n"
    "s_waitcnt lgkmcnt(8)\n"
    "v_mfma_f32_16x16x32_bf16 v[32:35], v[64:67], v[80:83], v[32:35]\n"
    "ds_read_b64_tr_b16 v[116:117], v243 offset:16384\n"
    "buffer_load_dwordx4 v245, s[12:15], s39 offen lds\n"
    "v_mfma_f32_16x16x32_bf16 v[32:35], v[72:75], v[84:87], v[32:35]\n"
    "ds_read_b64_tr_b16 v[118:119], v243 offset:17408\n"
    "v_mfma_f32_16x16x32_bf16 v[40:43], v[76:79], v[84:87], v[40:43]\n"
    "v_mfma_f32_16x16x32_bf16 v[40:43], v[68:71], v[80:83], v[40:43]\n"
    "s_waitcnt lgkmcnt(8)\n"
    "v_mfma_f32_16x16x32_bf16 v[56:59], v[68:71], v[88:91], v[56:59]\n"
    "v_add_u32 v240, 0xc000, v240\n"
    "v_mfma_f32_16x16x32_bf16 v[56:59], v[76:79], v[92:95], v[56:59]\n"
    "buffer_load_dwordx4 v246, s[12:15], s39 offen offset:1024 lds\n"
    "v_add_u32 v241, 0xc000, v241\n"
    "v_mfma_f32_16x16x32_bf16 v[48:51], v[72:75], v[92:95], v[48:51]\n"
    "v_add_u32 v242, 0xc000, v242\n"
    "v_mfma_f32_16x16x32_bf16 v[48:51], v[64:67], v[88:91], v[48:51]\n"
    "v_add_u32 v243, 0xc000, v243\n"
    "s_waitcnt vmcnt(9) lgkmcnt(0)\n"
    "s_barrier\n"
    "ds_read_b64_tr_b16 v[64:65], v242\n"
    "v_mfma_f32_16x16x32_bf16 v[4:7], v[112:115], v[96:99], v[4:7]\n"
    "ds_read_b64_tr_b16 v[66:67], v242 offset:1024\n"
    "v_mfma_f32_16x16x32_bf16 v[4:7], v[120:123], v[100:103], v[4:7]\n"
    "ds_read_b64_tr_b16 v[72:73], v242 offset:8192\n"
    "v_mfma_f32_16x16x32_bf16 v[12:15], v[124:127], v[100:103], v[12:15]\n"
    "ds_read_b64_tr_b16 v[74:75], v242 offset:9216\n"
    "s_add_u32 s38, s38, 128\n"
    "s_and_b32 s38, s38, 1023\n"
    "s_mul_i32 s39, s38, 1792\n"
    "s_add_u32 m0, s41, 16384\n"
    "v_mfma_f32_16x16x32_bf16 v[12:15], v[116:119], v[96:99], v[12:15]\n"
    "ds_read_b128 v[96:99], v240\n"
    "buffer_load_dwordx4 v245, s[8:11], s39 offen lds\n"
    "v_mfma_f32_16x16x32_bf16 v[28:31], v[116:119], v[104:107], v[28:31]\n"
    "ds_read_b128 v[100:103], v241\n"
    "v_mfma_f32_16x16x32_bf16 v[28:31], v[124:127], v[108:111], v[28:31]\n"
    "ds_read_b64_tr_b16 v[76:77], v242 offset:24576\n"
    "v_mfma_f32_16x16x32_bf16 v[20:23], v[120:123], v[108:111], v[20:23]\n"
    "ds_read_b64_tr_b16 v[78:79], v242 offset:25600\n"
    "v_mfma_f32_16x16x32_bf16 v[20:23], v[112:115], v[104:107], v[20:23]\n"
    "ds_read_b64_tr_b16 v[68:69], v242 offset:16384\n"
    "v_mfma_f32_16x16x32_bf16 v[36:39], v[112:115], v[80:83], v[36:39]\n"
    "ds_read_b64_tr_b16 v[70:71], v242 offset:17408\n"
    "buffer_load_dwordx4 v246, s[8:11], s39 offen offset:1024 lds\n"
    "v_mfma_f32_16x16x32_bf16 v[36:39], v[120:123], v[84:87], v[36:39]\n"
    "ds_read_b128 v[104:107], v240 offset:4096\n"
    "v_mfma_f32_16x16x32_bf16 v[44:47], v[124:127], v[84:87], v[44:47]\n"
    "ds_read_b128 v[108:111], v241 offset:4096\n"
    "v_mfma_f32_16x16x32_bf16 v[44:47], v[116:119], v[80:83], v[44:47]\n"
    "ds_read_b128 v[80:83], v240 offset:8192\n"
    "v_mfma_f32_16x16x32_bf16 v[60:63], v[116:119], v[88:91], v[60:63]\n"
    "ds_read_b128 v[84:87], v241 offset:8192\n"
    "s_add_u32 m0, s40, 16384\n"
    "v_mfma_f32_16x16x32_bf16 v[60:63], v[124:127], v[92:95], v[60:63]\n"
    "buffer_load_dwordx4 v244, s[4:7], s38 offen lds\n"
    "v_mfma_f32_16x16x32_bf16 v[52:55], v[120:123], v[92:95], v[52:55]\n"
    "v_mfma_f32_16x16x32_bf16 v[52:55], v[112:115], v[88:91], v[52:55]\n"
    "s_waitcnt lgkmcnt(4)\n"
    "v_mfma_f32_16x16x32_bf16 v[0:3], v[64:67], v[96:99], v[0:3]\n"
    "ds_read_b128 v[88:91], v240 offset:12288\n"
    "v_mfma_f32_16x16x32_bf16 v[0:3], v[72:75], v[100:103], v[0:3]\n"
    "ds_read_b128 v[92:95], v241 offset:12288\n"
    "v_mfma_f32_16x16x32_bf16 v[8:11], v[76:79], v[100:103], v[8:11]\n"
    "ds_read_b64_tr_b16 v[112:113], v243\n"
    "v_mfma_f32_16x16x32_bf16 v[8:11], v[68:71], v[96:99], v[8:11]\n"
    "ds_read_b64_tr_b16 v[114:115], v243 offset:1024\n"
    "buffer_load_dwordx4 v244, s[4:7], s38 offen offset:1024 lds\n"
    "s_waitcnt lgkmcnt(6)\n"
    "v_mfma_f32_16x16x32_bf16 v[24:27], v[68:71], v[104:107], v[24:27]\n"
    "ds_read_b64_tr_b16 v[120:121], v243 offset:8192\n"
    "v_mfma_f32_16x16x32_bf16 v[24:27], v[76:79], v[108:111], v[24:27]\n"
    "ds_read_b64_tr_b16 v[122:123], v243 offset:9216\n"
    "v_mfma_f32_16x16x32_bf16 v[16:19], v[72:75], v[108:111], v[16:19]\n"
    "ds_read_b64_tr_b16 v[124:125], v243 offset:24576\n"
    "v_mfma_f32_16x16x32_bf16 v[16:19], v[64:67], v[104:107], v[16:19]\n"
    "ds_read_b64_tr_b16 v[126:127], v243 offset:25600\n"
    "s_add_u32 m0, s42, 16384\n"
    "s_waitcnt lgkmcnt(8)\n"
    "v_mfma_f32_16x16x32_bf16 v[32:35], v[64:67], v[80:83], v[32:35]\n"
    "ds_read_b64_tr_b16 v[116:117], v243 offset:16384\n"
    "buffer_load_dwordx4 v245, s[12:15], s39 offen lds\n"
    "v_mfma_f32_16x16x32_bf16 v[32:35], v[72:75], v[84:87], v[32:35]\n"
    "ds_read_b64_tr_b16 v[118:119], v243 offset:17408\n"
    "v_mfma_f32_16x16x32_bf16 v[40:43], v[76:79], v[84:87], v[40:43]\n"
    "v_mfma_f32_16x16x32_bf16 v[40:43], v[68:71], v[80:83], v[40:43]\n"
    "s_waitcnt lgkmcnt(8)\n"
    "v_mfma_f32_16x16x32_bf16 v[56:59], v[68:71], v[88:91], v[56:59]\n"
    "s_lshl3_add_u32 s3, s34, s32\n"
    "s_lshl_b32 s3, s3, 13\n"
    "v_add_u32 v240, 0xc000, v240\n"
    "v_mfma_f32_16x16x32_bf16 v[56:59], v[76:79], v[92:95], v[56:59]\n"
    "buffer_load_dwordx4 v246, s[12:15], s39 offen offset:1024 lds\n"
    "s_sub_u32 s60, 0x1e0, s16\n"
    "s_and_b32 s60, s60, 0x380\n"
    "v_add_u32 v241, 0xc000, v241\n"
    "v_mfma_f32_16x16x32_bf16 v[48:51], v[72:75], v[92:95], v[48:51]\n"
    "s_add_u32 s3, s3, s60\n"
    "v_lshlrev_b32 v234, 6, v250\n"
    "v_add_u32 v242, 0xc000, v242\n"
    "v_mfma_f32_16x16x32_bf16 v[48:51], v[64:67], v[88:91], v[48:51]\n"
    "v_add_u32 v243, 0xc000, v243\n"
    "s_mov_b64 exec, 0xff\n"
    "buffer_load_dword v234, v234, s[16:19], s3 offen\n"
    "s_mov_b64 exec, -1\n"
    "s_waitcnt vmcnt(7) lgkmcnt(0)\n"
    "s_barrier\n"
    "ds_read_b64_tr_b16 v[64:65], v242\n"
    "v_mfma_f32_16x16x32_bf16 v[4:7], v[112:115], v[96:99], v[4:7]\n"
    "ds_read_b64_tr_b16 v[66:67], v242 offset:1024\n"
    "v_mfma_f32_16x16x32_bf16 v[4:7], v[120:123], v[100:103], v[4:7]\n"
    "ds_read_b64_tr_b16 v[72:73], v242 offset:8192\n"
    "v_mfma_f32_16x16x32_bf16 v[12:15], v[124:127], v[100:103], v[12:15]\n"
    "ds_read_b64_tr_b16 v[74:75], v242 offset:9216\n"
    "s_add_u32 s38, s38, 128\n"
    "s_and_b32 s38, s38, 1023\n"
    "s_mul_i32 s39, s38, 1792\n"
    "s_add_u32 m0, s41, 65536\n"
    "v_mfma_f32_16x16x32_bf16 v[12:15], v[116:119], v[96:99], v[12:15]\n"
    "ds_read_b128 v[96:99], v240\n"
    "buffer_load_dwordx4 v245, s[8:11], s39 offen lds\n"
    "v_mfma_f32_16x16x32_bf16 v[28:31], v[116:119], v[104:107], v[28:31]\n"
    "ds_read_b128 v[100:103], v241\n"
    "v_mfma_f32_16x16x32_bf16 v[28:31], v[124:127], v[108:111], v[28:31]\n"
    "ds_read_b64_tr_b16 v[76:77], v242 offset:24576\n"
    "v_mfma_f32_16x16x32_bf16 v[20:23], v[120:123], v[108:111], v[20:23]\n"
    "ds_read_b64_tr_b16 v[78:79], v242 offset:25600\n"
    "v_mfma_f32_16x16x32_bf16 v[20:23], v[112:115], v[104:107], v[20:23]\n"
    "ds_read_b64_tr_b16 v[68:69], v242 offset:16384\n"
    "v_mfma_f32_16x16x32_bf16 v[36:39], v[112:115], v[80:83], v[36:39]\n"
    "ds_read_b64_tr_b16 v[70:71], v242 offset:17408\n"
    "buffer_load_dwordx4 v246, s[8:11], s39 offen offset:1024 lds\n"
    "v_mfma_f32_16x16x32_bf16 v[36:39], v[120:123], v[84:87], v[36:39]\n"
    "ds_read_b128 v[104:107], v240 offset:4096\n"
    "v_mfma_f32_16x16x32_bf16 v[44:47], v[124:127], v[84:87], v[44:47]\n"
    "ds_read_b128 v[108:111], v241 offset:4096\n"
    "v_mfma_f32_16x16x32_bf16 v[44:47], v[116:119], v[80:83], v[44:47]\n"
    "ds_read_b128 v[80:83], v240 offset:8192\n"
    "v_mfma_f32_16x16x32_bf16 v[60:63], v[116:119], v[88:91], v[60:63]\n"
    "ds_read_b128 v[84:87], v241 offset:8192\n"
    "s_add_u32 m0, s40, 65536\n"
    "ds_read_b64_tr_b16 v[128:129], v236\n"
    "ds_read_b64_tr_b16 v[130:131], v236 offset:2048\n"
    "v_mfma_f32_16x16x32_bf16 v[60:63], v[124:127], v[92:95], v[60:63]\n"
    "buffer_load_dwordx4 v244, s[4:7], s38 offen lds\n"
    "ds_read_b64_tr_b16 v[132:133], v237\n"
    "ds_read_b64_tr_b16 v[134:135], v237 offset:2048\n"
    "v_mfma_f32_16x16x32_bf16 v[52:55], v[120:123], v[92:95], v[52:55]\n"
    "ds_read_b64_tr_b16 v[136:137], v238\n"
    "ds_read_b64_tr_b16 v[138:139], v238 offset:2048\n"
    "v_mfma_f32_16x16x32_bf16 v[52:55], v[112:115], v[88:91], v[52:55]\n"
    "ds_read_b64_tr_b16 v[140:141], v239\n"
    "ds_read_b64_tr_b16 v[142:143], v239 offset:2048\n"
    "s_waitcnt lgkmcnt(12)\n"
    "v_mfma_f32_16x16x32_bf16 v[0:3], v[64:67], v[96:99], v[0:3]\n"
    "ds_read_b128 v[88:91], v240 offset:12288\n"
    "v_mfma_f32_16x16x32_bf16 v[0:3], v[72:75], v[100:103], v[0:3]\n"
    "ds_read_b128 v[92:95], v241 offset:12288\n"
    "v_mfma_f32_16x16x32_bf16 v[8:11], v[76:79], v[100:103], v[8:11]\n"
    "ds_read_b64_tr_b16 v[112:113], v243\n"
    "v_mfma_f32_16x16x32_bf16 v[8:11], v[68:71], v[96:99], v[8:11]\n"
    "ds_read_b64_tr_b16 v[114:115], v243 offset:1024\n"
    "buffer_load_dwordx4 v244, s[4:7], s38 offen offset:1024 lds\n"
    "s_waitcnt lgkmcnt(14)\n"
    "v_mfma_f32_16x16x32_bf16 v[24:27], v[68:71], v[104:107], v[24:27]\n"
    "ds_read_b64_tr_b16 v[120:121], v243 offset:8192\n"
    "v_mfma_f32_16x16x32_bf16 v[24:27], v[76:79], v[108:111], v[24:27]\n"
    "ds_read_b64_tr_b16 v[122:123], v243 offset:9216\n"
    "v_mfma_f32_16x16x32_bf16 v[16:19], v[72:75], v[108:111], v[16:19]\n"
    "ds_read_b64_tr_b16 v[124:125], v243 offset:24576\n"
    "v_mfma_f32_16x16x32_bf16 v[16:19], v[64:67], v[104:107], v[16:19]\n"
    "ds_read_b64_tr_b16 v[126:127], v243 offset:25600\n"
    "s_add_u32 m0, s42, 65536\n"
    "s_waitcnt lgkmcnt(14)\n"
    "v_mfma_f32_16x16x32_bf16 v[32:35], v[64:67], v[80:83], v[32:35]\n"
    "ds_read_b64_tr_b16 v[116:117], v243 offset:16384\n"
    "buffer_load_dwordx4 v245, s[12:15], s39 offen lds\n"
    "v_mfma_f32_16x16x32_bf16 v[32:35], v[72:75], v[84:87], v[32:35]\n"
    "ds_read_b64_tr_b16 v[118:119], v243 offset:17408\n"
    "v_mfma_f32_16x16x32_bf16 v[40:43], v[76:79], v[84:87], v[40:43]\n"
    "v_mfma_f32_16x16x32_bf16 v[40:43], v[68:71], v[80:83], v[40:43]\n"
    "s_waitcnt lgkmcnt(8)\n"
    "v_mfma_f32_16x16x32_bf16 v[56:59], v[68:71], v[88:91], v[56:59]\n"
    "v_add_u32 v240, 0xfffe8000, v240\n"
    "v_mfma_f32_16x16x32_bf16 v[56:59], v[76:79], v[92:95], v[56:59]\n"
    "buffer_load_dwordx4 v246, s[12:15], s39 offen offset:1024 lds\n"
    "v_add_u32 v241, 0xfffe8000, v241\n"
    "v_mfma_f32_16x16x32_bf16 v[48:51], v[72:75], v[92:95], v[48:51]\n"
    "v_add_u32 v242, 0xfffe8000, v242\n"
    "v_mfma_f32_16x16x32_bf16 v[48:51], v[64:67], v[88:91], v[48:51]\n"
    "v_add_u32 v243, 0xfffe8000, v243\n"
    "s_waitcnt vmcnt(7) lgkmcnt(0)\n"
    "s_barrier\n"
    "ds_read_b64_tr_b16 v[64:65], v242\n"
    "v_mfma_f32_16x16x32_bf16 v[4:7], v[112:115], v[96:99], v[4:7]\n"
    "ds_read_b64_tr_b16 v[66:67], v242 offset:1024\n"
    "s_lshl3_add_u32 s62, s44, 917504\n"
    "s_mov_b32 m0, s43\n"
    "v_mfma_f32_16x16x32_bf16 v[4:7], v[120:123], v[100:103], v[4:7]\n"
    "ds_read_b64_tr_b16 v[72:73], v242 offset:8192\n"
    "buffer_load_dwordx4 v248, s[16:19], s62 offen lds\n"
    "v_mfma_f32_16x16x32_bf16 v[12:15], v[124:127], v[100:103], v[12:15]\n"
    "ds_read_b64_tr_b16 v[74:75], v242 offset:9216\n"
    "v_mfma_f32_16x16x32_bf16 v[12:15], v[116:119], v[96:99], v[12:15]\n"
    "ds_read_b128 v[96:99], v240\n"
    "v_mfma_f32_16x16x32_bf16 v[28:31], v[116:119], v[104:107], v[28:31]\n"
    "ds_read_b128 v[100:103], v241\n"
    "buffer_load_dwordx4 v249, s[16:19], s62 offen offset:1024 lds\n"
    "v_mfma_f32_16x16x32_bf16 v[28:31], v[124:127], v[108:111], v[28:31]\n"
    "ds_read_b64_tr_b16 v[76:77], v242 offset:24576\n"
    "v_mfma_f32_16x16x32_bf16 v[20:23], v[120:123], v[108:111], v[20:23]\n"
    "ds_read_b64_tr_b16 v[78:79], v242 offset:25600\n"
    "v_mfma_f32_16x16x32_bf16 v[20:23], v[112:115], v[104:107], v[20:23]\n"
    "ds_read_b64_tr_b16 v[68:69], v242 offset:16384\n"
    "s_add_u32 s38, s38, 128\n"
    "s_and_b32 s38, s38, 1023\n"
    "s_mul_i32 s39, s38, 1792\n"
    "s_add_u32 m0, s41, 114688\n"
    "v_mfma_f32_16x16x32_bf16 v[36:39], v[112:115], v[80:83], v[36:39]\n"
    "ds_read_b64_tr_b16 v[70:71], v242 offset:17408\n"
    "buffer_load_dwordx4 v245, s[8:11], s39 offen lds\n"
    "v_mfma_f32_16x16x32_bf16 v[36:39], v[120:123], v[84:87], v[36:39]\n"
    "ds_read_b128 v[104:107], v240 offset:4096\n"
    "v_mfma_f32_16x16x32_bf16 v[44:47], v[124:127], v[84:87], v[44:47]\n"
    "ds_read_b128 v[108:111], v241 offset:4096\n"
    "v_mfma_f32_16x16x32_bf16 v[44:47], v[116:119], v[80:83], v[44:47]\n"
    "ds_read_b128 v[80:83], v240 offset:8192\n"
    "buffer_load_dwordx4 v246, s[8:11], s39 offen offset:1024 lds\n"
    "v_mfma_f32_16x16x32_bf16 v[60:63], v[116:119], v[88:91], v[60:63]\n"
    "ds_read_b128 v[84:87], v241 offset:8192\n"
    "v_mfma_f32_16x16x32_bf16 v[60:63], v[124:127], v[92:95], v[60:63]\n"
    "v_mfma_f32_16x16x32_bf16 v[52:55], v[120:123], v[92:95], v[52:55]\n"
    "s_add_u32 m0, s40, 114688\n"
    "v_mfma_f32_16x16x32_bf16 v[52:55], v[112:115], v[88:91], v[52:55]\n"
    "buffer_load_dwordx4 v244, s[4:7], s38 offen lds\n"
    "s_waitcnt lgkmcnt(4)\n"
    "v_mfma_f32_16x16x32_bf16 v[0:3], v[64:67], v[96:99], v[0:3]\n"
    "ds_read_b128 v[88:91], v240 offset:12288\n"
    "v_mfma_f32_16x16x32_bf16 v[0:3], v[72:75], v[100:103], v[0:3]\n"
    "ds_read_b128 v[92:95], v241 offset:12288\n"
    "buffer_load_dwordx4 v244, s[4:7], s38 offen offset:1024 lds\n"
    "v_mfma_f32_16x16x32_bf16 v[8:11], v[76:79], v[100:103], v[8:11]\n"
    "ds_read_b64_tr_b16 v[112:113], v243\n"
    "v_mfma_f32_16x16x32_bf16 v[8:11], v[68:71], v[96:99], v[8:11]\n"
    "ds_read_b64_tr_b16 v[114:115], v243 offset:1024\n"
    "s_waitcnt lgkmcnt(6)\n"
    "v_mfma_f32_16x16x32_bf16 v[24:27], v[68:71], v[104:107], v[24:27]\n"
    "ds_read_b64_tr_b16 v[120:121], v243 offset:8192\n"
    "buffer_load_dwordx4 v[224:227], v251, s[20:23], s54 offen sc1\n"
    "v_mfma_f32_16x16x32_bf16 v[24:27], v[76:79], v[108:111], v[24:27]\n"
    "ds_read_b64_tr_b16 v[122:123], v243 offset:9216\n"
    "v_mfma_f32_16x16x32_bf16 v[16:19], v[72:75], v[108:111], v[16:19]\n"
    "ds_read_b64_tr_b16 v[124:125], v243 offset:24576\n"
    "v_mfma_f32_16x16x32_bf16 v[16:19], v[64:67], v[104:107], v[16:19]\n"
    "ds_read_b64_tr_b16 v[126:127], v243 offset:25600\n"
    "s_waitcnt lgkmcnt(8)\n"
    "v_mfma_f32_16x16x32_bf16 v[32:35], v[64:67], v[80:83], v[32:35]\n"
    "ds_read_b64_tr_b16 v[116:117], v243 offset:16384\n"
    "buffer_load_dwordx4 v[228:231], v251, s[20:23], s54 offen offset:16 sc1\n"
    "v_mfma_f32_16x16x32_bf16 v[32:35], v[72:75], v[84:87], v[32:35]\n"
    "ds_read_b64_tr_b16 v[118:119], v243 offset:17408\n"
    "v_mfma_f32_16x16x32_bf16 v[40:43], v[76:79], v[84:87], v[40:43]\n"
    "s_add_u32 m0, s42, 114688\n"
    "v_mfma_f32_16x16x32_bf16 v[40:43], v[68:71], v[80:83], v[40:43]\n"
    "buffer_load_dwordx4 v245, s[12:15], s39 offen lds\n"
    "s_waitcnt lgkmcnt(8)\n"
    "v_mfma_f32_16x16x32_bf16 v[56:59], v[68:71], v[88:91], v[56:59]\n"
    "v_add_u32 v240, 0xc000, v240\n"
    "v_mfma_f32_16x16x32_bf16 v[56:59], v[76:79], v[92:95], v[56:59]\n"
    "v_add_u32 v241, 0xc000, v241\n"
    "v_mfma_f32_16x16x32_bf16 v[48:51], v[72:75], v[92:95], v[48:51]\n"
    "v_add_u32 v242, 0xc000, v242\n"
    "v_mfma_f32_16x16x32_bf16 v[48:51], v[64:67], v[88:91], v[48:51]\n"
    "buffer_load_dwordx4 v246, s[12:15], s39 offen offset:1024 lds\n"
    "v_add_u32 v243, 0xc000, v243\n"
    "s_waitcnt vmcnt(10) lgkmcnt(0)\n"
    "s_barrier\n"
    "ds_read_b64_tr_b16 v[64:65], v242\n"
    "v_mfma_f32_16x16x32_bf16 v[4:7], v[112:115], v[96:99], v[4:7]\n"
    "ds_read_b64_tr_b16 v[66:67], v242 offset:1024\n"
    "v_mfma_f32_16x16x32_bf16 v[4:7], v[120:123], v[100:103], v[4:7]\n"
    "ds_read_b64_tr_b16 v[72:73], v242 offset:8192\n"
    "buffer_load_dwordx4 v[216:219], v247, s[20:23], s28 offen sc1\n"
    "v_mfma_f32_16x16x32_bf16 v[12:15], v[124:127], v[100:103], v[12:15]\n"
    "ds_read_b64_tr_b16 v[74:75], v242 offset:9216\n"
    "v_mfma_f32_16x16x32_bf16 v[12:15], v[116:119], v[96:99], v[12:15]\n"
    "ds_read_b128 v[96:99], v240\n"
    "v_mfma_f32_16x16x32_bf16 v[28:31], v[116:119], v[104:107], v[28:31]\n"
    "ds_read_b128 v[100:103], v241\n"
    "s_lshl_b32 s62, s45, 3\n"
    "s_add_u32 m0, s43, 16384\n"
    "v_mfma_f32_16x16x32_bf16 v[28:31], v[124:127], v[108:111], v[28:31]\n"
    "ds_read_b64_tr_b16 v[76:77], v242 offset:24576\n"
    "buffer_load_dwordx4 v248, s[16:19], s62 offen lds\n"
    "v_mfma_f32_16x16x32_bf16 v[20:23], v[120:123], v[108:111], v[20:23]\n"
    "ds_read_b64_tr_b16 v[78:79], v242 offset:25600\n"
    "v_mfma_f32_16x16x32_bf16 v[20:23], v[112:115], v[104:107], v[20:23]\n"
    "ds_read_b64_tr_b16 v[68:69], v242 offset:16384\n"
    "v_mfma_f32_16x16x32_bf16 v[36:39], v[112:115], v[80:83], v[36:39]\n"
    "ds_read_b64_tr_b16 v[70:71], v242 offset:17408\n"
    "v_mfma_f32_16x16x32_bf16 v[36:39], v[120:123], v[84:87], v[36:39]\n"
    "ds_read_b128 v[104:107], v240 offset:4096\n"
    "v_mfma_f32_16x16x32_bf16 v[44:47], v[124:127], v[84:87], v[44:47]\n"
    "ds_read_b128 v[108:111], v241 offset:4096\n"
    "buffer_load_dwordx4 v249, s[16:19], s62 offen offset:1024 lds\n"
    "v_mfma_f32_16x16x32_bf16 v[44:47], v[116:119], v[80:83], v[44:47]\n"
    "ds_read_b128 v[80:83], v240 offset:8192\n"
    "v_mfma_f32_16x16x32_bf16 v[60:63], v[116:119], v[88:91], v[60:63]\n"
    "ds_read_b128 v[84:87], v241 offset:8192\n"
    "v_mfma_f32_16x16x32_bf16 v[60:63], v[124:127], v[92:95], v[60:63]\n"
    "v_mfma_f32_16x16x32_bf16 v[52:55], v[120:123], v[92:95], v[52:55]\n"
    "s_lshl_b32 s62, s46, 3\n"
    "s_add_u32 m0, s43, 32768\n"
    "v_mfma_f32_16x16x32_bf16 v[52:55], v[112:115], v[88:91], v[52:55]\n"
    "buffer_load_dwordx4 v248, s[16:19], s62 offen lds\n"
    "s_waitcnt lgkmcnt(4)\n"
    "v_mfma_f32_16x16x32_bf16 v[0:3], v[64:67], v[96:99], v[0:3]\n"
    "ds_read_b128 v[88:91], v240 offset:12288\n"
    "v_mfma_f32_16x16x32_bf16 v[0:3], v[72:75], v[100:103], v[0:3]\n"
    "ds_read_b128 v[92:95], v241 offset:12288\n"
    "v_mfma_f32_16x16x32_bf16 v[8:11], v[76:79], v[100:103], v[8:11]\n"
    "ds_read_b64_tr_b16 v[112:113], v243\n"
    "v_mfma_f32_16x16x32_bf16 v[8:11], v[68:71], v[96:99], v[8:11]\n"
    "ds_read_b64_tr_b16 v[114:115], v243 offset:1024\n"
    "buffer_load_dwordx4 v249, s[16:19], s62 offen offset:1024 lds\n"
    "s_waitcnt lgkmcnt(6)\n"
    "v_mfma_f32_16x16x32_bf16 v[24:27], v[68:71], v[104:107], v[24:27]\n"
    "ds_read_b64_tr_b16 v[120:121], v243 offset:8192\n"
    "v_mfma_f32_16x16x32_bf16 v[24:27], v[76:79], v[108:111], v[24:27]\n"
    "ds_read_b64_tr_b16 v[122:123], v243 offset:9216\n"
    "v_mfma_f32_16x16x32_bf16 v[16:19], v[72:75], v[108:111], v[16:19]\n"
    "ds_read_b64_tr_b16 v[124:125], v243 offset:24576\n"
    "v_mfma_f32_16x16x32_bf16 v[16:19], v[64:67], v[104:107], v[16:19]\n"
    "ds_read_b64_tr_b16 v[126:127], v243 offset:25600\n"
    "s_lshl_b32 s62, s47, 3\n"
    "s_add_u32 m0, s43, 49152\n"
    "s_waitcnt lgkmcnt(8)\n"
    "v_mfma_f32_16x16x32_bf16 v[32:35], v[64:67], v[80:83], v[32:35]\n"
    "ds_read_b64_tr_b16 v[116:117], v243 offset:16384\n"
    "buffer_load_dwordx4 v248, s[16:19], s62 offen lds\n"
    "v_mfma_f32_16x16x32_bf16 v[32:35], v[72:75], v[84:87], v[32:35]\n"
    "ds_read_b64_tr_b16 v[118:119], v243 offset:17408\n"
    "v_mfma_f32_16x16x32_bf16 v[40:43], v[76:79], v[84:87], v[40:43]\n"
    "v_mfma_f32_16x16x32_bf16 v[40:43], v[68:71], v[80:83], v[40:43]\n"
    "s_waitcnt lgkmcnt(8)\n"
    "v_mfma_f32_16x16x32_bf16 v[56:59], v[68:71], v[88:91], v[56:59]\n"
    "v_add_u32 v240, 0xc000, v240\n"
    "v_mfma_f32_16x16x32_bf16 v[56:59], v[76:79], v[92:95], v[56:59]\n"
    "buffer_load_dwordx4 v249, s[16:19], s62 offen offset:1024 lds\n"
    "v_add_u32 v241, 0xc000, v241\n"
    "v_mfma_f32_16x16x32_bf16 v[48:51], v[72:75], v[92:95], v[48:51]\n"
    "v_add_u32 v242, 0xc000, v242\n"
    "v_mfma_f32_16x16x32_bf16 v[48:51], v[64:67], v[88:91], v[48:51]\n"
    "v_add_u32 v243, 0xc000, v243\n"
    "s_waitcnt vmcnt(7) lgkmcnt(0)\n"
    "s_barrier\n"
    "ds_read_b64_tr_b16 v[64:65], v242\n"
    "v_mfma_f32_16x16x32_bf16 v[4:7], v[112:115], v[96:99], v[4:7]\n"
    "ds_read_b64_tr_b16 v[66:67], v242 offset:1024\n"
    "v_xor_b32 v220, s36, v225\n"
    "v_bitop3_b32 v220, v227, s36, v220 bitop3:0xbe\n"
    "v_mfma_f32_16x16x32_bf16 v[4:7], v[120:123], v[100:103], v[4:7]\n"
    "ds_read_b64_tr_b16 v[72:73], v242 offset:8192\n"
    "v_bitop3_b32 v220, v229, s36, v220 bitop3:0xbe\n"
    "v_bitop3_b32 v220, v231, s36, v220 bitop3:0xbe\n"
    "v_mfma_f32_16x16x32_bf16 v[12:15], v[124:127], v[100:103], v[12:15]\n"
    "ds_read_b64_tr_b16 v[74:75], v242 offset:9216\n"
    "s_lshl_b32 s62, s48, 3\n"
    "s_add_u32 m0, s43, 65536\n"
    "v_mfma_f32_16x16x32_bf16 v[12:15], v[116:119], v[96:99], v[12:15]\n"
    "ds_read_b128 v[96:99], v240\n"
    "buffer_load_dwordx4 v248, s[16:19], s62 offen lds\n"
    "v_cmp_ne_u32 vcc, 0, v220\n"
    "v_mfma_f32_16x16x32_bf16 v[28:31], v[116:119], v[104:107], v[28:31]\n"
    "ds_read_b128 v[100:103], v241\n"
    "s_cbranch_vccnz Lsp0_stB\n"
    "Lret_stB:\n"
    "v_and_b32 v225, 0xffff0000, v224\n"
    "v_lshlrev_b32 v224, 16, v224\n"
    "v_mfma_f32_16x16x32_bf16 v[28:31], v[124:127], v[108:111], v[28:31]\n"
    "ds_read_b64_tr_b16 v[76:77], v242 offset:24576\n"
    "v_rcp_f32 v209, v225\n"
    "v_mfma_f32_16x16x32_bf16 v[20:23], v[120:123], v[108:111], v[20:23]\n"
    "ds_read_b64_tr_b16 v[78:79], v242 offset:25600\n"
    "v_mul_f32 v225, v225, v225\n"
    "v_mfma_f32_16x16x32_bf16 v[20:23], v[112:115], v[104:107], v[20:23]\n"
    "ds_read_b64_tr_b16 v[68:69], v242 offset:16384\n"
    "v_mul_f32 v208, 0xc0135761, v209\n"
    "v_mul_f32 v209, v209, v209\n"
    "v_mfma_f32_16x16x32_bf16 v[36:39], v[112:115], v[80:83], v[36:39]\n"
    "ds_read_b64_tr_b16 v[70:71], v242 offset:17408\n"
    "buffer_load_dwordx4 v249, s[16:19], s62 offen offset:1024 lds\n"
    "v_mul_f32 v209, v209, v208\n"
    "v_mfma_f32_16x16x32_bf16 v[36:39], v[120:123], v[84:87], v[36:39]\n"
    "ds_read_b128 v[104:107], v240 offset:4096\n"
    "v_mul_f32 v209, 0x3d372713, v209\n"
    "v_mfma_f32_16x16x32_bf16 v[44:47], v[124:127], v[84:87], v[44:47]\n"
    "ds_read_b128 v[108:111], v241 offset:4096\n"
    "v_and_b32 v227, 0xffff0000, v226\n"
    "v_lshlrev_b32 v226, 16, v226\n"
    "v_mfma_f32_16x16x32_bf16 v[44:47], v[116:119], v[80:83], v[44:47]\n"
    "ds_read_b128 v[80:83], v240 offset:8192\n"
    "v_rcp_f32 v211, v227\n"
    "v_mfma_f32_16x16x32_bf16 v[60:63], v[116:119], v[88:91], v[60:63]\n"
    "ds_read_b128 v[84:87], v241 offset:8192\n"
    "s_lshl_b32 s62, s49, 3\n"
    "s_add_u32 m0, s43, 81920\n"
    "ds_read_b64_tr_b16 v[144:145], v236\n"
    "ds_read_b64_tr_b16 v[146:147], v236 offset:2048\n"
    "v_mul_f32 v227, v227, v227\n"
    "v_mfma_f32_16x16x32_bf16 v[60:63], v[124:127], v[92:95], v[60:63]\n"
    "buffer_load_dwordx4 v248, s[16:19], s62 offen lds\n"
    "ds_read_b64_tr_b16 v[148:149], v237\n"
    "ds_read_b64_tr_b16 v[150:151], v237 offset:2048\n"
    "v_mul_f32 v210, 0xc0135761, v211\n"
    "v_mul_f32 v211, v211, v211\n"
    "v_mfma_f32_16x16x32_bf16 v[52:55], v[120:123], v[92:95], v[52:55]\n"
    "ds_read_b64_tr_b16 v[152:153], v238\n"
    "ds_read_b64_tr_b16 v[154:155], v238 offset:2048\n"
    "v_mul_f32 v211, v211, v210\n"
    "v_mfma_f32_16x16x32_bf16 v[52:55], v[112:115], v[88:91], v[52:55]\n"
    "ds_read_b64_tr_b16 v[156:157], v239\n"
    "ds_read_b64_tr_b16 v[158:159], v239 offset:2048\n"
    "v_mul_f32 v211, 0x3d372713, v211\n"
    "s_waitcnt lgkmcnt(12)\n"
    "v_mfma_f32_16x16x32_bf16 v[0:3], v[64:67], v[96:99], v[0:3]\n"
    "ds_read_b128 v[88:91], v240 offset:12288\n"
    "v_and_b32 v229, 0xffff0000, v228\n"
    "v_mfma_f32_16x16x32_bf16 v[0:3], v[72:75], v[100:103], v[0:3]\n"
    "ds_read_b128 v[92:95], v241 offset:12288\n"
    "v_lshlrev_b32 v228, 16, v228\n"
    "v_mfma_f32_16x16x32_bf16 v[8:11], v[76:79], v[100:103], v[8:11]\n"
    "ds_read_b64_tr_b16 v[112:113], v243\n"
    "v_rcp_f32 v213, v229\n"
    "v_mfma_f32_16x16x32_bf16 v[8:11], v[68:71], v[96:99], v[8:11]\n"
    "ds_read_b64_tr_b16 v[114:115], v243 offset:1024\n"
    "buffer_load_dwordx4 v249, s[16:19], s62 offen offset:1024 lds\n"
    "v_mul_f32 v229, v229, v229\n"
    "s_waitcnt lgkmcnt(14)\n"
    "v_mfma_f32_16x16x32_bf16 v[24:27], v[68:71], v[104:107], v[24:27]\n"
    "ds_read_b64_tr_b16 v[120:121], v243 offset:8192\n"
    "v_mul_f32 v212, 0xc0135761, v213\n"
    "v_mfma_f32_16x16x32_bf16 v[24:27], v[76:79], v[108:111], v[24:27]\n"
    "ds_read_b64_tr_b16 v[122:123], v243 offset:9216\n"
    "v_mul_f32 v213, v213, v213\n"
    "v_mfma_f32_16x16x32_bf16 v[16:19], v[72:75], v[108:111], v[16:19]\n"
    "ds_read_b64_tr_b16 v[124:125], v243 offset:24576\n"
    "v_mul_f32 v213, v213, v212\n"
    "s_and_b32 s3, s32, 3\n"
    "v_mfma_f32_16x16x32_bf16 v[16:19], v[64:67], v[104:107], v[16:19]\n"
    "ds_read_b64_tr_b16 v[126:127], v243 offset:25600\n"
    "s_lshl_b32 s62, s50, 3\n"
    "s_add_u32 m0, s43, 98304\n"
    "v_mul_f32 v213, 0x3d372713, v213\n"
    "s_lshl_b32 s3, s3, 12\n"
    "s_waitcnt lgkmcnt(14)\n"
    "v_mfma_f32_16x16x32_bf16 v[32:35], v[64:67], v[80:83], v[32:35]\n"
    "ds_read_b64_tr_b16 v[116:117], v243 offset:16384\n"
    "buffer_load_dwordx4 v248, s[16:19], s62 offen lds\n"
    "v_and_b32 v231, 0xffff0000, v230\n"
    "v_bfe_u32 v251, v250, 4, 4\n"
    "v_mfma_f32_16x16x32_bf16 v[32:35], v[72:75], v[84:87], v[32:35]\n"
    "ds_read_b64_tr_b16 v[118:119], v243 offset:17408\n"
    "s_waitcnt vmcnt(11)\n"
    "v_xor_b32 v220, s36, v217\n"
    "v_bitop3_b32 v220, v219, s36, v220 bitop3:0xbe\n"
    "v_lshlrev_b32 v230, 16, v230\n"
    "v_lshrrev_b32 v254, 9, v250\n"
    "v_mfma_f32_16x16x32_bf16 v[40:43], v[76:79], v[84:87], v[40:43]\n"
    "v_cmp_ne_u32 vcc, 0, v220\n"
    "v_rcp_f32 v215, v231\n"
    "v_lshl_or_b32 v251, v254, 4, v251\n"
    "v_mfma_f32_16x16x32_bf16 v[40:43], v[68:71], v[80:83], v[40:43]\n"
    "s_cbranch_vccnz Lsp0_cxB\n"
    "Lret_cxB:\n"
    "v_mul_f32 v231, v231, v231\n"
    "v_lshrrev_b32 v255, 4, v250\n"
    "s_waitcnt lgkmcnt(8)\n"
    "v_mfma_f32_16x16x32_bf16 v[56:59], v[68:71], v[88:91], v[56:59]\n"
    "v_permlane32_swap_b32 v216, v218\n"
    "v_mul_f32 v214, 0xc0135761, v215\n"
    "s_add_u32 s3, s3, 3328\n"
    "v_mfma_f32_16x16x32_bf16 v[56:59], v[76:79], v[92:95], v[56:59]\n"
    "v_add_f32 v216, v216, v218\n"
    "v_mov_b32 v218, v216\n"
    "buffer_load_dwordx4 v249, s[16:19], s62 offen offset:1024 lds\n"
    "v_mul_f32 v215, v215, v215\n"
    "v_lshl_add_u32 v251, v251, 2, s3\n"
    "v_mfma_f32_16x16x32_bf16 v[48:51], v[72:75], v[92:95], v[48:51]\n"
    "v_permlane16_swap_b32 v216, v218\n"
    "v_mul_f32 v215, v215, v214\n"
    "v_and_b32 v255, 48, v255\n"
    "v_mfma_f32_16x16x32_bf16 v[48:51], v[64:67], v[88:91], v[48:51]\n"
    "v_add_f32 v216, v216, v218\n"
    "v_mul_f32 v215, 0x3d372713, v215\n"
    "v_add_u32 v255, s3, v255\n"
    "v_xor_b32 v254, 0x1000, v255\n"
    "s_waitcnt vmcnt(6) lgkmcnt(0)\n"
    "s_barrier\n"
    "v_xor_b32 v252, 0x4000, v248\n"
    "v_add_u32 v253, 7168, v252\n"
    "ds_write_b32 v251, v216\n"
    "s_waitcnt lgkmcnt(0)\n"
    "s_barrier\n"
    "s_lshl3_add_u32 s62, s45, 917504\n"
    "s_add_u32 m0, s43, 112640\n"
    "v_mfma_f32_16x16x32_bf16 v[4:7], v[112:115], v[96:99], v[4:7]\n"
    "buffer_load_dwordx4 v252, s[16:19], s62 offen lds\n"
    "v_mfma_f32_16x16x32_bf16 v[4:7], v[120:123], v[100:103], v[4:7]\n"
    "ds_read_b64_tr_b16 v[64:65], v236 offset:16384\n"
    "ds_read_b64_tr_b16 v[66:67], v236 offset:18432\n"
    "ds_read_b64_tr_b16 v[68:69], v237 offset:16384\n"
    "ds_read_b64_tr_b16 v[70:71], v237 offset:18432\n"
    "v_mfma_f32_16x16x32_bf16 v[12:15], v[124:127], v[100:103], v[12:15]\n"
    "buffer_load_dwordx4 v253, s[16:19], s62 offen offset:1024 lds\n"
    "v_mfma_f32_16x16x32_bf16 v[12:15], v[116:119], v[96:99], v[12:15]\n"
    "ds_read_b64_tr_b16 v[72:73], v238 offset:16384\n"
    "ds_read_b64_tr_b16 v[74:75], v238 offset:18432\n"
    "ds_read_b64_tr_b16 v[76:77], v239 offset:16384\n"
    "ds_read_b64_tr_b16 v[78:79], v239 offset:18432\n"
    "s_add_u32 m0, s43, 114688\n"
    "v_mfma_f32_16x16x32_bf16 v[28:31], v[116:119], v[104:107], v[28:31]\n"
    "buffer_load_dwordx4 v248, s[16:19], s62 offen lds\n"
    "v_mfma_f32_16x16x32_bf16 v[28:31], v[124:127], v[108:111], v[28:31]\n"
    "ds_read_b64_tr_b16 v[220:221], v236 offset:49152\n"
    "ds_read_b64_tr_b16 v[222:223], v236 offset:51200\n"
    "ds_read_b64_tr_b16 v[240:241], v237 offset:49152\n"
    "ds_read_b64_tr_b16 v[242:243], v237 offset:51200\n"
    "v_mfma_f32_16x16x32_bf16 v[20:23], v[120:123], v[108:111], v[20:23]\n"
    "buffer_load_dwordx4 v249, s[16:19], s62 offen offset:1024 lds\n"
    "v_mfma_f32_16x16x32_bf16 v[20:23], v[112:115], v[104:107], v[20:23]\n"
    "ds_read_b128 v[96:99], v255\n"
    "ds_read_b128 v[100:103], v255 offset:64\n"
    "ds_read_b128 v[216:219], v254 offset:64\n"
    "ds_read_b128 v[104:107], v254\n"
    "ds_read_b64_tr_b16 v[244:245], v238 offset:49152\n"
    "ds_read_b64_tr_b16 v[246:247], v238 offset:51200\n"
    "ds_read_b64_tr_b16 v[232:233], v239 offset:49152\n"
    "ds_read_b64_tr_b16 v[234:235], v239 offset:51200\n"
    "s_lshl3_add_u32 s62, s46, 917504\n"
    "s_add_u32 m0, s43, 129024\n"
    "v_mfma_f32_16x16x32_bf16 v[36:39], v[112:115], v[80:83], v[36:39]\n"
    "buffer_load_dwordx4 v252, s[16:19], s62 offen lds\n"
    "v_mfma_f32_16x16x32_bf16 v[36:39], v[120:123], v[84:87], v[36:39]\n"
    "v_mfma_f32_16x16x32_bf16 v[44:47], v[124:127], v[84:87], v[44:47]\n"
    "buffer_load_dwordx4 v253, s[16:19], s62 offen offset:1024 lds\n"
    "v_mfma_f32_16x16x32_bf16 v[44:47], v[116:119], v[80:83], v[44:47]\n"
    "ds_read_b64_tr_b16 v[80:81], v236 offset:32768\n"
    "ds_read_b64_tr_b16 v[82:83], v236 offset:34816\n"
    "ds_read_b64_tr_b16 v[84:85], v237 offset:32768\n"
    "ds_read_b64_tr_b16 v[86:87], v237 offset:34816\n"
    "s_add_u32 m0, s43, 131072\n"
    "v_mfma_f32_16x16x32_bf16 v[60:63], v[116:119], v[88:91], v[60:63]\n"
    "buffer_load_dwordx4 v248, s[16:19], s62 offen lds\n"
    "v_mfma_f32_16x16x32_bf16 v[60:63], v[124:127], v[92:95], v[60:63]\n"
    "v_mfma_f32_16x16x32_bf16 v[52:55], v[120:123], v[92:95], v[52:55]\n"
    "v_mfma_f32_16x16x32_bf16 v[52:55], v[112:115], v[88:91], v[52:55]\n"
    "ds_read_b64_tr_b16 v[88:89], v238 offset:32768\n"
    "ds_read_b64_tr_b16 v[90:91], v238 offset:34816\n"
    "ds_read_b64_tr_b16 v[92:93], v239 offset:32768\n"
    "ds_read_b64_tr_b16 v[94:95], v239 offset:34816\n"
    "buffer_load_dwordx4 v249, s[16:19], s62 offen offset:1024 lds\n"
    "s_waitcnt lgkmcnt(0)\n"
    "s_setprio 1\n"
    "v_pk_fma_f32 v[0:1], v[224:225], v[96:97], v[0:1] op_sel_hi:[0,1,1] neg_lo:[1,0,0] neg_hi:[1,0,0]\n"
    "v_pk_fma_f32 v[2:3], v[224:225], v[98:99], v[2:3] op_sel_hi:[0,1,1] neg_lo:[1,0,0] neg_hi:[1,0,0]\n"
    "v_pk_fma_f32 v[4:5], v[224:225], v[104:105], v[4:5] op_sel_hi:[0,1,1] neg_lo:[1,0,0] neg_hi:[1,0,0]\n"
    "v_pk_fma_f32 v[6:7], v[224:225], v[106:107], v[6:7] op_sel_hi:[0,1,1] neg_lo:[1,0,0] neg_hi:[1,0,0]\n"
    "v_pk_mul_f32 v[112:113], v[0:1], v[0:1]\n"
    "v_pk_mul_f32 v[114:115], v[2:3], v[2:3]\n"
    "v_pk_mul_f32 v[116:117], v[4:5], v[4:5]\n"
    "v_pk_mul_f32 v[118:119], v[6:7], v[6:7]\n"
    "v_pk_fma_f32 v[112:113], v[208:209], v[112:113], v[208:209] op_sel:[1,0,0] op_sel_hi:[1,1,0]\n"
    "v_pk_fma_f32 v[114:115], v[208:209], v[114:115], v[208:209] op_sel:[1,0,0] op_sel_hi:[1,1,0]\n"
    "v_pk_fma_f32 v[116:117], v[208:209], v[116:117], v[208:209] op_sel:[1,0,0] op_sel_hi:[1,1,0]\n"
    "v_pk_fma_f32 v[118:119], v[208:209], v[118:119], v[208:209] op_sel:[1,0,0] op_sel_hi:[1,1,0]\n"
    "v_pk_mul_f32 v[112:113], v[112:113], v[0:1]\n"
    "v_pk_mul_f32 v[114:115], v[114:115], v[2:3]\n"
    "v_pk_mul_f32 v[116:117], v[116:117], v[4:5]\n"
    "v_pk_mul_f32 v[118:119], v[118:119], v[6:7]\n"
    "v_exp_f32 v112, v112\n"
    "v_pk_fma_f32 v[8:9], v[224:225], v[100:101], v[8:9] op_sel_hi:[0,1,1] neg_lo:[1,0,0] neg_hi:[1,0,0]\n"
    "v_exp_f32 v113, v113\n"
    "v_exp_f32 v114, v114\n"
    "v_pk_fma_f32 v[10:11], v[224:225], v[102:103], v[10:11] op_sel_hi:[0,1,1] neg_lo:[1,0,0] neg_hi:[1,0,0]\n"
    "v_exp_f32 v115, v115\n"
    "v_exp_f32 v116, v116\n"
    "v_pk_fma_f32 v[12:13], v[224:225], v[216:217], v[12:13] op_sel_hi:[0,1,1] neg_lo:[1,0,0] neg_hi:[1,0,0]\n"
    "v_exp_f32 v117, v117\n"
    "v_exp_f32 v118, v118\n"
    "v_pk_fma_f32 v[14:15], v[224:225], v[218:219], v[14:15] op_sel_hi:[0,1,1] neg_lo:[1,0,0] neg_hi:[1,0,0]\n"
    "v_exp_f32 v119, v119\n"
    "v_pk_fma_f32 v[112:113], v[224:225], v[112:113], v[224:225] op_sel:[1,0,1]\n"
    "v_pk_fma_f32 v[114:115], v[224:225], v[114:115], v[224:225] op_sel:[1,0,1]\n"
    "v_pk_fma_f32 v[116:117], v[224:225], v[116:117], v[224:225] op_sel:[1,0,1]\n"
    "v_pk_fma_f32 v[118:119], v[224:225], v[118:119], v[224:225] op_sel:[1,0,1]\n"
    "v_rcp_f32 v112, v112\n"
    "v_pk_mul_f32 v[8:9], v[8:9], v[0:1]\n"
    "v_rcp_f32 v113, v113\n"
    "v_rcp_f32 v114, v114\n"
    "v_pk_mul_f32 v[10:11], v[10:11], v[2:3]\n"
    "v_rcp_f32 v115, v115\n"
    "v_rcp_f32 v116, v116\n"
    "v_pk_mul_f32 v[12:13], v[12:13], v[4:5]\n"
    "v_rcp_f32 v117, v117\n"
    "v_rcp_f32 v118, v118\n"
    "v_pk_mul_f32 v[14:15], v[14:15], v[6:7]\n"
    "v_rcp_f32 v119, v119\n"
    "v_pk_mul_f32 v[8:9], v[8:9], v[112:113]\n"
    "v_pk_mul_f32 v[10:11], v[10:11], v[114:115]\n"
    "v_pk_mul_f32 v[12:13], v[12:13], v[116:117]\n"
    "v_pk_mul_f32 v[14:15], v[14:15], v[118:119]\n"
    "v_cvt_pk_bf16_f32 v0, v8, v9\n"
    "v_cvt_pk_bf16_f32 v1, v10, v11\n"
    "v_cvt_pk_bf16_f32 v2, v12, v13\n"
    "v_cvt_pk_bf16_f32 v3, v14, v15\n"
    "buffer_store_dwordx4 v[0:3], v250, s[20:23], s51 offen sc1\n"
    "v_pk_fma_f32 v[16:17], v[226:227], v[96:97], v[16:17] op_sel_hi:[0,1,1] neg_lo:[1,0,0] neg_hi:[1,0,0]\n"
    "v_pk_fma_f32 v[18:19], v[226:227], v[98:99], v[18:19] op_sel_hi:[0,1,1] neg_lo:[1,0,0] neg_hi:[1,0,0]\n"
    "v_pk_fma_f32 v[20:21], v[226:227], v[104:105], v[20:21] op_sel_hi:[0,1,1] neg_lo:[1,0,0] neg_hi:[1,0,0]\n"
    "v_pk_fma_f32 v[22:23], v[226:227], v[106:107], v[22:23] op_sel_hi:[0,1,1] neg_lo:[1,0,0] neg_hi:[1,0,0]\n"
    "v_pk_mul_f32 v[112:113], v[16:17], v[16:17]\n"
    "v_pk_mul_f32 v[114:115], v[18:19], v[18:19]\n"
    "v_pk_mul_f32 v[116:117], v[20:21], v[20:21]\n"
    "v_pk_mul_f32 v[118:119], v[22:23], v[22:23]\n"
    "v_pk_fma_f32 v[112:113], v[210:211], v[112:113], v[210:211] op_sel:[1,0,0] op_sel_hi:[1,1,0]\n"
    "v_pk_fma_f32 v[114:115], v[210:211], v[114:115], v[210:211] op_sel:[1,0,0] op_sel_hi:[1,1,0]\n"
    "v_pk_fma_f32 v[116:117], v[210:211], v[116:117], v[210:211] op_sel:[1,0,0] op_sel_hi:[1,1,0]\n"
    "v_pk_fma_f32 v[118:119], v[210:211], v[118:119], v[210:211] op_sel:[1,0,0] op_sel_hi:[1,1,0]\n"
    "v_pk_mul_f32 v[112:113], v[112:113], v[16:17]\n"
    "v_pk_mul_f32 v[114:115], v[114:115], v[18:19]\n"
    "v_pk_mul_f32 v[116:117], v[116:117], v[20:21]\n"
    "v_pk_mul_f32 v[118:119], v[118:119], v[22:23]\n"
    "v_exp_f32 v112, v112\n"
    "v_pk_fma_f32 v[24:25], v[226:227], v[100:101], v[24:25] op_sel_hi:[0,1,1] neg_lo:[1,0,0] neg_hi:[1,0,0]\n"
    "v_exp_f32 v113, v113\n"
    "v_exp_f32 v114, v114\n"
    "v_pk_fma_f32 v[26:27], v[226:227], v[102:103], v[26:27] op_sel_hi:[0,1,1] neg_lo:[1,0,0] neg_hi:[1,0,0]\n"
    "v_exp_f32 v115, v115\n"
    "v_exp_f32 v116, v116\n"
    "v_pk_fma_f32 v[28:29], v[226:227], v[216:217], v[28:29] op_sel_hi:[0,1,1] neg_lo:[1,0,0] neg_hi:[1,0,0]\n"
    "v_exp_f32 v117, v117\n"
    "v_exp_f32 v118, v118\n"
    "v_pk_fma_f32 v[30:31], v[226:227], v[218:219], v[30:31] op_sel_hi:[0,1,1] neg_lo:[1,0,0] neg_hi:[1,0,0]\n"
    "v_exp_f32 v119, v119\n"
    "v_pk_fma_f32 v[112:113], v[226:227], v[112:113], v[226:227] op_sel:[1,0,1]\n"
    "v_pk_fma_f32 v[114:115], v[226:227], v[114:115], v[226:227] op_sel:[1,0,1]\n"
    "v_pk_fma_f32 v[116:117], v[226:227], v[116:117], v[226:227] op_sel:[1,0,1]\n"
    "v_pk_fma_f32 v[118:119], v[226:227], v[118:119], v[226:227] op_sel:[1,0,1]\n"
    "v_rcp_f32 v112, v112\n"
    "v_pk_mul_f32 v[24:25], v[24:25], v[16:17]\n"
    "v_rcp_f32 v113, v113\n"
    "v_rcp_f32 v114, v114\n"
    "v_pk_mul_f32 v[26:27], v[26:27], v[18:19]\n"
    "v_rcp_f32 v115, v115\n"
    "v_rcp_f32 v116, v116\n"
    "v_pk_mul_f32 v[28:29], v[28:29], v[20:21]\n"
    "v_rcp_f32 v117, v117\n"
    "v_rcp_f32 v118, v118\n"
    "v_pk_mul_f32 v[30:31], v[30:31], v[22:23]\n"
    "v_rcp_f32 v119, v119\n"
    "v_pk_mul_f32 v[24:25], v[24:25], v[112:113]\n"
    "v_pk_mul_f32 v[26:27], v[26:27], v[114:115]\n"
    "v_pk_mul_f32 v[28:29], v[28:29], v[116:117]\n"
    "v_pk_mul_f32 v[30:31], v[30:31], v[118:119]\n"
    "v_cvt_pk_bf16_f32 v16, v24, v25\n"
    "v_cvt_pk_bf16_f32 v17, v26, v27\n"
    "v_cvt_pk_bf16_f32 v18, v28, v29\n"
    "v_cvt_pk_bf16_f32 v19, v30, v31\n"
    "buffer_store_dwordx4 v[16:19], v250, s[20:23], s51 offen offset:1024 sc1\n"
    "v_pk_fma_f32 v[32:33], v[228:229], v[96:97], v[32:33] op_sel_hi:[0,1,1] neg_lo:[1,0,0] neg_hi:[1,0,0]\n"
    "v_pk_fma_f32 v[34:35], v[228:229], v[98:99], v[34:35] op_sel_hi:[0,1,1] neg_lo:[1,0,0] neg_hi:[1,0,0]\n"
    "v_pk_fma_f32 v[36:37], v[228:229], v[104:105], v[36:37] op_sel_hi:[0,1,1] neg_lo:[1,0,0] neg_hi:[1,0,0]\n"
    "v_pk_fma_f32 v[38:39], v[228:229], v[106:107], v[38:39] op_sel_hi:[0,1,1] neg_lo:[1,0,0] neg_hi:[1,0,0]\n"
    "v_pk_mul_f32 v[112:113], v[32:33], v[32:33]\n"
    "v_pk_mul_f32 v[114:115], v[34:35], v[34:35]\n"
    "v_pk_mul_f32 v[116:117], v[36:37], v[36:37]\n"
    "v_pk_mul_f32 v[118:119], v[38:39], v[38:39]\n"
    "v_pk_fma_f32 v[112:113], v[212:213], v[112:113], v[212:213] op_sel:[1,0,0] op_sel_hi:[1,1,0]\n"
    "v_pk_fma_f32 v[114:115], v[212:213], v[114:115], v[212:213] op_sel:[1,0,0] op_sel_hi:[1,1,0]\n"
    "v_pk_fma_f32 v[116:117], v[212:213], v[116:117], v[212:213] op_sel:[1,0,0] op_sel_hi:[1,1,0]\n"
    "v_pk_fma_f32 v[118:119], v[212:213], v[118:119], v[212:213] op_sel:[1,0,0] op_sel_hi:[1,1,0]\n"
    "v_pk_mul_f32 v[112:113], v[112:113], v[32:33]\n"
    "v_pk_mul_f32 v[114:115], v[114:115], v[34:35]\n"
    "v_pk_mul_f32 v[116:117], v[116:117], v[36:37]\n"
    "v_pk_mul_f32 v[118:119], v[118:119], v[38:39]\n"
    "v_exp_f32 v112, v112\n"
    "v_pk_fma_f32 v[40:41], v[228:229], v[100:101], v[40:41] op_sel_hi:[0,1,1] neg_lo:[1,0,0] neg_hi:[1,0,0]\n"
    "v_exp_f32 v113, v113\n"
    "v_exp_f32 v114, v114\n"
    "v_pk_fma_f32 v[42:43], v[228:229], v[102:103], v[42:43] op_sel_hi:[0,1,1] neg_lo:[1,0,0] neg_hi:[1,0,0]\n"
    "v_exp_f32 v115, v115\n"
    "v_exp_f32 v116, v116\n"
    "v_pk_fma_f32 v[44:45], v[228:229], v[216:217], v[44:45] op_sel_hi:[0,1,1] neg_lo:[1,0,0] neg_hi:[1,0,0]\n"
    "v_exp_f32 v117, v117\n"
    "v_exp_f32 v118, v118\n"
    "v_pk_fma_f32 v[46:47], v[228:229], v[218:219], v[46:47] op_sel_hi:[0,1,1] neg_lo:[1,0,0] neg_hi:[1,0,0]\n"
    "v_exp_f32 v119, v119\n"
    "v_pk_fma_f32 v[112:113], v[228:229], v[112:113], v[228:229] op_sel:[1,0,1]\n"
    "v_pk_fma_f32 v[114:115], v[228:229], v[114:115], v[228:229] op_sel:[1,0,1]\n"
    "v_pk_fma_f32 v[116:117], v[228:229], v[116:117], v[228:229] op_sel:[1,0,1]\n"
    "v_pk_fma_f32 v[118:119], v[228:229], v[118:119], v[228:229] op_sel:[1,0,1]\n"
    "v_rcp_f32 v112, v112\n"
    "v_pk_mul_f32 v[40:41], v[40:41], v[32:33]\n"
    "v_rcp_f32 v113, v113\n"
    "v_rcp_f32 v114, v114\n"
    "v_pk_mul_f32 v[42:43], v[42:43], v[34:35]\n"
    "v_rcp_f32 v115, v115\n"
    "v_rcp_f32 v116, v116\n"
    "v_pk_mul_f32 v[44:45], v[44:45], v[36:37]\n"
    "v_rcp_f32 v117, v117\n"
    "v_rcp_f32 v118, v118\n"
    "v_pk_mul_f32 v[46:47], v[46:47], v[38:39]\n"
    "v_rcp_f32 v119, v119\n"
    "v_pk_mul_f32 v[40:41], v[40:41], v[112:113]\n"
    "v_pk_mul_f32 v[42:43], v[42:43], v[114:115]\n"
    "v_pk_mul_f32 v[44:45], v[44:45], v[116:117]\n"
    "v_pk_mul_f32 v[46:47], v[46:47], v[118:119]\n"
    "v_cvt_pk_bf16_f32 v32, v40, v41\n"
    "v_cvt_pk_bf16_f32 v33, v42, v43\n"
    "v_cvt_pk_bf16_f32 v34, v44, v45\n"
    "v_cvt_pk_bf16_f32 v35, v46, v47\n"
    "buffer_store_dwordx4 v[32:35], v250, s[20:23], s51 offen offset:2048 sc1\n"
    "v_pk_fma_f32 v[48:49], v[230:231], v[96:97], v[48:49] op_sel_hi:[0,1,1] neg_lo:[1,0,0] neg_hi:[1,0,0]\n"
    "v_pk_fma_f32 v[50:51], v[230:231], v[98:99], v[50:51] op_sel_hi:[0,1,1] neg_lo:[1,0,0] neg_hi:[1,0,0]\n"
    "v_pk_fma_f32 v[52:53], v[230:231], v[104:105], v[52:53] op_sel_hi:[0,1,1] neg_lo:[1,0,0] neg_hi:[1,0,0]\n"
    "v_pk_fma_f32 v[54:55], v[230:231], v[106:107], v[54:55] op_sel_hi:[0,1,1] neg_lo:[1,0,0] neg_hi:[1,0,0]\n"
    "v_pk_mul_f32 v[112:113], v[48:49], v[48:49]\n"
    "v_pk_mul_f32 v[114:115], v[50:51], v[50:51]\n"
    "v_pk_mul_f32 v[116:117], v[52:53], v[52:53]\n"
    "v_pk_mul_f32 v[118:119], v[54:55], v[54:55]\n"
    "v_pk_fma_f32 v[112:113], v[214:215], v[112:113], v[214:215] op_sel:[1,0,0] op_sel_hi:[1,1,0]\n"
    "v_pk_fma_f32 v[114:115], v[214:215], v[114:115], v[214:215] op_sel:[1,0,0] op_sel_hi:[1,1,0]\n"
    "v_pk_fma_f32 v[116:117], v[214:215], v[116:117], v[214:215] op_sel:[1,0,0] op_sel_hi:[1,1,0]\n"
    "v_pk_fma_f32 v[118:119], v[214:215], v[118:119], v[214:215] op_sel:[1,0,0] op_sel_hi:[1,1,0]\n"
    "v_pk_mul_f32 v[112:113], v[112:113], v[48:49]\n"
    "v_pk_mul_f32 v[114:115], v[114:115], v[50:51]\n"
    "v_pk_mul_f32 v[116:117], v[116:117], v[52:53]\n"
    "v_pk_mul_f32 v[118:119], v[118:119], v[54:55]\n"
    "v_exp_f32 v112, v112\n"
    "v_pk_fma_f32 v[56:57], v[230:231], v[100:101], v[56:57] op_sel_hi:[0,1,1] neg_lo:[1,0,0] neg_hi:[1,0,0]\n"
    "v_exp_f32 v113, v113\n"
    "v_exp_f32 v114, v114\n"
    "v_pk_fma_f32 v[58:59], v[230:231], v[102:103], v[58:59] op_sel_hi:[0,1,1] neg_lo:[1,0,0] neg_hi:[1,0,0]\n"
    "v_exp_f32 v115, v115\n"
    "v_exp_f32 v116, v116\n"
    "v_pk_fma_f32 v[60:61], v[230:231], v[216:217], v[60:61] op_sel_hi:[0,1,1] neg_lo:[1,0,0] neg_hi:[1,0,0]\n"
    "v_exp_f32 v117, v117\n"
    "v_exp_f32 v118, v118\n"
    "v_pk_fma_f32 v[62:63], v[230:231], v[218:219], v[62:63] op_sel_hi:[0,1,1] neg_lo:[1,0,0] neg_hi:[1,0,0]\n"
    "v_exp_f32 v119, v119\n"
    "v_pk_fma_f32 v[112:113], v[230:231], v[112:113], v[230:231] op_sel:[1,0,1]\n"
    "v_pk_fma_f32 v[114:115], v[230:231], v[114:115], v[230:231] op_sel:[1,0,1]\n"
    "v_pk_fma_f32 v[116:117], v[230:231], v[116:117], v[230:231] op_sel:[1,0,1]\n"
    "v_pk_fma_f32 v[118:119], v[230:231], v[118:119], v[230:231] op_sel:[1,0,1]\n"
    "v_rcp_f32 v112, v112\n"
    "v_pk_mul_f32 v[56:57], v[56:57], v[48:49]\n"
    "v_rcp_f32 v113, v113\n"
    "v_rcp_f32 v114, v114\n"
    "v_pk_mul_f32 v[58:59], v[58:59], v[50:51]\n"
    "v_rcp_f32 v115, v115\n"
    "v_rcp_f32 v116, v116\n"
    "v_pk_mul_f32 v[60:61], v[60:61], v[52:53]\n"
    "v_rcp_f32 v117, v117\n"
    "v_rcp_f32 v118, v118\n"
    "v_pk_mul_f32 v[62:63], v[62:63], v[54:55]\n"
    "v_rcp_f32 v119, v119\n"
    "v_pk_mul_f32 v[56:57], v[56:57], v[112:113]\n"
    "v_pk_mul_f32 v[58:59], v[58:59], v[114:115]\n"
    "v_pk_mul_f32 v[60:61], v[60:61], v[116:117]\n"
    "v_pk_mul_f32 v[62:63], v[62:63], v[118:119]\n"
    "v_cvt_pk_bf16_f32 v48, v56, v57\n"
    "v_cvt_pk_bf16_f32 v49, v58, v59\n"
    "v_cvt_pk_bf16_f32 v50, v60, v61\n"
    "v_cvt_pk_bf16_f32 v51, v62, v63\n"
    "buffer_store_dwordx4 v[48:51], v250, s[20:23], s51 offen offset:3072 sc1\n"
    "v_mov_b32 v254, s43\n"
    "v_mov_b32 v255, 1\n"
    "v_mov_b32 v208, s36\n"
    "v_mov_b32 v209, s37\n"
    "v_and_b32 v211, 0x70, v250\n"
    "v_min_u32 v211, 0x60, v211\n"
    "v_lshlrev_b32 v211, 1, v211\n"
    "s_lshl_b32 s3, s56, 12\n"
    "s_lshr_b32 s60, s32, 2\n"
    "s_lshl_b32 s60, s60, 11\n"
    "s_add_u32 s3, s3, s60\n"
    "s_add_u32 s3, s3, 8388608\n"
    "v_add_u32 v210, s3, v250\n"
    "s_lshl_b32 s56, s56, 5\n"
    "s_add_u32 s56, s56, 43008\n"
    "s_mov_b32 s38, 0\n"
    "s_and_b32 s60, s32, 3\n"
    "s_lshl_b32 s3, s60, 6\n"
    "v_lshrrev_b32 v4, 4, v250\n"
    "v_and_b32 v5, 48, v4\n"
    "v_add_u32 v5, s3, v5\n"
    "v_and_b32 v4, 15, v4\n"
    "v_add_u32 v4, s31, v4\n"
    "v_cvt_f32_u32 v5, v5\n"
    "v_cvt_f32_u32 v4, v4\n"
    "v_lshlrev_b32 v6, 27, v250\n"
    "v_xor_b32 v6, 0x3f803f80, v6\n"
    "s_cmp_eq_u32 s60, 0\n"
    "s_cselect_b32 s28, 0xffff, 0\n"
    "s_mov_b32 s29, 0\n"
    "v_mul_f32 v4, 0x3b000000, v4\n"
    "v_add_f32 v12, 0x00000000, v4\n"
    "v_add_f32 v20, 0x3d000000, v4\n"
    "v_add_f32 v28, 0x3d800000, v4\n"
    "v_add_f32 v36, 0x3dc00000, v4\n"
    "v_mul_f32 v13, v12, v5\n"
    "v_mul_f32 v21, v20, v5\n"
    "v_mul_f32 v29, v28, v5\n"
    "v_mul_f32 v37, v36, v5\n"
    "v_cos_f32 v14, v12\n"
    "v_cos_f32 v22, v20\n"
    "v_cos_f32 v30, v28\n"
    "v_cos_f32 v38, v36\n"
    "v_fract_f32 v13, v13\n"
    "v_fract_f32 v21, v21\n"
    "ds_read_b32 v228, v254 offset:1548\n"
    "v_fract_f32 v29, v29\n"
    "v_fract_f32 v37, v37\n"
    "v_add_f32 v12, v12, v13\n"
    "v_add_f32 v20, v20, v21\n"
    "v_add_f32 v28, v28, v29\n"
    "v_add_f32 v36, v36, v37\n"
    "v_cos_f32 v8, v13\n"
    "v_cos_f32 v16, v21\n"
    "v_cos_f32 v24, v29\n"
    "v_cos_f32 v32, v37\n"
    "v_sin_f32_e64 v9, -v13\n"
    "v_sin_f32_e64 v17, -v21\n"
    "v_sin_f32_e64 v25, -v29\n"
    "v_sin_f32_e64 v33, -v37\n"
    "v_cos_f32 v10, v12\n"
    "v_cos_f32 v18, v20\n"
    "v_cos_f32 v26, v28\n"
    "v_cos_f32 v34, v36\n"
    "v_sin_f32_e64 v11, -v12\n"
    "v_sin_f32_e64 v19, -v20\n"
    "v_sin_f32_e64 v27, -v28\n"
    "v_sin_f32_e64 v35, -v36\n"
    "v_add_f32 v14, v14, v14\n"
    "v_add_f32 v22, v22, v22\n"
    "v_add_f32 v30, v30, v30\n"
    "v_add_f32 v38, v38, v38\n"
    "v_cvt_pk_bf16_f32 v160, v8, v9\n"
    "v_cvt_pk_bf16_f32 v176, v16, v17\n"
    "v_cvt_pk_bf16_f32 v192, v24, v25\n"
    "v_cvt_pk_bf16_f32 v96, v32, v33\n"
    "v_pk_fma_f32 v[8:9], v[14:15], v[10:11], v[8:9] op_sel_hi:[0,1,1] neg_lo:[0,0,1] neg_hi:[0,0,1]\n"
    "v_pk_fma_f32 v[16:17], v[22:23], v[18:19], v[16:17] op_sel_hi:[0,1,1] neg_lo:[0,0,1] neg_hi:[0,0,1]\n"
    "v_pk_fma_f32 v[24:25], v[30:31], v[26:27], v[24:25] op_sel_hi:[0,1,1] neg_lo:[0,0,1] neg_hi:[0,0,1]\n"
    "v_pk_fma_f32 v[32:33], v[38:39], v[34:35], v[32:33] op_sel_hi:[0,1,1] neg_lo:[0,0,1] neg_hi:[0,0,1]\n"
    "v_cvt_pk_bf16_f32 v161, v10, v11\n"
    "v_cvt_pk_bf16_f32 v177, v18, v19\n"
    "v_cvt_pk_bf16_f32 v193, v26, v27\n"
    "v_cvt_pk_bf16_f32 v97, v34, v35\n"
    "v_pk_fma_f32 v[10:11], v[14:15], v[8:9], v[10:11] op_sel_hi:[0,1,1] neg_lo:[0,0,1] neg_hi:[0,0,1]\n"
    "v_pk_fma_f32 v[18:19], v[22:23], v[16:17], v[18:19] op_sel_hi:[0,1,1] neg_lo:[0,0,1] neg_hi:[0,0,1]\n"
    "v_pk_fma_f32 v[26:27], v[30:31], v[24:25], v[26:27] op_sel_hi:[0,1,1] neg_lo:[0,0,1] neg_hi:[0,0,1]\n"
    "v_pk_fma_f32 v[34:35], v[38:39], v[32:33], v[34:35] op_sel_hi:[0,1,1] neg_lo:[0,0,1] neg_hi:[0,0,1]\n"
    "v_cvt_pk_bf16_f32 v162, v8, v9\n"
    "s_waitcnt lgkmcnt(0)\n"
    "v_cmp_eq_u32 vcc, 0, v228\n"
    "s_nop 0\n"
    "s_cbranch_vccnz Lls_B12\n"
    "Llr_B12:\n"
    "s_waitcnt vmcnt(0)\n"
    "s_mov_b64 exec, 1\n"
    "buffer_store_dwordx2 v[208:209], off, s[20:23], s52 sc1\n"
    "s_mov_b64 exec, -1\n"
    "s_setprio 0\n"
    "v_cvt_pk_bf16_f32 v178, v16, v17\n"
    "v_cvt_pk_bf16_f32 v194, v24, v25\n"
    "v_cvt_pk_bf16_f32 v98, v32, v33\n"
    "s_lshl3_add_u32 s62, s47, 917504\n"
    "s_add_u32 m0, s43, 14336\n"
    "v_pk_fma_f32 v[8:9], v[14:15], v[10:11], v[8:9] op_sel_hi:[0,1,1] neg_lo:[0,0,1] neg_hi:[0,0,1]\n"
    "buffer_load_dwordx4 v252, s[16:19], s62 offen lds\n"
    "v_pk_fma_f32 v[16:17], v[22:23], v[18:19], v[16:17] op_sel_hi:[0,1,1] neg_lo:[0,0,1] neg_hi:[0,0,1]\n"
    "v_pk_fma_f32 v[24:25], v[30:31], v[26:27], v[24:25] op_sel_hi:[0,1,1] neg_lo:[0,0,1] neg_hi:[0,0,1]\n"
    "v_pk_fma_f32 v[32:33], v[38:39], v[34:35], v[32:33] op_sel_hi:[0,1,1] neg_lo:[0,0,1] neg_hi:[0,0,1]\n"
    "v_cvt_pk_bf16_f32 v163, v10, v11\n"
    "v_cvt_pk_bf16_f32 v179, v18, v19\n"
    "v_cvt_pk_bf16_f32 v195, v26, v27\n"
    "v_cvt_pk_bf16_f32 v99, v34, v35\n"
    "buffer_load_dwordx4 v253, s[16:19], s62 offen offset:1024 lds\n"
    "v_pk_fma_f32 v[10:11], v[14:15], v[8:9], v[10:11] op_sel_hi:[0,1,1] neg_lo:[0,0,1] neg_hi:[0,0,1]\n"
    "v_pk_fma_f32 v[18:19], v[22:23], v[16:17], v[18:19] op_sel_hi:[0,1,1] neg_lo:[0,0,1] neg_hi:[0,0,1]\n"
    "v_pk_fma_f32 v[26:27], v[30:31], v[24:25], v[26:27] op_sel_hi:[0,1,1] neg_lo:[0,0,1] neg_hi:[0,0,1]\n"
    "v_pk_fma_f32 v[34:35], v[38:39], v[32:33], v[34:35] op_sel_hi:[0,1,1] neg_lo:[0,0,1] neg_hi:[0,0,1]\n"
    "v_cvt_pk_bf16_f32 v164, v8, v9\n"
    "s_add_u32 m0, s43, 16384\n"
    "v_cvt_pk_bf16_f32 v180, v16, v17\n"
    "buffer_load_dwordx4 v248, s[16:19], s62 offen lds\n"
    "v_cvt_pk_bf16_f32 v196, v24, v25\n"
    "v_cvt_pk_bf16_f32 v100, v32, v33\n"
    "v_pk_fma_f32 v[8:9], v[14:15], v[10:11], v[8:9] op_sel_hi:[0,1,1] neg_lo:[0,0,1] neg_hi:[0,0,1]\n"
    "v_pk_fma_f32 v[16:17], v[22:23], v[18:19], v[16:17] op_sel_hi:[0,1,1] neg_lo:[0,0,1] neg_hi:[0,0,1]\n"
    "v_pk_fma_f32 v[24:25], v[30:31], v[26:27], v[24:25] op_sel_hi:[0,1,1] neg_lo:[0,0,1] neg_hi:[0,0,1]\n"
    "v_pk_fma_f32 v[32:33], v[38:39], v[34:35], v[32:33] op_sel_hi:[0,1,1] neg_lo:[0,0,1] neg_hi:[0,0,1]\n"
    "v_cvt_pk_bf16_f32 v165, v10, v11\n"
    "buffer_load_dwordx4 v249, s[16:19], s62 offen offset:1024 lds\n"
    "v_cvt_pk_bf16_f32 v181, v18, v19\n"
    "v_cvt_pk_bf16_f32 v197, v26, v27\n"
    "v_cvt_pk_bf16_f32 v101, v34, v35\n"
    "v_pk_fma_f32 v[10:11], v[14:15], v[8:9], v[10:11] op_sel_hi:[0,1,1] neg_lo:[0,0,1] neg_hi:[0,0,1]\n"
    "v_pk_fma_f32 v[18:19], v[22:23], v[16:17], v[18:19] op_sel_hi:[0,1,1] neg_lo:[0,0,1] neg_hi:[0,0,1]\n"
    "v_pk_fma_f32 v[26:27], v[30:31], v[24:25], v[26:27] op_sel_hi:[0,1,1] neg_lo:[0,0,1] neg_hi:[0,0,1]\n"
    "s_lshl3_add_u32 s62, s48, 917504\n"
    "s_add_u32 m0, s43, 30720\n"
    "v_pk_fma_f32 v[34:35], v[38:39], v[32:33], v[34:35] op_sel_hi:[0,1,1] neg_lo:[0,0,1] neg_hi:[0,0,1]\n"
    "buffer_load_dwordx4 v252, s[16:19], s62 offen lds\n"
    "v_cvt_pk_bf16_f32 v166, v8, v9\n"
    "v_cvt_pk_bf16_f32 v182, v16, v17\n"
    "v_cvt_pk_bf16_f32 v198, v24, v25\n"
    "v_cvt_pk_bf16_f32 v102, v32, v33\n"
    "v_pk_fma_f32 v[8:9], v[14:15], v[10:11], v[8:9] op_sel_hi:[0,1,1] neg_lo:[0,0,1] neg_hi:[0,0,1]\n"
    "v_pk_fma_f32 v[16:17], v[22:23], v[18:19], v[16:17] op_sel_hi:[0,1,1] neg_lo:[0,0,1] neg_hi:[0,0,1]\n"
    "v_pk_fma_f32 v[24:25], v[30:31], v[26:27], v[24:25] op_sel_hi:[0,1,1] neg_lo:[0,0,1] neg_hi:[0,0,1]\n"
    "buffer_load_dwordx4 v253, s[16:19], s62 offen offset:1024 lds\n"
    "v_pk_fma_f32 v[32:33], v[38:39], v[34:35], v[32:33] op_sel_hi:[0,1,1] neg_lo:[0,0,1] neg_hi:[0,0,1]\n"
    "v_cvt_pk_bf16_f32 v167, v10, v11\n"
    "v_cvt_pk_bf16_f32 v183, v18, v19\n"
    "v_cvt_pk_bf16_f32 v199, v26, v27\n"
    "v_cvt_pk_bf16_f32 v103, v34, v35\n"
    "s_add_u32 m0, s43, 32768\n"
    "v_pk_fma_f32 v[10:11], v[14:15], v[8:9], v[10:11] op_sel_hi:[0,1,1] neg_lo:[0,0,1] neg_hi:[0,0,1]\n"
    "buffer_load_dwordx4 v248, s[16:19], s62 offen lds\n"
    "v_pk_fma_f32 v[18:19], v[22:23], v[16:17], v[18:19] op_sel_hi:[0,1,1] neg_lo:[0,0,1] neg_hi:[0,0,1]\n"
    "v_pk_fma_f32 v[26:27], v[30:31], v[24:25], v[26:27] op_sel_hi:[0,1,1] neg_lo:[0,0,1] neg_hi:[0,0,1]\n"
    "v_pk_fma_f32 v[34:35], v[38:39], v[32:33], v[34:35] op_sel_hi:[0,1,1] neg_lo:[0,0,1] neg_hi:[0,0,1]\n"
    "v_cvt_pk_bf16_f32 v168, v8, v9\n"
    "v_cvt_pk_bf16_f32 v184, v16, v17\n"
    "v_cvt_pk_bf16_f32 v200, v24, v25\n"
    "v_cvt_pk_bf16_f32 v104, v32, v33\n"
    "buffer_load_dwordx4 v249, s[16:19], s62 offen offset:1024 lds\n"
    "v_pk_fma_f32 v[8:9], v[14:15], v[10:11], v[8:9] op_sel_hi:[0,1,1] neg_lo:[0,0,1] neg_hi:[0,0,1]\n"
    "v_pk_fma_f32 v[16:17], v[22:23], v[18:19], v[16:17] op_sel_hi:[0,1,1] neg_lo:[0,0,1] neg_hi:[0,0,1]\n"
    "v_pk_fma_f32 v[24:25], v[30:31], v[26:27], v[24:25] op_sel_hi:[0,1,1] neg_lo:[0,0,1] neg_hi:[0,0,1]\n"
    "v_pk_fma_f32 v[32:33], v[38:39], v[34:35], v[32:33] op_sel_hi:[0,1,1] neg_lo:[0,0,1] neg_hi:[0,0,1]\n"
    "v_cvt_pk_bf16_f32 v169, v10, v11\n"
    "v_cvt_pk_bf16_f32 v185, v18, v19\n"
    "v_cvt_pk_bf16_f32 v201, v26, v27\n"
    "v_cvt_pk_bf16_f32 v105, v34, v35\n"
    "v_pk_fma_f32 v[10:11], v[14:15], v[8:9], v[10:11] op_sel_hi:[0,1,1] neg_lo:[0,0,1] neg_hi:[0,0,1]\n"
    "v_pk_fma_f32 v[18:19], v[22:23], v[16:17], v[18:19] op_sel_hi:[0,1,1] neg_lo:[0,0,1] neg_hi:[0,0,1]\n"
    "v_pk_fma_f32 v[26:27], v[30:31], v[24:25], v[26:27] op_sel_hi:[0,1,1] neg_lo:[0,0,1] neg_hi:[0,0,1]\n"
    "v_pk_fma_f32 v[34:35], v[38:39], v[32:33], v[34:35] op_sel_hi:[0,1,1] neg_lo:[0,0,1] neg_hi:[0,0,1]\n"
    "v_cvt_pk_bf16_f32 v170, v8, v9\n"
    "v_cvt_pk_bf16_f32 v186, v16, v17\n"
    "v_cvt_pk_bf16_f32 v202, v24, v25\n"
    "v_cvt_pk_bf16_f32 v106, v32, v33\n"
    "v_pk_fma_f32 v[8:9], v[14:15], v[10:11], v[8:9] op_sel_hi:[0,1,1] neg_lo:[0,0,1] neg_hi:[0,0,1]\n"
    "v_pk_fma_f32 v[16:17], v[22:23], v[18:19], v[16:17] op_sel_hi:[0,1,1] neg_lo:[0,0,1] neg_hi:[0,0,1]\n"
    "v_pk_fma_f32 v[24:25], v[30:31], v[26:27], v[24:25] op_sel_hi:[0,1,1] neg_lo:[0,0,1] neg_hi:[0,0,1]\n"
    "v_pk_fma_f32 v[32:33], v[38:39], v[34:35], v[32:33] op_sel_hi:[0,1,1] neg_lo:[0,0,1] neg_hi:[0,0,1]\n"
    "v_cvt_pk_bf16_f32 v171, v10, v11\n"
    "v_cvt_pk_bf16_f32 v187, v18, v19\n"
    "v_cvt_pk_bf16_f32 v203, v26, v27\n"
    "v_cvt_pk_bf16_f32 v107, v34, v35\n"
    "v_pk_fma_f32 v[10:11], v[14:15], v[8:9], v[10:11] op_sel_hi:[0,1,1] neg_lo:[0,0,1] neg_hi:[0,0,1]\n"
    "v_pk_fma_f32 v[18:19], v[22:23], v[16:17], v[18:19] op_sel_hi:[0,1,1] neg_lo:[0,0,1] neg_hi:[0,0,1]\n"
    "v_pk_fma_f32 v[26:27], v[30:31], v[24:25], v[26:27] op_sel_hi:[0,1,1] neg_lo:[0,0,1] neg_hi:[0,0,1]\n"
    "v_pk_fma_f32 v[34:35], v[38:39], v[32:33], v[34:35] op_sel_hi:[0,1,1] neg_lo:[0,0,1] neg_hi:[0,0,1]\n"
    "v_cvt_pk_bf16_f32 v172, v8, v9\n"
    "v_cvt_pk_bf16_f32 v188, v16, v17\n"
    "v_cvt_pk_bf16_f32 v204, v24, v25\n"
    "v_cvt_pk_bf16_f32 v108, v32, v33\n"
    "v_pk_fma_f32 v[8:9], v[14:15], v[10:11], v[8:9] op_sel_hi:[0,1,1] neg_lo:[0,0,1] neg_hi:[0,0,1]\n"
    "v_pk_fma_f32 v[16:17], v[22:23], v[18:19], v[16:17] op_sel_hi:[0,1,1] neg_lo:[0,0,1] neg_hi:[0,0,1]\n"
    "v_pk_fma_f32 v[24:25], v[30:31], v[26:27], v[24:25] op_sel_hi:[0,1,1] neg_lo:[0,0,1] neg_hi:[0,0,1]\n"
    "v_pk_fma_f32 v[32:33], v[38:39], v[34:35], v[32:33] op_sel_hi:[0,1,1] neg_lo:[0,0,1] neg_hi:[0,0,1]\n"
    "v_cvt_pk_bf16_f32 v173, v10, v11\n"
    "v_cvt_pk_bf16_f32 v189, v18, v19\n"
    "v_cvt_pk_bf16_f32 v205, v26, v27\n"
    "v_cvt_pk_bf16_f32 v109, v34, v35\n"
    "v_pk_fma_f32 v[10:11], v[14:15], v[8:9], v[10:11] op_sel_hi:[0,1,1] neg_lo:[0,0,1] neg_hi:[0,0,1]\n"
    "v_pk_fma_f32 v[18:19], v[22:23], v[16:17], v[18:19] op_sel_hi:[0,1,1] neg_lo:[0,0,1] neg_hi:[0,0,1]\n"
    "v_pk_fma_f32 v[26:27], v[30:31], v[24:25], v[26:27] op_sel_hi:[0,1,1] neg_lo:[0,0,1] neg_hi:[0,0,1]\n"
    "v_pk_fma_f32 v[34:35], v[38:39], v[32:33], v[34:35] op_sel_hi:[0,1,1] neg_lo:[0,0,1] neg_hi:[0,0,1]\n"
    "v_cvt_pk_bf16_f32 v174, v8, v9\n"
    "v_cvt_pk_bf16_f32 v190, v16, v17\n"
    "v_cvt_pk_bf16_f32 v206, v24, v25\n"
    "v_cvt_pk_bf16_f32 v110, v32, v33\n"
    "v_cndmask_b32 v160, v160, v6, s[28:29]\n"
    "v_cndmask_b32 v176, v176, v6, s[28:29]\n"
    "v_cndmask_b32 v192, v192, v6, s[28:29]\n"
    "v_cndmask_b32 v96, v96, v6, s[28:29]\n"
    "v_cvt_pk_bf16_f32 v175, v10, v11\n"
    "v_cvt_pk_bf16_f32 v191, v18, v19\n"
    "v_cvt_pk_bf16_f32 v207, v26, v27\n"
    "v_cvt_pk_bf16_f32 v111, v34, v35\n"
    "s_nop 0\n"
    "LbC:\n"
    "v_mov_b32 v254, s43\n"
    "v_mov_b32 v255, 1\n"
    "s_movk_i32 s63, 0x110\n"
    "v_lshrrev_b32 v208, 8, v250\n"
    "v_bfe_u32 v209, v250, 7, 1\n"
    "v_lshl_or_b32 v228, v208, 2, v209\n"
    "v_and_b32 v251, 0x70, v250\n"
    "v_lshlrev_b32 v209, 14, v209\n"
    "v_mad_u32_u24 v208, v208, s63, v209\n"
    "v_lshlrev_b32 v209, 1, v251\n"
    "v_lshl_or_b32 v251, v228, 15, v251\n"
    "s_and_b32 s60, s32, 3\n"
    "s_lshl_b32 s3, s60, 19\n"
    "s_add_u32 s57, s57, s3\n"
    "s_add_u32 s57, s57, 65536\n"
    "s_mul_i32 s3, s60, 0x440\n"
    "s_and_b32 s3, s3, 0xc40\n"
    "v_xor_b32 v208, v208, v209\n"
    "v_xor_b32 v208, s3, v208\n"
    "v_add_u32 v208, 98304, v208\n"
    "v_xor_b32 v209, 16, v208\n"
    "v_and_b32 v212, 48, v250\n"
    "v_bfe_u32 v213, v250, 6, 1\n"
    "v_lshl_or_b32 v212, v213, 7, v212\n"
    "v_bfe_u32 v213, v250, 7, 1\n"
    "v_lshl_or_b32 v212, v213, 6, v212\n"
    "v_lshrrev_b32 v213, 8, v250\n"
    "v_lshl_or_b32 v212, v213, 12, v212\n"
    "v_mul_u32_u24 v213, 1, v250\n"
    "s_and_b32 s3, s32, 3\n"
    "s_lshl2_add_u32 s53, s55, s3\n"
    "s_lshl_b32 s53, s53, 14\n"
    "s_add_u32 s53, s53, 6291456\n"
    "s_lshr_b32 s60, s55, 4\n"
    "s_lshl2_add_u32 s3, s60, s3\n"
    "s_mul_i32 s54, s3, 1024\n"
    "s_add_u32 s54, s54, 34816\n"
    "v_lshrrev_b32 v214, 4, v250\n"
    "v_and_b32 v215, 48, v214\n"
    "v_xor_b32 v215, v215, v250\n"
    "v_and_b32 v215, 0x70, v215\n"
    "v_and_b32 v214, 15, v214\n"
    "v_lshl_or_b32 v214, v214, 8, v215\n"
    "s_add_u32 s3, s43, 63488\n"
    "v_add_u32 v214, s3, v214\n"
    "v_xor_b32 v215, 64, v214\n"
    "v_add_u32 v236, 0x10000, v236\n"
    "v_add_u32 v237, 0x10000, v237\n"
    "v_add_u32 v238, 0x10000, v238\n"
    "v_add_u32 v239, 0x10000, v239\n"
    "s_waitcnt vmcnt(0)\n"
    "s_mov_b64 exec, 1\n"
    "ds_write_b32 v254, v255 offset:1556\n"
    "s_mov_b64 exec, -1\n"
    "ds_read_b32 v228, v254 offset:1564\n"
    "s_waitcnt lgkmcnt(0)\n"
    "v_cmp_eq_u32 vcc, 0, v228\n"
    "s_nop 0\n"
    "s_cbranch_vccnz Lls_B28\n"
    "Llr_B28:\n"
    "s_cmp_eq_u32 s38, 0\n"
    "s_cbranch_scc1 LbE\n"
    "LbD:\n"
    "LbE:\n"
    "buffer_load_dwordx4 v[112:115], v210, s[20:23], s44 offen sc1\n"
    "buffer_load_dwordx4 v[116:119], v210, s[20:23], s44 offen offset:1024 sc1\n"
    "buffer_load_dwordx4 v[120:123], v210, s[20:23], s45 offen sc1\n"
    "buffer_load_dwordx4 v[124:127], v210, s[20:23], s45 offen offset:1024 sc1\n"
    "buffer_load_dwordx4 v[48:51], v210, s[20:23], s46 offen sc1\n"
    "buffer_load_dwordx4 v[52:55], v210, s[20:23], s46 offen offset:1024 sc1\n"
    "LbG:\n"
    "LbH:\n"
    "s_waitcnt vmcnt(5)\n"
    "v_mfma_f32_16x16x32_bf16 v[0:3], v[128:131], v[112:115], 0\n"
    "s_mov_b64 exec, 1\n"
    "ds_write_b32 v254, v255 offset:1568\n"
    "s_mov_b64 exec, -1\n"
    "v_mfma_f32_16x16x32_bf16 v[8:11], v[132:135], v[112:115], 0\n"
    "v_mfma_f32_16x16x32_bf16 v[16:19], v[136:139], v[112:115], 0\n"
    "v_mfma_f32_16x16x32_bf16 v[24:27], v[140:143], v[112:115], 0\n"
    "s_waitcnt vmcnt(4)\n"
    "v_mfma_f32_16x16x32_bf16 v[28:31], v[140:143], v[116:119], 0\n"
    "buffer_load_dwordx4 v[56:59], v210, s[20:23], s47 offen sc1\n"
    "buffer_load_dwordx4 v[60:63], v210, s[20:23], s47 offen offset:1024 sc1\n"
    "v_mfma_f32_16x16x32_bf16 v[20:23], v[136:139], v[116:119], 0\n"
    "v_mfma_f32_16x16x32_bf16 v[12:15], v[132:135], v[116:119], 0\n"
    "v_mfma_f32_16x16x32_bf16 v[4:7], v[128:131], v[116:119], 0\n"
    "s_waitcnt vmcnt(5)\n"
    "v_mfma_f32_16x16x32_bf16 v[0:3], v[64:67], v[120:123], v[0:3]\n"
    "v_mfma_f32_16x16x32_bf16 v[8:11], v[68:71], v[120:123], v[8:11]\n"
    "v_mfma_f32_16x16x32_bf16 v[16:19], v[72:75], v[120:123], v[16:19]\n"
    "v_mfma_f32_16x16x32_bf16 v[24:27], v[76:79], v[120:123], v[24:27]\n"
    "s_waitcnt vmcnt(4)\n"
    "v_mfma_f32_16x16x32_bf16 v[28:31], v[76:79], v[124:127], v[28:31]\n"
    "buffer_load_dwordx4 v[128:131], v210, s[20:23], s48 offen sc1\n"
    "buffer_load_dwordx4 v[132:135], v210, s[20:23], s48 offen offset:1024 sc1\n"
    "buffer_load_dwordx2 v[230:231], v211, s[20:23], s56 offen offset:896 sc1\n"
    "v_mfma_f32_16x16x32_bf16 v[20:23], v[72:75], v[124:127], v[20:23]\n"
    "v_mfma_f32_16x16x32_bf16 v[12:15], v[68:71], v[124:127], v[12:15]\n"
    "v_mfma_f32_16x16x32_bf16 v[4:7], v[64:67], v[124:127], v[4:7]\n"
    "s_waitcnt vmcnt(6)\n"
    "v_mfma_f32_16x16x32_bf16 v[0:3], v[80:83], v[48:51], v[0:3]\n"
    "v_mfma_f32_16x16x32_bf16 v[8:11], v[84:87], v[48:51], v[8:11]\n"
    "v_mfma_f32_16x16x32_bf16 v[16:19], v[88:91], v[48:51], v[16:19]\n"
    "v_mfma_f32_16x16x32_bf16 v[24:27], v[92:95], v[48:51], v[24:27]\n"
    "s_waitcnt vmcnt(5)\n"
    "v_mfma_f32_16x16x32_bf16 v[28:31], v[92:95], v[52:55], v[28:31]\n"
    "buffer_load_dwordx4 v[136:139], v210, s[20:23], s49 offen sc1\n"
    "buffer_load_dwordx4 v[140:143], v210, s[20:23], s49 offen offset:1024 sc1\n"
    "v_mfma_f32_16x16x32_bf16 v[20:23], v[88:91], v[52:55], v[20:23]\n"
    "v_mfma_f32_16x16x32_bf16 v[12:15], v[84:87], v[52:55], v[12:15]\n"
    "v_mfma_f32_16x16x32_bf16 v[4:7], v[80:83], v[52:55], v[4:7]\n"
    "ds_read_b64_tr_b16 v[64:65], v236\n"
    "ds_read_b64_tr_b16 v[66:67], v236 offset:2048\n"
    "ds_read_b64_tr_b16 v[68:69], v237\n"
    "ds_read_b64_tr_b16 v[70:71], v237 offset:2048\n"
    "ds_read_b64_tr_b16 v[72:73], v238\n"
    "ds_read_b64_tr_b16 v[74:75], v238 offset:2048\n"
    "ds_read_b64_tr_b16 v[76:77], v239\n"
    "ds_read_b64_tr_b16 v[78:79], v239 offset:2048\n"
    "s_waitcnt vmcnt(6)\n"
    "v_mfma_f32_16x16x32_bf16 v[0:3], v[220:223], v[56:59], v[0:3]\n"
    "v_mfma_f32_16x16x32_bf16 v[8:11], v[240:243], v[56:59], v[8:11]\n"
    "v_mfma_f32_16x16x32_bf16 v[16:19], v[244:247], v[56:59], v[16:19]\n"
    "v_mfma_f32_16x16x32_bf16 v[24:27], v[232:235], v[56:59], v[24:27]\n"
    "s_waitcnt vmcnt(5)\n"
    "v_mfma_f32_16x16x32_bf16 v[28:31], v[232:235], v[60:63], v[28:31]\n"
    "buffer_load_dwordx4 v[112:115], v210, s[20:23], s50 offen sc1\n"
    "buffer_load_dwordx4 v[116:119], v210, s[20:23], s50 offen offset:1024 sc1\n"
    "v_mfma_f32_16x16x32_bf16 v[20:23], v[244:247], v[60:63], v[20:23]\n"
    "v_mfma_f32_16x16x32_bf16 v[12:15], v[240:243], v[60:63], v[12:15]\n"
    "v_mfma_f32_16x16x32_bf16 v[4:7], v[220:223], v[60:63], v[4:7]\n"
    "ds_read_b64_tr_b16 v[80:81], v236 offset:16384\n"
    "ds_read_b64_tr_b16 v[82:83], v236 offset:18432\n"
    "ds_read_b64_tr_b16 v[84:85], v237 offset:16384\n"
    "ds_read_b64_tr_b16 v[86:87], v237 offset:18432\n"
    "ds_read_b64_tr_b16 v[88:89], v238 offset:16384\n"
    "ds_read_b64_tr_b16 v[90:91], v238 offset:18432\n"
    "ds_read_b64_tr_b16 v[92:93], v239 offset:16384\n"
    "ds_read_b64_tr_b16 v[94:95], v239 offset:18432\n"
    "s_waitcnt vmcnt(6) lgkmcnt(14)\n"
    "v_mfma_f32_16x16x32_bf16 v[0:3], v[64:67], v[128:131], v[0:3]\n"
    "s_waitcnt lgkmcnt(12)\n"
    "v_mfma_f32_16x16x32_bf16 v[8:11], v[68:71], v[128:131], v[8:11]\n"
    "s_waitcnt lgkmcnt(10)\n"
    "v_mfma_f32_16x16x32_bf16 v[16:19], v[72:75], v[128:131], v[16:19]\n"
    "s_waitcnt lgkmcnt(8)\n"
    "v_mfma_f32_16x16x32_bf16 v[24:27], v[76:79], v[128:131], v[24:27]\n"
    "s_waitcnt vmcnt(5)\n"
    "v_mfma_f32_16x16x32_bf16 v[28:31], v[76:79], v[132:135], v[28:31]\n"
    "s_waitcnt vmcnt(4)\n"
    "v_cmp_ne_u64 vcc, s[36:37], v[230:231]\n"
    "s_nop 0\n"
    "s_cbranch_vccnz Lsp0_hbB\n"
    "Lret_hbB:\n"
    "v_add_u32 v210, 114688, v210\n"
    "buffer_load_dwordx4 v[120:123], v210, s[20:23], s44 offen sc1\n"
    "buffer_load_dwordx4 v[124:127], v210, s[20:23], s44 offen offset:1024 sc1\n"
    "v_mfma_f32_16x16x32_bf16 v[20:23], v[72:75], v[132:135], v[20:23]\n"
    "v_mfma_f32_16x16x32_bf16 v[12:15], v[68:71], v[132:135], v[12:15]\n"
    "v_mfma_f32_16x16x32_bf16 v[4:7], v[64:67], v[132:135], v[4:7]\n"
    "ds_read_b64_tr_b16 v[64:65], v236 offset:32768\n"
    "ds_read_b64_tr_b16 v[66:67], v236 offset:34816\n"
    "ds_read_b64_tr_b16 v[68:69], v237 offset:32768\n"
    "ds_read_b64_tr_b16 v[70:71], v237 offset:34816\n"
    "ds_read_b64_tr_b16 v[72:73], v238 offset:32768\n"
    "ds_read_b64_tr_b16 v[74:75], v238 offset:34816\n"
    "ds_read_b64_tr_b16 v[76:77], v239 offset:32768\n"
    "ds_read_b64_tr_b16 v[78:79], v239 offset:34816\n"
    "s_waitcnt vmcnt(5) lgkmcnt(14)\n"
    "v_mfma_f32_16x16x32_bf16 v[0:3], v[80:83], v[136:139], v[0:3]\n"
    "s_waitcnt lgkmcnt(12)\n"
    "v_mfma_f32_16x16x32_bf16 v[8:11], v[84:87], v[136:139], v[8:11]\n"
    "s_waitcnt lgkmcnt(10)\n"
    "v_mfma_f32_16x16x32_bf16 v[16:19], v[88:91], v[136:139], v[16:19]\n"
    "s_waitcnt lgkmcnt(8)\n"
    "v_mfma_f32_16x16x32_bf16 v[24:27], v[92:95], v[136:139], v[24:27]\n"
    "s_waitcnt vmcnt(4)\n"
    "v_mfma_f32_16x16x32_bf16 v[28:31], v[92:95], v[140:143], v[28:31]\n"
    "buffer_load_dwordx4 v[220:223], v210, s[20:23], s45 offen sc1\n"
    "buffer_load_dwordx4 v[240:243], v210, s[20:23], s45 offen offset:1024 sc1\n"
    "v_mfma_f32_16x16x32_bf16 v[20:23], v[88:91], v[140:143], v[20:23]\n"
    "v_mfma_f32_16x16x32_bf16 v[12:15], v[84:87], v[140:143], v[12:15]\n"
    "v_mfma_f32_16x16x32_bf16 v[4:7], v[80:83], v[140:143], v[4:7]\n"
    "s_waitcnt vmcnt(5) lgkmcnt(6)\n"
    "v_mfma_f32_16x16x32_bf16 v[0:3], v[64:67], v[112:115], v[0:3]\n"
    "s_waitcnt lgkmcnt(4)\n"
    "v_mfma_f32_16x16x32_bf16 v[8:11], v[68:71], v[112:115], v[8:11]\n"
    "s_waitcnt lgkmcnt(2)\n"
    "v_mfma_f32_16x16x32_bf16 v[16:19], v[72:75], v[112:115], v[16:19]\n"
    "s_waitcnt lgkmcnt(0)\n"
    "v_mfma_f32_16x16x32_bf16 v[24:27], v[76:79], v[112:115], v[24:27]\n"
    "s_waitcnt vmcnt(4)\n"
    "v_mfma_f32_16x16x32_bf16 v[28:31], v[76:79], v[116:119], v[28:31]\n"
    "buffer_load_dwordx4 v[244:247], v210, s[20:23], s46 offen sc1\n"
    "buffer_load_dwordx4 v[232:235], v210, s[20:23], s46 offen offset:1024 sc1\n"
    "v_mfma_f32_16x16x32_bf16 v[20:23], v[72:75], v[116:119], v[20:23]\n"
    "v_mfma_f32_16x16x32_bf16 v[12:15], v[68:71], v[116:119], v[12:15]\n"
    "v_mfma_f32_16x16x32_bf16 v[4:7], v[64:67], v[116:119], v[4:7]\n"
    "v_add_u32 v236, 0xc000, v236\n"
    "v_add_u32 v237, 0xc000, v237\n"
    "v_add_u32 v238, 0xc000, v238\n"
    "v_add_u32 v239, 0xc000, v239\n"
    "ds_read_b64_tr_b16 v[80:81], v236\n"
    "ds_read_b64_tr_b16 v[82:83], v236 offset:2048\n"
    "ds_read_b64_tr_b16 v[84:85], v237\n"
    "ds_read_b64_tr_b16 v[86:87], v237 offset:2048\n"
    "ds_read_b64_tr_b16 v[88:89], v238\n"
    "ds_read_b64_tr_b16 v[90:91], v238 offset:2048\n"
    "ds_read_b64_tr_b16 v[92:93], v239\n"
    "ds_read_b64_tr_b16 v[94:95], v239 offset:2048\n"
    "s_waitcnt vmcnt(5)\n"
    "v_mfma_f32_16x16x32_bf16 v[0:3], v[144:147], v[120:123], v[0:3]\n"
    "v_mfma_f32_16x16x32_bf16 v[8:11], v[148:151], v[120:123], v[8:11]\n"
    "v_mfma_f32_16x16x32_bf16 v[16:19], v[152:155], v[120:123], v[16:19]\n"
    "v_mfma_f32_16x16x32_bf16 v[24:27], v[156:159], v[120:123], v[24:27]\n"
    "s_waitcnt vmcnt(4)\n"
    "v_mfma_f32_16x16x32_bf16 v[28:31], v[156:159], v[124:127], v[28:31]\n"
    "buffer_load_dwordx4 v[128:131], v210, s[20:23], s47 offen sc1\n"
    "buffer_load_dwordx4 v[132:135], v210, s[20:23], s47 offen offset:1024 sc1\n"
    "v_mfma_f32_16x16x32_bf16 v[20:23], v[152:155], v[124:127], v[20:23]\n"
    "v_mfma_f32_16x16x32_bf16 v[12:15], v[148:151], v[124:127], v[12:15]\n"
    "v_mfma_f32_16x16x32_bf16 v[4:7], v[144:147], v[124:127], v[4:7]\n"
    "ds_read_b64_tr_b16 v[64:65], v236 offset:16384\n"
    "ds_read_b64_tr_b16 v[66:67], v236 offset:18432\n"
    "ds_read_b64_tr_b16 v[68:69], v237 offset:16384\n"
    "ds_read_b64_tr_b16 v[70:71], v237 offset:18432\n"
    "ds_read_b64_tr_b16 v[72:73], v238 offset:16384\n"
    "ds_read_b64_tr_b16 v[74:75], v238 offset:18432\n"
    "ds_read_b64_tr_b16 v[76:77], v239 offset:16384\n"
    "ds_read_b64_tr_b16 v[78:79], v239 offset:18432\n"
    "s_waitcnt vmcnt(5) lgkmcnt(14)\n"
    "v_mfma_f32_16x16x32_bf16 v[0:3], v[80:83], v[220:223], v[0:3]\n"
    "s_waitcnt lgkmcnt(12)\n"
    "v_mfma_f32_16x16x32_bf16 v[8:11], v[84:87], v[220:223], v[8:11]\n"
    "s_waitcnt lgkmcnt(10)\n"
    "v_mfma_f32_16x16x32_bf16 v[16:19], v[88:91], v[220:223], v[16:19]\n"
    "s_waitcnt lgkmcnt(8)\n"
    "v_mfma_f32_16x16x32_bf16 v[24:27], v[92:95], v[220:223], v[24:27]\n"
    "s_waitcnt vmcnt(4)\n"
    "v_mfma_f32_16x16x32_bf16 v[28:31], v[92:95], v[240:243], v[28:31]\n"
    "buffer_load_dwordx4 v[136:139], v210, s[20:23], s48 offen sc1\n"
    "buffer_load_dwordx4 v[140:143], v210, s[20:23], s48 offen offset:1024 sc1\n"
    "buffer_load_dwordx4 v[216:219], v213, s[20:23], s54 offen sc1\n"
    "v_mfma_f32_16x16x32_bf16 v[20:23], v[88:91], v[240:243], v[20:23]\n"
    "buffer_load_dwordx4 v[224:227], v251, s[4:7], s57 offen\n"
    "v_mfma_f32_16x16x32_bf16 v[12:15], v[84:87], v[240:243], v[12:15]\n"
    "v_mfma_f32_16x16x32_bf16 v[4:7], v[80:83], v[240:243], v[4:7]\n"
    "v_add_u32 v236, 0xfffe4000, v236\n"
    "v_add_u32 v237, 0xfffe4000, v237\n"
    "v_add_u32 v238, 0xfffe4000, v238\n"
    "v_add_u32 v239, 0xfffe4000, v239\n"
    "ds_read_b64_tr_b16 v[80:81], v236 offset:16384\n"
    "ds_read_b64_tr_b16 v[82:83], v236 offset:18432\n"
    "ds_read_b64_tr_b16 v[84:85], v237 offset:16384\n"
    "ds_read_b64_tr_b16 v[86:87], v237 offset:18432\n"
    "ds_read_b64_tr_b16 v[88:89], v238 offset:16384\n"
    "ds_read_b64_tr_b16 v[90:91], v238 offset:18432\n"
    "ds_read_b64_tr_b16 v[92:93], v239 offset:16384\n"
    "ds_read_b64_tr_b16 v[94:95], v239 offset:18432\n"
    "s_waitcnt vmcnt(7) lgkmcnt(14)\n"
    "v_mfma_f32_16x16x32_bf16 v[0:3], v[64:67], v[244:247], v[0:3]\n"
    "s_waitcnt lgkmcnt(12)\n"
    "v_mfma_f32_16x16x32_bf16 v[8:11], v[68:71], v[244:247], v[8:11]\n"
    "s_waitcnt lgkmcnt(10)\n"
    "v_mfma_f32_16x16x32_bf16 v[16:19], v[72:75], v[244:247], v[16:19]\n"
    "s_waitcnt lgkmcnt(8)\n"
    "v_mfma_f32_16x16x32_bf16 v[24:27], v[76:79], v[244:247], v[24:27]\n"
    "s_waitcnt vmcnt(6)\n"
    "v_mfma_f32_16x16x32_bf16 v[28:31], v[76:79], v[232:235], v[28:31]\n"
    "buffer_load_dwordx4 v[112:115], v210, s[20:23], s49 offen sc1\n"
    "buffer_load_dwordx4 v[116:119], v210, s[20:23], s49 offen offset:1024 sc1\n"
    "v_mfma_f32_16x16x32_bf16 v[20:23], v[72:75], v[232:235], v[20:23]\n"
    "v_mfma_f32_16x16x32_bf16 v[12:15], v[68:71], v[232:235], v[12:15]\n"
    "v_mfma_f32_16x16x32_bf16 v[4:7], v[64:67], v[232:235], v[4:7]\n"
    "ds_read_b64_tr_b16 v[64:65], v236 offset:32768\n"
    "ds_read_b64_tr_b16 v[66:67], v236 offset:34816\n"
    "ds_read_b64_tr_b16 v[68:69], v237 offset:32768\n"
    "ds_read_b64_tr_b16 v[70:71], v237 offset:34816\n"
    "ds_read_b64_tr_b16 v[72:73], v238 offset:32768\n"
    "ds_read_b64_tr_b16 v[74:75], v238 offset:34816\n"
    "ds_read_b64_tr_b16 v[76:77], v239 offset:32768\n"
    "ds_read_b64_tr_b16 v[78:79], v239 offset:34816\n"
    "s_waitcnt vmcnt(7) lgkmcnt(14)\n"
    "v_mfma_f32_16x16x32_bf16 v[0:3], v[80:83], v[128:131], v[0:3]\n"
    "s_waitcnt lgkmcnt(12)\n"
    "v_mfma_f32_16x16x32_bf16 v[8:11], v[84:87], v[128:131], v[8:11]\n"
    "s_waitcnt lgkmcnt(10)\n"
    "v_mfma_f32_16x16x32_bf16 v[16:19], v[88:91], v[128:131], v[16:19]\n"
    "s_waitcnt lgkmcnt(8)\n"
    "v_mfma_f32_16x16x32_bf16 v[24:27], v[92:95], v[128:131], v[24:27]\n"
    "s_waitcnt vmcnt(6)\n"
    "v_mfma_f32_16x16x32_bf16 v[28:31], v[92:95], v[132:135], v[28:31]\n"
    "buffer_load_dwordx4 v[144:147], v210, s[20:23], s50 offen sc1\n"
    "buffer_load_dwordx4 v[148:151], v210, s[20:23], s50 offen offset:1024 sc1\n"
    "v_mfma_f32_16x16x32_bf16 v[20:23], v[88:91], v[132:135], v[20:23]\n"
    "v_mfma_f32_16x16x32_bf16 v[12:15], v[84:87], v[132:135], v[12:15]\n"
    "v_mfma_f32_16x16x32_bf16 v[4:7], v[80:83], v[132:135], v[4:7]\n"
    "ds_read_b32 v228, v254 offset:1552\n"
    "s_waitcnt lgkmcnt(0)\n"
    "v_cmp_eq_u32 vcc, 0, v228\n"
    "s_nop 0\n"
    "s_cbranch_vccnz Lls_B16\n"
    "Llr_B16:\n"
    "ds_read_b64_tr_b16 v[80:81], v236 offset:49152\n"
    "ds_read_b64_tr_b16 v[82:83], v236 offset:51200\n"
    "ds_read_b64_tr_b16 v[84:85], v237 offset:49152\n"
    "ds_read_b64_tr_b16 v[86:87], v237 offset:51200\n"
    "ds_read_b64_tr_b16 v[88:89], v238 offset:49152\n"
    "ds_read_b64_tr_b16 v[90:91], v238 offset:51200\n"
    "ds_read_b64_tr_b16 v[92:93], v239 offset:49152\n"
    "ds_read_b64_tr_b16 v[94:95], v239 offset:51200\n"
    "s_waitcnt vmcnt(7)\n"
    "v_mfma_f32_16x16x32_bf16 v[0:3], v[64:67], v[136:139], v[0:3]\n"
    "v_mfma_f32_16x16x32_bf16 v[8:11], v[68:71], v[136:139], v[8:11]\n"
    "v_mfma_f32_16x16x32_bf16 v[16:19], v[72:75], v[136:139], v[16:19]\n"
    "v_mfma_f32_16x16x32_bf16 v[24:27], v[76:79], v[136:139], v[24:27]\n"
    "s_waitcnt vmcnt(6)\n"
    "v_mfma_f32_16x16x32_bf16 v[28:31], v[76:79], v[140:143], v[28:31]\n"
    "s_waitcnt vmcnt(5)\n"
    "v_cmp_ne_u64 vcc, s[36:37], v[216:217]\n"
    "v_cmp_ne_u64 s[60:61], s[36:37], v[218:219]\n"
    "s_nop 0\n"
    "s_or_b64 vcc, vcc, s[60:61]\n"
    "s_cbranch_vccnz Lsp0_y\n"
    "Lret_y:\n"
    "buffer_load_dwordx4 v[152:155], v212, s[20:23], s53 offen sc1\n"
    "buffer_load_dwordx4 v[156:159], v212, s[20:23], s53 offen offset:256 sc1\n"
    "buffer_load_dwordx4 v[120:123], v212, s[20:23], s53 offen offset:512 sc1\n"
    "buffer_load_dwordx4 v[124:127], v212, s[20:23], s53 offen offset:768 sc1\n"
    "buffer_load_dwordx4 v[220:223], v212, s[20:23], s53 offen offset:1024 sc1\n"
    "buffer_load_dwordx4 v[240:243], v212, s[20:23], s53 offen offset:1280 sc1\n"
    "buffer_load_dwordx4 v[244:247], v212, s[20:23], s53 offen offset:1536 sc1\n"
    "buffer_load_dwordx4 v[232:235], v212, s[20:23], s53 offen offset:1792 sc1\n"
    "buffer_load_dwordx4 v[128:131], v212, s[20:23], s53 offen offset:2048 sc1\n"
    "buffer_load_dwordx4 v[132:135], v212, s[20:23], s53 offen offset:2304 sc1\n"
    "v_mfma_f32_16x16x32_bf16 v[20:23], v[72:75], v[140:143], v[20:23]\n"
    "v_mfma_f32_16x16x32_bf16 v[12:15], v[68:71], v[140:143], v[12:15]\n"
    "s_mov_b64 exec, 1\n"
    "ds_write_b32 v254, v255 offset:1560\n"
    "s_mov_b64 exec, -1\n"
    "v_mfma_f32_16x16x32_bf16 v[4:7], v[64:67], v[140:143], v[4:7]\n"
    "s_lshr_b32 s3, s43, 1\n"
    "s_sub_u32 s3, 146432, s3\n"
    "ds_read_b64_tr_b16 v[64:65], v236\n"
    "ds_read_b64_tr_b16 v[68:69], v237\n"
    "ds_read_b64_tr_b16 v[72:73], v238\n"
    "ds_read_b64_tr_b16 v[76:77], v239\n"
    "s_mov_b32 exec_lo, 0\n"
    "v_add_u32 v236, s3, v236\n"
    "v_add_u32 v237, s3, v237\n"
    "v_add_u32 v238, s3, v238\n"
    "v_add_u32 v239, s3, v239\n"
    "s_mov_b32 exec_lo, -1\n"
    "ds_read_b64_tr_b16 v[66:67], v236 offset:2048\n"
    "ds_read_b64_tr_b16 v[70:71], v237 offset:2048\n"
    "ds_read_b64_tr_b16 v[74:75], v238 offset:2048\n"
    "ds_read_b64_tr_b16 v[78:79], v239 offset:2048\n"
    "s_waitcnt vmcnt(13) lgkmcnt(14)\n"
    "v_mfma_f32_16x16x32_bf16 v[0:3], v[80:83], v[112:115], v[0:3]\n"
    "s_waitcnt lgkmcnt(13)\n"
    "v_mfma_f32_16x16x32_bf16 v[8:11], v[84:87], v[112:115], v[8:11]\n"
    "s_waitcnt lgkmcnt(11)\n"
    "v_mfma_f32_16x16x32_bf16 v[16:19], v[88:91], v[112:115], v[16:19]\n"
    "s_waitcnt lgkmcnt(9)\n"
    "v_mfma_f32_16x16x32_bf16 v[24:27], v[92:95], v[112:115], v[24:27]\n"
    "s_waitcnt vmcnt(12)\n"
    "v_mfma_f32_16x16x32_bf16 v[28:31], v[92:95], v[116:119], v[28:31]\n"
    "buffer_load_dwordx4 v[136:139], v212, s[20:23], s53 offen offset:2560 sc1\n"
    "buffer_load_dwordx4 v[140:143], v212, s[20:23], s53 offen offset:2816 sc1\n"
    "v_mfma_f32_16x16x32_bf16 v[20:23], v[88:91], v[116:119], v[20:23]\n"
    "v_mfma_f32_16x16x32_bf16 v[12:15], v[84:87], v[116:119], v[12:15]\n"
    "v_mfma_f32_16x16x32_bf16 v[4:7], v[80:83], v[116:119], v[4:7]\n"
    "s_waitcnt vmcnt(13) lgkmcnt(3)\n"
    "v_mfma_f32_16x16x32_bf16 v[0:3], v[64:67], v[144:147], v[0:3]\n"
    "s_waitcnt lgkmcnt(2)\n"
    "v_mfma_f32_16x16x32_bf16 v[8:11], v[68:71], v[144:147], v[8:11]\n"
    "s_waitcnt lgkmcnt(1)\n"
    "v_mfma_f32_16x16x32_bf16 v[16:19], v[72:75], v[144:147], v[16:19]\n"
    "s_waitcnt lgkmcnt(0)\n"
    "v_mfma_f32_16x16x32_bf16 v[24:27], v[76:79], v[144:147], v[24:27]\n"
    "s_waitcnt vmcnt(12)\n"
    "v_mfma_f32_16x16x32_bf16 v[28:31], v[76:79], v[148:151], v[28:31]\n"
    "v_mfma_f32_16x16x32_bf16 v[20:23], v[72:75], v[148:151], v[20:23]\n"
    "v_mfma_f32_16x16x32_bf16 v[12:15], v[68:71], v[148:151], v[12:15]\n"
    "v_mfma_f32_16x16x32_bf16 v[4:7], v[64:67], v[148:151], v[4:7]\n"
    "s_waitcnt vmcnt(10)\n"
    "v_swap_b32 v153, v156\n"
    "s_waitcnt vmcnt(9)\n"
    "v_swap_b32 v154, v120\n"
    "s_waitcnt vmcnt(8)\n"
    "v_swap_b32 v155, v124\n"
    "v_swap_b32 v158, v121\n"
    "s_nop 0\n"
    "v_mfma_f32_16x16x32_bf16 v[32:35], v[160:163], v[152:155], 0\n"
    "v_swap_b32 v159, v125\n"
    "v_mfma_f32_16x16x32_bf16 v[40:43], v[176:179], v[152:155], 0\n"
    "v_swap_b32 v123, v126\n"
    "v_mfma_f32_16x16x32_bf16 v[48:51], v[192:195], v[152:155], 0\n"
    "buffer_load_dwordx4 v[80:83], v212, s[20:23], s53 offen offset:3072 sc1\n"
    "v_mfma_f32_16x16x32_bf16 v[56:59], v[96:99], v[152:155], 0\n"
    "buffer_load_dwordx4 v[84:87], v212, s[20:23], s53 offen offset:3328 sc1\n"
    "v_mfma_f32_16x16x32_bf16 v[60:63], v[96:99], v[156:159], 0\n"
    "buffer_load_dwordx4 v[88:91], v212, s[20:23], s53 offen offset:3584 sc1\n"
    "v_mfma_f32_16x16x32_bf16 v[52:55], v[192:195], v[156:159], 0\n"
    "buffer_load_dwordx4 v[92:95], v212, s[20:23], s53 offen offset:3840 sc1\n"
    "v_mfma_f32_16x16x32_bf16 v[44:47], v[176:179], v[156:159], 0\n"
    "v_mfma_f32_16x16x32_bf16 v[36:39], v[160:163], v[156:159], 0\n"
    "v_mfma_f32_16x16x32_bf16 v[0:3], v[160:163], v[120:123], v[0:3]\n"
    "v_mfma_f32_16x16x32_bf16 v[8:11], v[176:179], v[120:123], v[8:11]\n"
    "v_mfma_f32_16x16x32_bf16 v[16:19], v[192:195], v[120:123], v[16:19]\n"
    "v_mfma_f32_16x16x32_bf16 v[24:27], v[96:99], v[120:123], v[24:27]\n"
    "v_mfma_f32_16x16x32_bf16 v[28:31], v[96:99], v[124:127], v[28:31]\n"
    "v_mfma_f32_16x16x32_bf16 v[20:23], v[192:195], v[124:127], v[20:23]\n"
    "v_mfma_f32_16x16x32_bf16 v[12:15], v[176:179], v[124:127], v[12:15]\n"
    "v_mfma_f32_16x16x32_bf16 v[4:7], v[160:163], v[124:127], v[4:7]\n"
    "s_waitcnt vmcnt(10)\n"
    "v_swap_b32 v221, v240\n"
    "s_waitcnt vmcnt(9)\n"
    "v_swap_b32 v222, v244\n"
    "s_waitcnt vmcnt(8)\n"
    "v_swap_b32 v223, v232\n"
    "v_swap_b32 v242, v245\n"
    "s_nop 0\n"
    "v_mfma_f32_16x16x32_bf16 v[32:35], v[164:167], v[220:223], v[32:35]\n"
    "v_swap_b32 v243, v233\n"
    "v_mfma_f32_16x16x32_bf16 v[40:43], v[180:183], v[220:223], v[40:43]\n"
    "v_swap_b32 v247, v234\n"
    "v_mfma_f32_16x16x32_bf16 v[48:51], v[196:199], v[220:223], v[48:51]\n"
    "v_mfma_f32_16x16x32_bf16 v[56:59], v[100:103], v[220:223], v[56:59]\n"
    "v_mfma_f32_16x16x32_bf16 v[60:63], v[100:103], v[240:243], v[60:63]\n"
    "v_mfma_f32_16x16x32_bf16 v[52:55], v[196:199], v[240:243], v[52:55]\n"
    "v_mfma_f32_16x16x32_bf16 v[44:47], v[180:183], v[240:243], v[44:47]\n"
    "v_mfma_f32_16x16x32_bf16 v[36:39], v[164:167], v[240:243], v[36:39]\n"
    "v_mfma_f32_16x16x32_bf16 v[0:3], v[164:167], v[244:247], v[0:3]\n"
    "v_mfma_f32_16x16x32_bf16 v[8:11], v[180:183], v[244:247], v[8:11]\n"
    "v_mfma_f32_16x16x32_bf16 v[16:19], v[196:199], v[244:247], v[16:19]\n"
    "v_mfma_f32_16x16x32_bf16 v[24:27], v[100:103], v[244:247], v[24:27]\n"
    "v_mfma_f32_16x16x32_bf16 v[28:31], v[100:103], v[232:235], v[28:31]\n"
    "v_mfma_f32_16x16x32_bf16 v[20:23], v[196:199], v[232:235], v[20:23]\n"
    "v_mfma_f32_16x16x32_bf16 v[12:15], v[180:183], v[232:235], v[12:15]\n"
    "v_mfma_f32_16x16x32_bf16 v[4:7], v[164:167], v[232:235], v[4:7]\n"
    "s_waitcnt vmcnt(6)\n"
    "v_swap_b32 v129, v132\n"
    "s_waitcnt vmcnt(5)\n"
    "v_swap_b32 v130, v136\n"
    "s_waitcnt vmcnt(4)\n"
    "v_swap_b32 v131, v140\n"
    "s_nop 1\n"
    "v_mfma_f32_16x16x32_bf16 v[32:35], v[168:171], v[128:131], v[32:35]\n"
    "s_waitcnt vmcnt(2)\n"
    "v_swap_b32 v81, v84\n"
    "s_waitcnt vmcnt(1)\n"
    "v_swap_b32 v82, v88\n"
    "v_mfma_f32_16x16x32_bf16 v[40:43], v[184:187], v[128:131], v[40:43]\n"
    "s_waitcnt vmcnt(0)\n"
    "v_swap_b32 v83, v92\n"
    "v_swap_b32 v134, v137\n"
    "v_mfma_f32_16x16x32_bf16 v[48:51], v[200:203], v[128:131], v[48:51]\n"
    "v_swap_b32 v135, v141\n"
    "v_mfma_f32_16x16x32_bf16 v[56:59], v[104:107], v[128:131], v[56:59]\n"
    "v_mfma_f32_16x16x32_bf16 v[32:35], v[172:175], v[80:83], v[32:35]\n"
    "v_swap_b32 v139, v142\n"
    "v_mfma_f32_16x16x32_bf16 v[40:43], v[188:191], v[80:83], v[40:43]\n"
    "v_swap_b32 v86, v89\n"
    "v_mfma_f32_16x16x32_bf16 v[48:51], v[204:207], v[80:83], v[48:51]\n"
    "v_swap_b32 v87, v93\n"
    "v_mfma_f32_16x16x32_bf16 v[56:59], v[108:111], v[80:83], v[56:59]\n"
    "v_swap_b32 v91, v94\n"
    "v_mfma_f32_16x16x32_bf16 v[36:39], v[168:171], v[132:135], v[36:39]\n"
    "v_mfma_f32_16x16x32_bf16 v[36:39], v[172:175], v[84:87], v[36:39]\n"
    "ds_write_b128 v214, v[32:35]\n"
    "v_mfma_f32_16x16x32_bf16 v[44:47], v[188:191], v[84:87], v[44:47]\n"
    "ds_write_b128 v215, v[40:43]\n"
    "v_mfma_f32_16x16x32_bf16 v[44:47], v[184:187], v[132:135], v[44:47]\n"
    "v_mfma_f32_16x16x32_bf16 v[52:55], v[200:203], v[132:135], v[52:55]\n"
    "ds_write_b128 v214, v[48:51] offset:128\n"
    "v_mfma_f32_16x16x32_bf16 v[52:55], v[204:207], v[84:87], v[52:55]\n"
    "ds_write_b128 v215, v[56:59] offset:128\n"
    "v_mfma_f32_16x16x32_bf16 v[60:63], v[108:111], v[84:87], v[60:63]\n"
    "ds_write_b128 v214, v[36:39] offset:16384\n"
    "v_mfma_f32_16x16x32_bf16 v[60:63], v[104:107], v[132:135], v[60:63]\n"
    "v_mfma_f32_16x16x32_bf16 v[0:3], v[168:171], v[136:139], v[0:3]\n"
    "ds_write_b128 v215, v[44:47] offset:16384\n"
    "v_mfma_f32_16x16x32_bf16 v[0:3], v[172:175], v[88:91], v[0:3]\n"
    "v_mfma_f32_16x16x32_bf16 v[8:11], v[188:191], v[88:91], v[8:11]\n"
    "ds_write_b128 v214, v[52:55] offset:16512\n"
    "v_mfma_f32_16x16x32_bf16 v[8:11], v[184:187], v[136:139], v[8:11]\n"
    "v_mfma_f32_16x16x32_bf16 v[16:19], v[200:203], v[136:139], v[16:19]\n"
    "s_nop 0\n"
    "ds_write_b128 v215, v[60:63] offset:16512\n"
    "v_mfma_f32_16x16x32_bf16 v[16:19], v[204:207], v[88:91], v[16:19]\n"
    "v_mfma_f32_16x16x32_bf16 v[24:27], v[108:111], v[88:91], v[24:27]\n"
    "ds_write_b128 v214, v[0:3] offset:32768\n"
    "v_mfma_f32_16x16x32_bf16 v[24:27], v[104:107], v[136:139], v[24:27]\n"
    "v_mfma_f32_16x16x32_bf16 v[4:7], v[168:171], v[140:143], v[4:7]\n"
    "v_lshlrev_b32 v160, 16, v224\n"
    "ds_write_b128 v215, v[8:11] offset:32768\n"
    "v_mfma_f32_16x16x32_bf16 v[4:7], v[172:175], v[92:95], v[4:7]\n"
    "v_and_b32 v161, 0xffff0000, v224\n"
    "v_mfma_f32_16x16x32_bf16 v[12:15], v[188:191], v[92:95], v[12:15]\n"
    "v_lshlrev_b32 v162, 16, v225\n"
    "ds_write_b128 v214, v[16:19] offset:32896\n"
    "v_mfma_f32_16x16x32_bf16 v[12:15], v[184:187], v[140:143], v[12:15]\n"
    "v_and_b32 v163, 0xffff0000, v225\n"
    "v_mfma_f32_16x16x32_bf16 v[20:23], v[200:203], v[140:143], v[20:23]\n"
    "v_lshlrev_b32 v164, 16, v226\n"
    "ds_write_b128 v215, v[24:27] offset:32896\n"
    "v_mfma_f32_16x16x32_bf16 v[20:23], v[204:207], v[92:95], v[20:23]\n"
    "v_and_b32 v165, 0xffff0000, v226\n"
    "v_mfma_f32_16x16x32_bf16 v[28:31], v[108:111], v[92:95], v[28:31]\n"
    "v_lshlrev_b32 v166, 16, v227\n"
    "v_and_b32 v167, 0xffff0000, v227\n"
    "ds_write_b128 v214, v[4:7] offset:49152\n"
    "v_mfma_f32_16x16x32_bf16 v[28:31], v[104:107], v[140:143], v[28:31]\n"
    "ds_write_b128 v215, v[12:15] offset:49152\n"
    "s_nop 0\n"
    "ds_write_b128 v214, v[20:23] offset:49280\n"
    "s_nop 4\n"
    "ds_write_b128 v215, v[28:31] offset:49280\n"
    "s_waitcnt lgkmcnt(0)\n"
    "s_barrier\n"
    "ds_read_b128 v[64:67], v208\n"
    "ds_read_b128 v[68:71], v209\n"
    "ds_read_b128 v[72:75], v208 offset:4096\n"
    "ds_read_b128 v[76:79], v209 offset:4096\n"
    "ds_read_b128 v[80:83], v208 offset:8192\n"
    "ds_read_b128 v[84:87], v209 offset:8192\n"
    "ds_read_b128 v[88:91], v208 offset:12288\n"
    "ds_read_b128 v[92:95], v209 offset:12288\n"
    "s_waitcnt lgkmcnt(5)\n"
    "v_pk_add_f32 v[64:65], v[64:65], v[72:73]\n"
    "v_pk_add_f32 v[66:67], v[66:67], v[74:75]\n"
    "s_waitcnt lgkmcnt(4)\n"
    "v_pk_add_f32 v[68:69], v[68:69], v[76:77]\n"
    "v_pk_add_f32 v[70:71], v[70:71], v[78:79]\n"
    "v_pk_add_f32 v[160:161], v[160:161], v[64:65]\n"
    "v_pk_add_f32 v[162:163], v[162:163], v[66:67]\n"
    "v_pk_add_f32 v[164:165], v[164:165], v[68:69]\n"
    "v_pk_add_f32 v[166:167], v[166:167], v[70:71]\n"
    "s_waitcnt lgkmcnt(1)\n"
    "v_pk_add_f32 v[80:81], v[80:81], v[88:89]\n"
    "v_pk_add_f32 v[82:83], v[82:83], v[90:91]\n"
    "s_waitcnt lgkmcnt(0)\n"
    "v_pk_add_f32 v[84:85], v[84:85], v[92:93]\n"
    "v_pk_add_f32 v[86:87], v[86:87], v[94:95]\n"
    "v_pk_add_f32 v[160:161], v[160:161], v[80:81]\n"
    "v_pk_add_f32 v[162:163], v[162:163], v[82:83]\n"
    "v_pk_add_f32 v[164:165], v[164:165], v[84:85]\n"
    "v_pk_add_f32 v[166:167], v[166:167], v[86:87]\n"
    "v_cvt_pk_bf16_f32 v0, v160, v161\n"
    "v_cvt_pk_bf16_f32 v1, v162, v163\n"
    "v_cvt_pk_bf16_f32 v2, v164, v165\n"
    "v_cvt_pk_bf16_f32 v3, v166, v167\n"
    "buffer_store_dwordx4 v[0:3], v251, s[24:27], s57 offen\n"
    "s_branch Lfin\n"
    "LfA:\n"
    "s_and_b32 s61, s32, 1\n"
    "v_xor_b32 v3, v5, v2\n"
    "v_lshlrev_b32 v3, 4, v3\n"
    "v_and_b32 v4, 7, v1\n"
    "v_lshl_or_b32 v3, v4, 7, v3\n"
    "v_bfe_u32 v4, v1, 3, 1\n"
    "v_lshl_or_b32 v3, v4, 11, v3\n"
    "s_lshl_b32 s3, s61, 10\n"
    "s_add_u32 s3, s3, 65536\n"
    "v_add_u32 v240, s3, v3\n"
    "v_xor_b32 v241, 64, v240\n"
    "v_bfe_u32 v3, v1, 2, 3\n"
    "v_xor_b32 v3, s32, v3\n"
    "v_and_b32 v4, 3, v1\n"
    "v_lshlrev_b32 v4, 3, v4\n"
    "v_lshl_or_b32 v3, v3, 5, v4\n"
    "v_bfe_u32 v4, v1, 2, 2\n"
    "v_lshl_or_b32 v3, v4, 8, v3\n"
    "v_lshl_or_b32 v3, v2, 11, v3\n"
    "v_add_u32 v242, 81920, v3\n"
    "v_xor_b32 v243, 32, v242\n"
    "v_lshrrev_b32 v3, 5, v1\n"
    "v_xor_b32 v3, v3, v5\n"
    "v_and_b32 v3, 1, v3\n"
    "v_add_u32 v3, -1, v3\n"
    "v_and_b32 v220, 0x3f803f80, v3\n"
    "v_and_b32 v3, 1, v1\n"
    "v_mul_u32_u24 v3, 0xaaaa, v3\n"
    "v_add_u32 v3, 0x4444, v3\n"
    "v_lshl_or_b32 v247, v3, 16, v3\n"
    "v_mov_b32 v221, v220\n"
    "v_mov_b32 v222, v220\n"
    "v_mov_b32 v223, v220\n"
    "v_mov_b32 v216, 0\n"
    "v_mov_b32 v217, 0\n"
    "v_mov_b32 v218, 0\n"
    "v_mov_b32 v219, 0\n"
    "s_and_b32 s3, s32, 3\n"
    "s_lshl_b32 s3, s3, 4\n"
    "v_bfe_u32 v2, v250, 4, 4\n"
    "v_lshrrev_b32 v3, 6, v250\n"
    "v_and_b32 v3, 12, v3\n"
    "v_add_u32 v4, s3, v2\n"
    "v_add_u32 v5, s3, v3\n"
    "v_cvt_f32_u32 v2, v2\n"
    "v_cvt_f32_u32 v10, v3\n"
    "v_cvt_f32_u32 v4, v4\n"
    "v_cvt_f32_u32 v42, v5\n"
    "v_mul_f32 v6, 0x3c800000, v4\n"
    "v_mul_f32 v7, 0x3a000000, v4\n"
    "v_mul_f32 v8, 0x3d000000, v2\n"
    "v_mul_f32 v4, 0x3a000000, v2\n"
    "v_add_f32 v5, 0x3c000000, v4\n"
    "v_mov_b32 v9, 0x36000000\n"
    "v_add_f32 v11, 0x3f800000, v10\n"
    "v_add_f32 v12, 0x40000000, v10\n"
    "v_add_f32 v13, 0x40400000, v10\n"
    "v_add_f32 v14, 0x41800000, v10\n"
    "v_add_f32 v15, 0x41880000, v10\n"
    "v_add_f32 v16, 0x41900000, v10\n"
    "v_add_f32 v17, 0x41980000, v10\n"
    "v_add_f32 v43, 0x3f800000, v42\n"
    "v_add_f32 v44, 0x40000000, v42\n"
    "v_add_f32 v45, 0x40400000, v42\n"
    "v_pk_mul_f32 v[18:19], v[10:11], v[6:7] op_sel_hi:[1,0]\n"
    "v_pk_mul_f32 v[20:21], v[12:13], v[6:7] op_sel_hi:[1,0]\n"
    "v_pk_mul_f32 v[22:23], v[14:15], v[6:7] op_sel_hi:[1,0]\n"
    "v_pk_mul_f32 v[24:25], v[16:17], v[6:7] op_sel_hi:[1,0]\n"
    "v_cos_f32 v26, v18\n"
    "v_sin_f32 v34, v18\n"
    "v_cos_f32 v27, v19\n"
    "v_sin_f32 v35, v19\n"
    "v_cos_f32 v28, v20\n"
    "v_sin_f32 v36, v20\n"
    "v_cos_f32 v29, v21\n"
    "v_sin_f32 v37, v21\n"
    "v_cos_f32 v30, v22\n"
    "v_sin_f32 v38, v22\n"
    "v_cos_f32 v31, v23\n"
    "v_sin_f32 v39, v23\n"
    "v_cos_f32 v32, v24\n"
    "v_sin_f32 v40, v24\n"
    "v_cos_f32 v33, v25\n"
    "v_sin_f32 v41, v25\n"
    "s_add_u32 m0, s42, 65536\n"
    "s_nop 0\n"
    "buffer_load_dwordx4 v245, s[12:15], s59 offen lds\n"
    "buffer_load_dwordx4 v246, s[12:15], s59 offen offset:1024 lds\n"
    "s_add_u32 m0, s45, 65536\n"
    "s_nop 0\n"
    "buffer_load_dwordx4 v245, s[8:11], s50 offen lds\n"
    "s_add_u32 m0, s47, 65536\n"
    "s_nop 0\n"
    "buffer_load_dwordx4 v244, s[4:7], s57 offen lds\n"
    "v_cvt_pk_bf16_f32 v200, v26, v27\n"
    "v_cvt_pk_bf16_f32 v201, v28, v29\n"
    "v_cvt_pk_bf16_f32 v202, v30, v31\n"
    "v_cvt_pk_bf16_f32 v203, v32, v33\n"
    "v_cvt_pk_bf16_f32 v204, v34, v35\n"
    "v_cvt_pk_bf16_f32 v205, v36, v37\n"
    "v_cvt_pk_bf16_f32 v206, v38, v39\n"
    "v_cvt_pk_bf16_f32 v207, v40, v41\n"
    "v_pk_mul_f32 v[18:19], v[10:11], v[8:9] op_sel_hi:[1,0]\n"
    "v_pk_mul_f32 v[20:21], v[12:13], v[8:9] op_sel_hi:[1,0]\n"
    "s_add_u32 m0, s42, 114688\n"
    "v_pk_mul_f32 v[22:23], v[14:15], v[8:9] op_sel_hi:[1,0]\n"
    "buffer_load_dwordx4 v245, s[12:15], s39 offen lds\n"
    "v_pk_mul_f32 v[24:25], v[16:17], v[8:9] op_sel_hi:[1,0]\n"
    "v_cos_f32 v26, v18\n"
    "v_sin_f32 v34, v18\n"
    "v_cos_f32 v27, v19\n"
    "v_sin_f32 v35, v19\n"
    "v_cos_f32 v28, v20\n"
    "v_sin_f32 v36, v20\n"
    "v_cos_f32 v29, v21\n"
    "v_sin_f32 v37, v21\n"
    "v_cos_f32 v30, v22\n"
    "v_sin_f32 v38, v22\n"
    "v_cos_f32 v31, v23\n"
    "v_sin_f32 v39, v23\n"
    "v_cos_f32 v32, v24\n"
    "v_sin_f32 v40, v24\n"
    "v_cos_f32 v33, v25\n"
    "v_sin_f32 v41, v25\n"
    "v_cvt_pk_bf16_f32 v160, v26, v27\n"
    "v_cvt_pk_bf16_f32 v161, v28, v29\n"
    "v_cvt_pk_bf16_f32 v162, v30, v31\n"
    "v_cvt_pk_bf16_f32 v163, v32, v33\n"
    "buffer_load_dwordx4 v246, s[12:15], s39 offen offset:1024 lds\n"
    "v_cvt_pk_bf16_f32 v164, v34, v35\n"
    "v_cvt_pk_bf16_f32 v165, v36, v37\n"
    "v_cvt_pk_bf16_f32 v166, v38, v39\n"
    "v_cvt_pk_bf16_f32 v167, v40, v41\n"
    "v_pk_mul_f32 v[18:19], v[6:7], v[10:11] op_sel:[1,0]\n"
    "v_pk_mul_f32 v[20:21], v[6:7], v[12:13] op_sel:[1,0]\n"
    "v_pk_mul_f32 v[22:23], v[6:7], v[14:15] op_sel:[1,0]\n"
    "v_pk_mul_f32 v[24:25], v[6:7], v[16:17] op_sel:[1,0]\n"
    "v_cos_f32 v168, v18\n"
    "v_sin_f32 v176, v18\n"
    "v_cos_f32 v169, v19\n"
    "v_sin_f32 v177, v19\n"
    "v_cos_f32 v170, v20\n"
    "v_sin_f32 v178, v20\n"
    "v_cos_f32 v171, v21\n"
    "v_sin_f32 v179, v21\n"
    "v_cos_f32 v172, v22\n"
    "v_sin_f32 v180, v22\n"
    "v_cos_f32 v173, v23\n"
    "v_sin_f32 v181, v23\n"
    "s_add_u32 m0, s45, 114688\n"
    "v_cos_f32 v174, v24\n"
    "buffer_load_dwordx4 v245, s[8:11], s53 offen lds\n"
    "v_sin_f32 v182, v24\n"
    "v_cos_f32 v175, v25\n"
    "v_sin_f32 v183, v25\n"
    "v_pk_mul_f32 v[18:19], v[42:43], v[4:5] op_sel_hi:[1,0]\n"
    "v_pk_mul_f32 v[20:21], v[44:45], v[4:5] op_sel_hi:[1,0]\n"
    "v_pk_mul_f32 v[22:23], v[4:5], v[42:43] op_sel:[1,0]\n"
    "v_pk_mul_f32 v[24:25], v[4:5], v[44:45] op_sel:[1,0]\n"
    "v_cos_f32 v184, v18\n"
    "v_sin_f32 v192, v18\n"
    "v_cos_f32 v185, v19\n"
    "v_sin_f32 v193, v19\n"
    "v_cos_f32 v186, v20\n"
    "v_sin_f32 v194, v20\n"
    "v_cos_f32 v187, v21\n"
    "v_sin_f32 v195, v21\n"
    "v_cos_f32 v188, v22\n"
    "v_sin_f32 v196, v22\n"
    "v_cos_f32 v189, v23\n"
    "v_sin_f32 v197, v23\n"
    "v_cos_f32 v190, v24\n"
    "s_add_u32 m0, s47, 114688\n"
    "v_sin_f32 v198, v24\n"
    "buffer_load_dwordx4 v244, s[4:7], s58 offen lds\n"
    "v_cos_f32 v191, v25\n"
    "v_sin_f32 v199, v25\n"
    "v_pk_mul_f32 v[184:185], v[8:9], v[184:185] op_sel:[1,0]\n"
    "v_pk_mul_f32 v[186:187], v[8:9], v[186:187] op_sel:[1,0]\n"
    "v_pk_mul_f32 v[188:189], v[8:9], v[188:189] op_sel:[1,0]\n"
    "v_pk_mul_f32 v[190:191], v[8:9], v[190:191] op_sel:[1,0]\n"
    "v_pk_mul_f32 v[192:193], v[8:9], v[192:193] op_sel:[1,0]\n"
    "v_pk_mul_f32 v[194:195], v[8:9], v[194:195] op_sel:[1,0]\n"
    "v_pk_mul_f32 v[196:197], v[8:9], v[196:197] op_sel:[1,0]\n"
    "v_pk_mul_f32 v[198:199], v[8:9], v[198:199] op_sel:[1,0]\n"
    "s_waitcnt vmcnt(4)\n"
    "s_barrier\n"
    "ds_read_b64_tr_b16 v[64:65], v242\n"
    "ds_read_b64_tr_b16 v[66:67], v242 offset:1024\n"
    "ds_read_b128 v[96:99], v240\n"
    "ds_read_b128 v[100:103], v241\n"
    "ds_read_b64_tr_b16 v[72:73], v242 offset:8192\n"
    "ds_read_b64_tr_b16 v[74:75], v242 offset:9216\n"
    "ds_read_b64_tr_b16 v[76:77], v242 offset:24576\n"
    "ds_read_b64_tr_b16 v[78:79], v242 offset:25600\n"
    "ds_read_b64_tr_b16 v[68:69], v242 offset:16384\n"
    "ds_read_b64_tr_b16 v[70:71], v242 offset:17408\n"
    "ds_read_b128 v[104:107], v240 offset:4096\n"
    "ds_read_b128 v[108:111], v241 offset:4096\n"
    "ds_read_b128 v[80:83], v240 offset:8192\n"
    "ds_read_b128 v[84:87], v241 offset:8192\n"
    "s_waitcnt lgkmcnt(8)\n"
    "v_mfma_f32_16x16x32_bf16 v[0:3], v[64:67], v[96:99], 0\n"
    "ds_read_b128 v[88:91], v240 offset:12288\n"
    "s_add_u32 s38, s38, 128\n"
    "s_and_b32 s38, s38, 1023\n"
    "s_mul_i32 s39, s38, 1792\n"
    "s_add_u32 m0, s41, 16384\n"
    "v_mfma_f32_16x16x32_bf16 v[0:3], v[72:75], v[100:103], v[0:3]\n"
    "ds_read_b128 v[92:95], v241 offset:12288\n"
    "buffer_load_dwordx4 v245, s[8:11], s39 offen lds\n"
    "s_waitcnt lgkmcnt(6)\n"
    "v_mfma_f32_16x16x32_bf16 v[8:11], v[76:79], v[100:103], 0\n"
    "ds_read_b64_tr_b16 v[112:113], v243\n"
    "v_mfma_f32_16x16x32_bf16 v[8:11], v[68:71], v[96:99], v[8:11]\n"
    "ds_read_b64_tr_b16 v[114:115], v243 offset:1024\n"
    "buffer_load_dwordx4 v246, s[8:11], s39 offen offset:1024 lds\n"
    "v_smfmac_f32_16x16x64_bf16 v[216:219], v[220:223], v[64:71], v247\n"
    "ds_read_b64_tr_b16 v[120:121], v243 offset:8192\n"
    "s_add_u32 m0, s40, 16384\n"
    "v_smfmac_f32_16x16x64_bf16 v[216:219], v[220:223], v[72:79], v247\n"
    "ds_read_b64_tr_b16 v[122:123], v243 offset:9216\n"
    "buffer_load_dwordx4 v244, s[4:7], s38 offen lds\n"
    "s_waitcnt lgkmcnt(8)\n"
    "v_mfma_f32_16x16x32_bf16 v[24:27], v[68:71], v[104:107], 0\n"
    "ds_read_b64_tr_b16 v[124:125], v243 offset:24576\n"
    "v_mfma_f32_16x16x32_bf16 v[24:27], v[76:79], v[108:111], v[24:27]\n"
    "ds_read_b64_tr_b16 v[126:127], v243 offset:25600\n"
    "v_mfma_f32_16x16x32_bf16 v[16:19], v[72:75], v[108:111], 0\n"
    "ds_read_b64_tr_b16 v[116:117], v243 offset:16384\n"
    "buffer_load_dwordx4 v244, s[4:7], s38 offen offset:1024 lds\n"
    "v_mfma_f32_16x16x32_bf16 v[16:19], v[64:67], v[104:107], v[16:19]\n"
    "ds_read_b64_tr_b16 v[118:119], v243 offset:17408\n"
    "s_add_u32 m0, s42, 16384\n"
    "s_waitcnt lgkmcnt(10)\n"
    "v_mfma_f32_16x16x32_bf16 v[32:35], v[64:67], v[80:83], 0\n"
    "buffer_load_dwordx4 v245, s[12:15], s39 offen lds\n"
    "v_mfma_f32_16x16x32_bf16 v[32:35], v[72:75], v[84:87], v[32:35]\n"
    "v_mfma_f32_16x16x32_bf16 v[40:43], v[76:79], v[84:87], 0\n"
    "buffer_load_dwordx4 v246, s[12:15], s39 offen offset:1024 lds\n"
    "v_add_u32 v240, 0xc000, v240\n"
    "v_mfma_f32_16x16x32_bf16 v[40:43], v[68:71], v[80:83], v[40:43]\n"
    "v_add_u32 v241, 0xc000, v241\n"
    "s_waitcnt lgkmcnt(8)\n"
    "v_mfma_f32_16x16x32_bf16 v[56:59], v[68:71], v[88:91], 0\n"
    "s_lshl3_add_u32 s3, s2, s32\n"
    "s_lshl_b32 s3, s3, 10\n"
    "v_lshlrev_b32 v233, 3, v250\n"
    "v_add_u32 v242, 0xc000, v242\n"
    "v_mfma_f32_16x16x32_bf16 v[56:59], v[76:79], v[92:95], v[56:59]\n"
    "s_mov_b64 exec, 255\n"
    "buffer_load_dword v233, v233, s[16:19], s3 offen\n"
    "s_mov_b64 exec, -1\n"
    "v_add_u32 v243, 0xc000, v243\n"
    "v_mfma_f32_16x16x32_bf16 v[48:51], v[72:75], v[92:95], 0\n"
    "v_mfma_f32_16x16x32_bf16 v[48:51], v[64:67], v[88:91], v[48:51]\n"
    "s_waitcnt vmcnt(7) lgkmcnt(0)\n"
    "s_barrier\n"
    "ds_read_b64_tr_b16 v[64:65], v242\n"
    "v_mfma_f32_16x16x32_bf16 v[4:7], v[112:115], v[96:99], 0\n"
    "ds_read_b64_tr_b16 v[66:67], v242 offset:1024\n"
    "s_lshl1_add_u32 s30, s30, s61\n"
    "s_lshl_b32 s54, s30, 9\n"
    "s_add_u32 s54, s54, 16384\n"
    "v_mfma_f32_16x16x32_bf16 v[4:7], v[120:123], v[100:103], v[4:7]\n"
    "ds_read_b64_tr_b16 v[72:73], v242 offset:8192\n"
    "s_mul_i32 s30, s30, 56\n"
    "s_lshl1_add_u32 s30, s31, s30\n"
    "v_mfma_f32_16x16x32_bf16 v[12:15], v[124:127], v[100:103], 0\n"
    "ds_read_b64_tr_b16 v[74:75], v242 offset:9216\n"
    "s_add_u32 s38, s38, 128\n"
    "s_and_b32 s38, s38, 1023\n"
    "s_mul_i32 s39, s38, 1792\n"
    "s_add_u32 m0, s41, 65536\n"
    "s_bfe_u32 s3, s32, 0x10001\n"
    "s_add_u32 s30, s30, s3\n"
    "s_lshl_b32 s51, s30, 12\n"
    "v_mfma_f32_16x16x32_bf16 v[12:15], v[116:119], v[96:99], v[12:15]\n"
    "ds_read_b128 v[96:99], v240\n"
    "buffer_load_dwordx4 v245, s[8:11], s39 offen lds\n"
    "s_add_u32 s51, s51, 8388608\n"
    "s_lshl_b32 s52, s30, 5\n"
    "v_mfma_f32_16x16x32_bf16 v[28:31], v[116:119], v[104:107], 0\n"
    "ds_read_b128 v[100:103], v241\n"
    "s_add_u32 s52, s52, 43008\n"
    "v_and_b32 v251, 0xf0, v250\n"
    "v_lshlrev_b32 v251, 1, v251\n"
    "v_mfma_f32_16x16x32_bf16 v[28:31], v[124:127], v[108:111], v[28:31]\n"
    "ds_read_b64_tr_b16 v[76:77], v242 offset:24576\n"
    "s_and_b32 s60, s32, 3\n"
    "s_lshr_b32 s61, s32, 2\n"
    "v_mfma_f32_16x16x32_bf16 v[20:23], v[120:123], v[108:111], 0\n"
    "ds_read_b64_tr_b16 v[78:79], v242 offset:25600\n"
    "s_and_b32 s3, s34, 3\n"
    "s_lshl2_add_u32 s55, s33, s3\n"
    "v_mfma_f32_16x16x32_bf16 v[20:23], v[112:115], v[104:107], v[20:23]\n"
    "ds_read_b64_tr_b16 v[68:69], v242 offset:16384\n"
    "s_lshl_b32 s43, s60, 12\n"
    "s_lshl_b32 s3, s61, 11\n"
    "s_add_u32 s43, s43, s3\n"
    "v_mfma_f32_16x16x32_bf16 v[36:39], v[112:115], v[80:83], 0\n"
    "ds_read_b64_tr_b16 v[70:71], v242 offset:17408\n"
    "buffer_load_dwordx4 v246, s[8:11], s39 offen offset:1024 lds\n"
    "s_lshl_b32 s28, s34, 5\n"
    "s_or_b32 s28, s28, 0x60\n"
    "v_mfma_f32_16x16x32_bf16 v[36:39], v[120:123], v[84:87], v[36:39]\n"
    "ds_read_b128 v[104:107], v240 offset:4096\n"
    "s_sub_u32 s28, s28, s16\n"
    "s_xor_b32 s28, s28, 0x200\n"
    "s_and_b32 s28, s28, 0x380\n"
    "v_mfma_f32_16x16x32_bf16 v[44:47], v[124:127], v[84:87], 0\n"
    "ds_read_b128 v[108:111], v241 offset:4096\n"
    "s_lshr_b32 s31, s28, 1\n"
    "s_lshl_b32 s57, s55, 10\n"
    "v_mfma_f32_16x16x32_bf16 v[44:47], v[116:119], v[80:83], v[44:47]\n"
    "ds_read_b128 v[80:83], v240 offset:8192\n"
    "s_add_u32 s57, s57, s28\n"
    "s_xor_b32 s3, s61, s55\n"
    "v_mfma_f32_16x16x32_bf16 v[60:63], v[116:119], v[88:91], 0\n"
    "ds_read_b128 v[84:87], v241 offset:8192\n"
    "s_add_u32 m0, s40, 65536\n"
    "s_and_b32 s3, s3, 1\n"
    "s_lshl_b32 s3, s3, 14\n"
    "s_add_u32 s28, s28, s3\n"
    "v_mfma_f32_16x16x32_bf16 v[60:63], v[124:127], v[92:95], v[60:63]\n"
    "buffer_load_dwordx4 v244, s[4:7], s38 offen lds\n"
    "s_mul_i32 s3, s60, 229376\n"
    "s_add_u32 s28, s28, s3\n"
    "v_mfma_f32_16x16x32_bf16 v[52:55], v[120:123], v[92:95], 0\n"
    "v_lshrrev_b32 v208, 8, v250\n"
    "v_bfe_u32 v209, v250, 5, 2\n"
    "v_xor_b32 v209, v209, v208\n"
    "v_mfma_f32_16x16x32_bf16 v[52:55], v[112:115], v[88:91], v[52:55]\n"
    "v_and_b32 v210, 16, v250\n"
    "v_lshl_or_b32 v209, v209, 5, v210\n"
    "v_lshrrev_b32 v210, 7, v250\n"
    "v_lshl_or_b32 v209, v210, 10, v209\n"
    "s_waitcnt lgkmcnt(4)\n"
    "v_mfma_f32_16x16x32_bf16 v[0:3], v[64:67], v[96:99], v[0:3]\n"
    "ds_read_b128 v[88:91], v240 offset:12288\n"
    "v_add_u32 v248, s28, v209\n"
    "v_add_u32 v249, 7168, v248\n"
    "s_cmp_eq_u32 s33, 7\n"
    "s_cselect_b32 s3, 0, s33\n"
    "v_mfma_f32_16x16x32_bf16 v[0:3], v[72:75], v[100:103], v[0:3]\n"
    "ds_read_b128 v[92:95], v241 offset:12288\n"
    "s_lshl_b32 s44, s3, 12\n"
    "s_add_u32 s45, s44, 4096\n"
    "v_mfma_f32_16x16x32_bf16 v[8:11], v[76:79], v[100:103], v[8:11]\n"
    "ds_read_b64_tr_b16 v[112:113], v243\n"
    "s_cmp_eq_u32 s45, 28672\n"
    "s_cselect_b32 s45, 0, s45\n"
    "s_add_u32 s46, s45, 4096\n"
    "s_cmp_eq_u32 s46, 28672\n"
    "s_cselect_b32 s46, 0, s46\n"
    "v_mfma_f32_16x16x32_bf16 v[8:11], v[68:71], v[96:99], v[8:11]\n"
    "ds_read_b64_tr_b16 v[114:115], v243 offset:1024\n"
    "buffer_load_dwordx4 v244, s[4:7], s38 offen offset:1024 lds\n"
    "s_add_u32 s47, s46, 4096\n"
    "s_cmp_eq_u32 s47, 28672\n"
    "s_cselect_b32 s47, 0, s47\n"
    "v_smfmac_f32_16x16x64_bf16 v[216:219], v[220:223], v[64:71], v247\n"
    "ds_read_b64_tr_b16 v[120:121], v243 offset:8192\n"
    "s_add_u32 s48, s47, 4096\n"
    "s_cmp_eq_u32 s48, 28672\n"
    "s_cselect_b32 s48, 0, s48\n"
    "v_smfmac_f32_16x16x64_bf16 v[216:219], v[220:223], v[72:79], v247\n"
    "ds_read_b64_tr_b16 v[122:123], v243 offset:9216\n"
    "s_add_u32 s49, s48, 4096\n"
    "s_cmp_eq_u32 s49, 28672\n"
    "s_cselect_b32 s49, 0, s49\n"
    "s_add_u32 s50, s49, 4096\n"
    "s_waitcnt lgkmcnt(8)\n"
    "v_mfma_f32_16x16x32_bf16 v[24:27], v[68:71], v[104:107], v[24:27]\n"
    "ds_read_b64_tr_b16 v[124:125], v243 offset:24576\n"
    "s_cmp_eq_u32 s50, 28672\n"
    "s_cselect_b32 s50, 0, s50\n"
    "v_lshrrev_b32 v209, 6, v250\n"
    "v_mfma_f32_16x16x32_bf16 v[24:27], v[76:79], v[108:111], v[24:27]\n"
    "ds_read_b64_tr_b16 v[126:127], v243 offset:25600\n"
    "s_add_u32 m0, s42, 65536\n"
    "v_bfe_u32 v210, v250, 7, 2\n"
    "v_lshlrev_b32 v210, 5, v210\n"
    "v_mfma_f32_16x16x32_bf16 v[16:19], v[72:75], v[108:111], v[16:19]\n"
    "ds_read_b64_tr_b16 v[116:117], v243 offset:16384\n"
    "buffer_load_dwordx4 v245, s[12:15], s39 offen lds\n"
    "v_lshl_or_b32 v209, v209, 7, v210\n"
    "v_bfe_u32 v210, v250, 4, 2\n"
    "v_lshl_or_b32 v209, v210, 3, v209\n"
    "v_mfma_f32_16x16x32_bf16 v[16:19], v[64:67], v[104:107], v[16:19]\n"
    "ds_read_b64_tr_b16 v[118:119], v243 offset:17408\n"
    "s_lshl_b32 s3, s60, 12\n"
    "v_add_u32 v236, s3, v209\n"
    "s_waitcnt lgkmcnt(10)\n"
    "v_mfma_f32_16x16x32_bf16 v[32:35], v[64:67], v[80:83], v[32:35]\n"
    "v_xor_b32 v237, 32, v236\n"
    "v_xor_b32 v238, 64, v236\n"
    "v_mfma_f32_16x16x32_bf16 v[32:35], v[72:75], v[84:87], v[32:35]\n"
    "v_xor_b32 v239, 0x60, v236\n"
    "s_add_u32 s58, s2, 8\n"
    "s_and_b32 s58, s58, 255\n"
    "v_mfma_f32_16x16x32_bf16 v[40:43], v[76:79], v[84:87], v[40:43]\n"
    "s_lshl_b32 s53, s58, 13\n"
    "s_lshr_b32 s3, s58, 2\n"
    "v_add_u32 v240, 0xfffe8000, v240\n"
    "v_mfma_f32_16x16x32_bf16 v[40:43], v[68:71], v[80:83], v[40:43]\n"
    "buffer_load_dwordx4 v246, s[12:15], s39 offen offset:1024 lds\n"
    "s_mul_i32 s3, s3, 128\n"
    "s_add_u32 s53, s53, s3\n"
    "v_add_u32 v241, 0xfffe8000, v241\n"
    "s_waitcnt lgkmcnt(8)\n"
    "v_mfma_f32_16x16x32_bf16 v[56:59], v[68:71], v[88:91], v[56:59]\n"
    "s_lshl_b32 s3, s60, 11\n"
    "s_add_u32 s53, s53, s3\n"
    "s_add_u32 s53, s53, 1048576\n"
    "v_add_u32 v242, 0xfffe8000, v242\n"
    "v_mfma_f32_16x16x32_bf16 v[56:59], v[76:79], v[92:95], v[56:59]\n"
    "s_mul_i32 s56, s55, 56\n"
    "s_mul_i32 s3, s60, 7\n"
    "v_add_u32 v243, 0xfffe8000, v243\n"
    "v_mfma_f32_16x16x32_bf16 v[48:51], v[72:75], v[92:95], v[48:51]\n"
    "s_add_u32 s56, s56, s3\n"
    "s_mov_b32 s59, 0\n"
    "v_mfma_f32_16x16x32_bf16 v[48:51], v[64:67], v[88:91], v[48:51]\n"
    "v_readfirstlane_b32 s35, v252\n"
    "v_readfirstlane_b32 s37, v253\n"
    "s_add_u32 s36, s35, 0x632be59b\n"
    "s_xor_b32 s37, s37, 0x9e3779b9\n"
    "s_add_u32 s35, s35, 1\n"
    "s_sleep 5\n"
    "s_waitcnt vmcnt(7) lgkmcnt(0)\n"
    "s_barrier\n"
    "ds_read_b64_tr_b16 v[64:65], v242\n"
    "v_mfma_f32_16x16x32_bf16 v[4:7], v[112:115], v[96:99], v[4:7]\n"
    "ds_read_b64_tr_b16 v[66:67], v242 offset:1024\n"
    "s_lshl_b32 s62, s44, 3\n"
    "s_mov_b32 m0, s43\n"
    "v_mfma_f32_16x16x32_bf16 v[4:7], v[120:123], v[100:103], v[4:7]\n"
    "ds_read_b64_tr_b16 v[72:73], v242 offset:8192\n"
    "buffer_load_dwordx4 v248, s[16:19], s62 offen lds\n"
    "v_mfma_f32_16x16x32_bf16 v[12:15], v[124:127], v[100:103], v[12:15]\n"
    "ds_read_b64_tr_b16 v[74:75], v242 offset:9216\n"
    "v_mfma_f32_16x16x32_bf16 v[12:15], v[116:119], v[96:99], v[12:15]\n"
    "ds_read_b128 v[96:99], v240\n"
    "v_mfma_f32_16x16x32_bf16 v[28:31], v[116:119], v[104:107], v[28:31]\n"
    "ds_read_b128 v[100:103], v241\n"
    "buffer_load_dwordx4 v249, s[16:19], s62 offen offset:1024 lds\n"
    "v_mfma_f32_16x16x32_bf16 v[28:31], v[124:127], v[108:111], v[28:31]\n"
    "ds_read_b64_tr_b16 v[76:77], v242 offset:24576\n"
    "v_mfma_f32_16x16x32_bf16 v[20:23], v[120:123], v[108:111], v[20:23]\n"
    "ds_read_b64_tr_b16 v[78:79], v242 offset:25600\n"
    "v_mfma_f32_16x16x32_bf16 v[20:23], v[112:115], v[104:107], v[20:23]\n"
    "ds_read_b64_tr_b16 v[68:69], v242 offset:16384\n"
    "s_add_u32 s38, s38, 128\n"
    "s_and_b32 s38, s38, 1023\n"
    "s_mul_i32 s39, s38, 1792\n"
    "s_add_u32 m0, s41, 114688\n"
    "v_mfma_f32_16x16x32_bf16 v[36:39], v[112:115], v[80:83], v[36:39]\n"
    "ds_read_b64_tr_b16 v[70:71], v242 offset:17408\n"
    "buffer_load_dwordx4 v245, s[8:11], s39 offen lds\n"
    "v_mfma_f32_16x16x32_bf16 v[36:39], v[120:123], v[84:87], v[36:39]\n"
    "ds_read_b128 v[104:107], v240 offset:4096\n"
    "v_add_f32 v216, v216, v217\n"
    "v_add_f32 v218, v218, v219\n"
    "v_mfma_f32_16x16x32_bf16 v[44:47], v[124:127], v[84:87], v[44:47]\n"
    "ds_read_b128 v[108:111], v241 offset:4096\n"
    "s_lshl3_add_u32 s28, s2, s32\n"
    "s_lshl_b32 s28, s28, 8\n"
    "s_add_u32 s28, s28, 15728640\n"
    "v_mfma_f32_16x16x32_bf16 v[44:47], v[116:119], v[80:83], v[44:47]\n"
    "ds_read_b128 v[80:83], v240 offset:8192\n"
    "buffer_load_dwordx4 v246, s[8:11], s39 offen offset:1024 lds\n"
    "v_mfma_f32_16x16x32_bf16 v[60:63], v[116:119], v[88:91], v[60:63]\n"
    "ds_read_b128 v[84:87], v241 offset:8192\n"
    "v_mov_b32 v217, s36\n"
    "v_mov_b32 v219, s36\n"
    "v_mfma_f32_16x16x32_bf16 v[60:63], v[124:127], v[92:95], v[60:63]\n"
    "v_mfma_f32_16x16x32_bf16 v[52:55], v[120:123], v[92:95], v[52:55]\n"
    "s_mov_b64 exec, 0xffff\n"
    "buffer_store_dwordx4 v[216:219], v250, s[20:23], s28 offen sc1\n"
    "s_mov_b64 exec, -1\n"
    "s_add_u32 m0, s40, 114688\n"
    "v_mfma_f32_16x16x32_bf16 v[52:55], v[112:115], v[88:91], v[52:55]\n"
    "v_lshrrev_b32 v247, 8, v250\n"
    "v_and_b32 v220, 0xf0, v250\n"
    "v_lshl_or_b32 v247, v247, 12, v220\n"
    "s_andn2_b32 s28, s28, 0x3000\n"
    "buffer_load_dwordx4 v244, s[4:7], s38 offen lds\n"
    "s_waitcnt lgkmcnt(4)\n"
    "v_mfma_f32_16x16x32_bf16 v[0:3], v[64:67], v[96:99], v[0:3]\n"
    "ds_read_b128 v[88:91], v240 offset:12288\n"
    "v_mfma_f32_16x16x32_bf16 v[0:3], v[72:75], v[100:103], v[0:3]\n"
    "ds_read_b128 v[92:95], v241 offset:12288\n"
    "v_mfma_f32_16x16x32_bf16 v[8:11], v[76:79], v[100:103], v[8:11]\n"
    "ds_read_b64_tr_b16 v[112:113], v243\n"
    "v_mfma_f32_16x16x32_bf16 v[8:11], v[68:71], v[96:99], v[8:11]\n"
    "ds_read_b64_tr_b16 v[114:115], v243 offset:1024\n"
    "buffer_load_dwordx4 v244, s[4:7], s38 offen offset:1024 lds\n"
    "s_waitcnt lgkmcnt(6)\n"
    "v_mfma_f32_16x16x32_bf16 v[24:27], v[68:71], v[104:107], v[24:27]\n"
    "ds_read_b64_tr_b16 v[120:121], v243 offset:8192\n"
    "v_mfma_f32_16x16x32_bf16 v[24:27], v[76:79], v[108:111], v[24:27]\n"
    "ds_read_b64_tr_b16 v[122:123], v243 offset:9216\n"
    "v_mfma_f32_16x16x32_bf16 v[16:19], v[72:75], v[108:111], v[16:19]\n"
    "ds_read_b64_tr_b16 v[124:125], v243 offset:24576\n"
    "v_mfma_f32_16x16x32_bf16 v[16:19], v[64:67], v[104:107], v[16:19]\n"
    "ds_read_b64_tr_b16 v[126:127], v243 offset:25600\n"
    "s_add_u32 m0, s42, 114688\n"
    "s_waitcnt lgkmcnt(8)\n"
    "v_mfma_f32_16x16x32_bf16 v[32:35], v[64:67], v[80:83], v[32:35]\n"
    "ds_read_b64_tr_b16 v[116:117], v243 offset:16384\n"
    "buffer_load_dwordx4 v245, s[12:15], s39 offen lds\n"
    "v_mfma_f32_16x16x32_bf16 v[32:35], v[72:75], v[84:87], v[32:35]\n"
    "ds_read_b64_tr_b16 v[118:119], v243 offset:17408\n"
    "v_mfma_f32_16x16x32_bf16 v[40:43], v[76:79], v[84:87], v[40:43]\n"
    "v_mfma_f32_16x16x32_bf16 v[40:43], v[68:71], v[80:83], v[40:43]\n"
    "s_waitcnt lgkmcnt(8)\n"
    "v_mfma_f32_16x16x32_bf16 v[56:59], v[68:71], v[88:91], v[56:59]\n"
    "v_add_u32 v240, 0xc000, v240\n"
    "v_mfma_f32_16x16x32_bf16 v[56:59], v[76:79], v[92:95], v[56:59]\n"
    "buffer_load_dwordx4 v246, s[12:15], s39 offen offset:1024 lds\n"
    "v_add_u32 v241, 0xc000, v241\n"
    "v_mfma_f32_16x16x32_bf16 v[48:51], v[72:75], v[92:95], v[48:51]\n"
    "v_add_u32 v242, 0xc000, v242\n"
    "v_mfma_f32_16x16x32_bf16 v[48:51], v[64:67], v[88:91], v[48:51]\n"
    "v_add_u32 v243, 0xc000, v243\n"
    "s_waitcnt vmcnt(9) lgkmcnt(0)\n"
    "s_barrier\n"
    "ds_read_b64_tr_b16 v[64:65], v242\n"
    "v_mfma_f32_16x16x32_bf16 v[4:7], v[112:115], v[96:99], v[4:7]\n"
    "ds_read_b64_tr_b16 v[66:67], v242 offset:1024\n"
    "v_mfma_f32_16x16x32_bf16 v[4:7], v[120:123], v[100:103], v[4:7]\n"
    "ds_read_b64_tr_b16 v[72:73], v242 offset:8192\n"
    "v_mfma_f32_16x16x32_bf16 v[12:15], v[124:127], v[100:103], v[12:15]\n"
    "ds_read_b64_tr_b16 v[74:75], v242 offset:9216\n"
    "s_add_u32 s38, s38, 128\n"
    "s_and_b32 s38, s38, 1023\n"
    "s_mul_i32 s39, s38, 1792\n"
    "s_add_u32 m0, s41, 16384\n"
    "v_mfma_f32_16x16x32_bf16 v[12:15], v[116:119], v[96:99], v[12:15]\n"
    "ds_read_b128 v[96:99], v240\n"
    "buffer_load_dwordx4 v245, s[8:11], s39 offen lds\n"
    "v_mfma_f32_16x16x32_bf16 v[28:31], v[116:119], v[104:107], v[28:31]\n"
    "ds_read_b128 v[100:103], v241\n"
    "v_mfma_f32_16x16x32_bf16 v[28:31], v[124:127], v[108:111], v[28:31]\n"
    "ds_read_b64_tr_b16 v[76:77], v242 offset:24576\n"
    "v_mfma_f32_16x16x32_bf16 v[20:23], v[120:123], v[108:111], v[20:23]\n"
    "ds_read_b64_tr_b16 v[78:79], v242 offset:25600\n"
    "v_mfma_f32_16x16x32_bf16 v[20:23], v[112:115], v[104:107], v[20:23]\n"
    "ds_read_b64_tr_b16 v[68:69], v242 offset:16384\n"
    "v_mfma_f32_16x16x32_bf16 v[36:39], v[112:115], v[80:83], v[36:39]\n"
    "ds_read_b64_tr_b16 v[70:71], v242 offset:17408\n"
    "buffer_load_dwordx4 v246, s[8:11], s39 offen offset:1024 lds\n"
    "v_mfma_f32_16x16x32_bf16 v[36:39], v[120:123], v[84:87], v[36:39]\n"
    "ds_read_b128 v[104:107], v240 offset:4096\n"
    "v_mfma_f32_16x16x32_bf16 v[44:47], v[124:127], v[84:87], v[44:47]\n"
    "ds_read_b128 v[108:111], v241 offset:4096\n"
    "v_mfma_f32_16x16x32_bf16 v[44:47], v[116:119], v[80:83], v[44:47]\n"
    "ds_read_b128 v[80:83], v240 offset:8192\n"
    "v_mfma_f32_16x16x32_bf16 v[60:63], v[116:119], v[88:91], v[60:63]\n"
    "ds_read_b128 v[84:87], v241 offset:8192\n"
    "s_add_u32 m0, s40, 16384\n"
    "v_mfma_f32_16x16x32_bf16 v[60:63], v[124:127], v[92:95], v[60:63]\n"
    "buffer_load_dwordx4 v244, s[4:7], s38 offen lds\n"
    "v_mfma_f32_16x16x32_bf16 v[52:55], v[120:123], v[92:95], v[52:55]\n"
    "v_mfma_f32_16x16x32_bf16 v[52:55], v[112:115], v[88:91], v[52:55]\n"
    "s_waitcnt lgkmcnt(4)\n"
    "v_mfma_f32_16x16x32_bf16 v[0:3], v[64:67], v[96:99], v[0:3]\n"
    "ds_read_b128 v[88:91], v240 offset:12288\n"
    "v_mfma_f32_16x16x32_bf16 v[0:3], v[72:75], v[100:103], v[0:3]\n"
    "ds_read_b128 v[92:95], v241 offset:12288\n"
    "v_mfma_f32_16x16x32_bf16 v[8:11], v[76:79], v[100:103], v[8:11]\n"
    "ds_read_b64_tr_b16 v[112:113], v243\n"
    "v_mfma_f32_16x16x32_bf16 v[8:11], v[68:71], v[96:99], v[8:11]\n"
    "ds_read_b64_tr_b16 v[114:115], v243 offset:1024\n"
    "buffer_load_dwordx4 v244, s[4:7], s38 offen offset:1024 lds\n"
    "s_waitcnt lgkmcnt(6)\n"
    "v_mfma_f32_16x16x32_bf16 v[24:27], v[68:71], v[104:107], v[24:27]\n"
    "ds_read_b64_tr_b16 v[120:121], v243 offset:8192\n"
    "v_mfma_f32_16x16x32_bf16 v[24:27], v[76:79], v[108:111], v[24:27]\n"
    "ds_read_b64_tr_b16 v[122:123], v243 offset:9216\n"
    "v_mfma_f32_16x16x32_bf16 v[16:19], v[72:75], v[108:111], v[16:19]\n"
    "ds_read_b64_tr_b16 v[124:125], v243 offset:24576\n"
    "v_mfma_f32_16x16x32_bf16 v[16:19], v[64:67], v[104:107], v[16:19]\n"
    "ds_read_b64_tr_b16 v[126:127], v243 offset:25600\n"
    "s_add_u32 m0, s42, 16384\n"
    "s_waitcnt lgkmcnt(8)\n"
    "v_mfma_f32_16x16x32_bf16 v[32:35], v[64:67], v[80:83], v[32:35]\n"
    "ds_read_b64_tr_b16 v[116:117], v243 offset:16384\n"
    "buffer_load_dwordx4 v245, s[12:15], s39 offen lds\n"
    "v_mfma_f32_16x16x32_bf16 v[32:35], v[72:75], v[84:87], v[32:35]\n"
    "ds_read_b64_tr_b16 v[118:119], v243 offset:17408\n"
    "v_mfma_f32_16x16x32_bf16 v[40:43], v[76:79], v[84:87], v[40:43]\n"
    "v_mfma_f32_16x16x32_bf16 v[40:43], v[68:71], v[80:83], v[40:43]\n"
    "s_waitcnt lgkmcnt(8)\n"
    "v_mfma_f32_16x16x32_bf16 v[56:59], v[68:71], v[88:91], v[56:59]\n"
    "s_lshl3_add_u32 s3, s34, s32\n"
    "s_lshl_b32 s3, s3, 13\n"
    "v_add_u32 v240, 0xc000, v240\n"
    "v_mfma_f32_16x16x32_bf16 v[56:59], v[76:79], v[92:95], v[56:59]\n"
    "buffer_load_dwordx4 v246, s[12:15], s39 offen offset:1024 lds\n"
    "s_sub_u32 s60, 0x1e0, s16\n"
    "s_and_b32 s60, s60, 0x380\n"
    "v_add_u32 v241, 0xc000, v241\n"
    "v_mfma_f32_16x16x32_bf16 v[48:51], v[72:75], v[92:95], v[48:51]\n"
    "s_add_u32 s3, s3, s60\n"
    "v_lshlrev_b32 v234, 6, v250\n"
    "v_add_u32 v242, 0xc000, v242\n"
    "v_mfma_f32_16x16x32_bf16 v[48:51], v[64:67], v[88:91], v[48:51]\n"
    "v_add_u32 v243, 0xc000, v243\n"
    "s_mov_b64 exec, 0xff\n"
    "buffer_load_dword v234, v234, s[16:19], s3 offen\n"
    "s_mov_b64 exec, -1\n"
    "s_waitcnt vmcnt(7) lgkmcnt(0)\n"
    "s_barrier\n"
    "ds_read_b64_tr_b16 v[64:65], v242\n"
    "v_mfma_f32_16x16x32_bf16 v[4:7], v[112:115], v[96:99], v[4:7]\n"
    "ds_read_b64_tr_b16 v[66:67], v242 offset:1024\n"
    "v_mfma_f32_16x16x32_bf16 v[4:7], v[120:123], v[100:103], v[4:7]\n"
    "ds_read_b64_tr_b16 v[72:73], v242 offset:8192\n"
    "v_mfma_f32_16x16x32_bf16 v[12:15], v[124:127], v[100:103], v[12:15]\n"
    "ds_read_b64_tr_b16 v[74:75], v242 offset:9216\n"
    "s_add_u32 s38, s38, 128\n"
    "s_and_b32 s38, s38, 1023\n"
    "s_mul_i32 s39, s38, 1792\n"
    "s_add_u32 m0, s41, 65536\n"
    "v_mfma_f32_16x16x32_bf16 v[12:15], v[116:119], v[96:99], v[12:15]\n"
    "ds_read_b128 v[96:99], v240\n"
    "buffer_load_dwordx4 v245, s[8:11], s39 offen lds\n"
    "v_mfma_f32_16x16x32_bf16 v[28:31], v[116:119], v[104:107], v[28:31]\n"
    "ds_read_b128 v[100:103], v241\n"
    "v_mfma_f32_16x16x32_bf16 v[28:31], v[124:127], v[108:111], v[28:31]\n"
    "ds_read_b64_tr_b16 v[76:77], v242 offset:24576\n"
    "v_mfma_f32_16x16x32_bf16 v[20:23], v[120:123], v[108:111], v[20:23]\n"
    "ds_read_b64_tr_b16 v[78:79], v242 offset:25600\n"
    "v_mfma_f32_16x16x32_bf16 v[20:23], v[112:115], v[104:107], v[20:23]\n"
    "ds_read_b64_tr_b16 v[68:69], v242 offset:16384\n"
    "v_mfma_f32_16x16x32_bf16 v[36:39], v[112:115], v[80:83], v[36:39]\n"
    "ds_read_b64_tr_b16 v[70:71], v242 offset:17408\n"
    "buffer_load_dwordx4 v246, s[8:11], s39 offen offset:1024 lds\n"
    "v_mfma_f32_16x16x32_bf16 v[36:39], v[120:123], v[84:87], v[36:39]\n"
    "ds_read_b128 v[104:107], v240 offset:4096\n"
    "v_mfma_f32_16x16x32_bf16 v[44:47], v[124:127], v[84:87], v[44:47]\n"
    "ds_read_b128 v[108:111], v241 offset:4096\n"
    "v_mfma_f32_16x16x32_bf16 v[44:47], v[116:119], v[80:83], v[44:47]\n"
    "ds_read_b128 v[80:83], v240 offset:8192\n"
    "v_mfma_f32_16x16x32_bf16 v[60:63], v[116:119], v[88:91], v[60:63]\n"
    "ds_read_b128 v[84:87], v241 offset:8192\n"
    "s_add_u32 m0, s40, 65536\n"
    "ds_read_b64_tr_b16 v[128:129], v236\n"
    "ds_read_b64_tr_b16 v[130:131], v236 offset:2048\n"
    "v_mfma_f32_16x16x32_bf16 v[60:63], v[124:127], v[92:95], v[60:63]\n"
    "buffer_load_dwordx4 v244, s[4:7], s38 offen lds\n"
    "ds_read_b64_tr_b16 v[132:133], v237\n"
    "ds_read_b64_tr_b16 v[134:135], v237 offset:2048\n"
    "v_mfma_f32_16x16x32_bf16 v[52:55], v[120:123], v[92:95], v[52:55]\n"
    "ds_read_b64_tr_b16 v[136:137], v238\n"
    "ds_read_b64_tr_b16 v[138:139], v238 offset:2048\n"
    "v_mfma_f32_16x16x32_bf16 v[52:55], v[112:115], v[88:91], v[52:55]\n"
    "ds_read_b64_tr_b16 v[140:141], v239\n"
    "ds_read_b64_tr_b16 v[142:143], v239 offset:2048\n"
    "s_waitcnt lgkmcnt(12)\n"
    "v_mfma_f32_16x16x32_bf16 v[0:3], v[64:67], v[96:99], v[0:3]\n"
    "ds_read_b128 v[88:91], v240 offset:12288\n"
    "v_mfma_f32_16x16x32_bf16 v[0:3], v[72:75], v[100:103], v[0:3]\n"
    "ds_read_b128 v[92:95], v241 offset:12288\n"
    "v_mfma_f32_16x16x32_bf16 v[8:11], v[76:79], v[100:103], v[8:11]\n"
    "ds_read_b64_tr_b16 v[112:113], v243\n"
    "v_mfma_f32_16x16x32_bf16 v[8:11], v[68:71], v[96:99], v[8:11]\n"
    "ds_read_b64_tr_b16 v[114:115], v243 offset:1024\n"
    "buffer_load_dwordx4 v244, s[4:7], s38 offen offset:1024 lds\n"
    "s_waitcnt lgkmcnt(14)\n"
    "v_mfma_f32_16x16x32_bf16 v[24:27], v[68:71], v[104:107], v[24:27]\n"
    "ds_read_b64_tr_b16 v[120:121], v243 offset:8192\n"
    "v_mfma_f32_16x16x32_bf16 v[24:27], v[76:79], v[108:111], v[24:27]\n"
    "ds_read_b64_tr_b16 v[122:123], v243 offset:9216\n"
    "v_mfma_f32_16x16x32_bf16 v[16:19], v[72:75], v[108:111], v[16:19]\n"
    "ds_read_b64_tr_b16 v[124:125], v243 offset:24576\n"
    "v_mfma_f32_16x16x32_bf16 v[16:19], v[64:67], v[104:107], v[16:19]\n"
    "ds_read_b64_tr_b16 v[126:127], v243 offset:25600\n"
    "s_add_u32 m0, s42, 65536\n"
    "s_waitcnt lgkmcnt(14)\n"
    "v_mfma_f32_16x16x32_bf16 v[32:35], v[64:67], v[80:83], v[32:35]\n"
    "ds_read_b64_tr_b16 v[116:117], v243 offset:16384\n"
    "buffer_load_dwordx4 v245, s[12:15], s39 offen lds\n"
    "v_mfma_f32_16x16x32_bf16 v[32:35], v[72:75], v[84:87], v[32:35]\n"
    "ds_read_b64_tr_b16 v[118:119], v243 offset:17408\n"
    "v_mfma_f32_16x16x32_bf16 v[40:43], v[76:79], v[84:87], v[40:43]\n"
    "v_mfma_f32_16x16x32_bf16 v[40:43], v[68:71], v[80:83], v[40:43]\n"
    "s_waitcnt lgkmcnt(8)\n"
    "v_mfma_f32_16x16x32_bf16 v[56:59], v[68:71], v[88:91], v[56:59]\n"
    "v_add_u32 v240, 0xfffe8000, v240\n"
    "v_mfma_f32_16x16x32_bf16 v[56:59], v[76:79], v[92:95], v[56:59]\n"
    "buffer_load_dwordx4 v246, s[12:15], s39 offen offset:1024 lds\n"
    "v_add_u32 v241, 0xfffe8000, v241\n"
    "v_mfma_f32_16x16x32_bf16 v[48:51], v[72:75], v[92:95], v[48:51]\n"
    "v_add_u32 v242, 0xfffe8000, v242\n"
    "v_mfma_f32_16x16x32_bf16 v[48:51], v[64:67], v[88:91], v[48:51]\n"
    "v_add_u32 v243, 0xfffe8000, v243\n"
    "s_waitcnt vmcnt(7) lgkmcnt(0)\n"
    "s_barrier\n"
    "ds_read_b64_tr_b16 v[64:65], v242\n"
    "v_mfma_f32_16x16x32_bf16 v[4:7], v[112:115], v[96:99], v[4:7]\n"
    "ds_read_b64_tr_b16 v[66:67], v242 offset:1024\n"
    "s_lshl3_add_u32 s62, s44, 917504\n"
    "s_mov_b32 m0, s43\n"
    "v_mfma_f32_16x16x32_bf16 v[4:7], v[120:123], v[100:103], v[4:7]\n"
    "ds_read_b64_tr_b16 v[72:73], v242 offset:8192\n"
    "buffer_load_dwordx4 v248, s[16:19], s62 offen lds\n"
    "v_mfma_f32_16x16x32_bf16 v[12:15], v[124:127], v[100:103], v[12:15]\n"
    "ds_read_b64_tr_b16 v[74:75], v242 offset:9216\n"
    "v_mfma_f32_16x16x32_bf16 v[12:15], v[116:119], v[96:99], v[12:15]\n"
    "ds_read_b128 v[96:99], v240\n"
    "v_mfma_f32_16x16x32_bf16 v[28:31], v[116:119], v[104:107], v[28:31]\n"
    "ds_read_b128 v[100:103], v241\n"
    "buffer_load_dwordx4 v249, s[16:19], s62 offen offset:1024 lds\n"
    "v_mfma_f32_16x16x32_bf16 v[28:31], v[124:127], v[108:111], v[28:31]\n"
    "ds_read_b64_tr_b16 v[76:77], v242 offset:24576\n"
    "v_mfma_f32_16x16x32_bf16 v[20:23], v[120:123], v[108:111], v[20:23]\n"
    "ds_read_b64_tr_b16 v[78:79], v242 offset:25600\n"
    "v_mfma_f32_16x16x32_bf16 v[20:23], v[112:115], v[104:107], v[20:23]\n"
    "ds_read_b64_tr_b16 v[68:69], v242 offset:16384\n"
    "s_add_u32 s38, s38, 128\n"
    "s_and_b32 s38, s38, 1023\n"
    "s_mul_i32 s39, s38, 1792\n"
    "s_add_u32 m0, s41, 114688\n"
    "v_mfma_f32_16x16x32_bf16 v[36:39], v[112:115], v[80:83], v[36:39]\n"
    "ds_read_b64_tr_b16 v[70:71], v242 offset:17408\n"
    "buffer_load_dwordx4 v245, s[8:11], s39 offen lds\n"
    "v_mfma_f32_16x16x32_bf16 v[36:39], v[120:123], v[84:87], v[36:39]\n"
    "ds_read_b128 v[104:107], v240 offset:4096\n"
    "v_mfma_f32_16x16x32_bf16 v[44:47], v[124:127], v[84:87], v[44:47]\n"
    "ds_read_b128 v[108:111], v241 offset:4096\n"
    "v_mfma_f32_16x16x32_bf16 v[44:47], v[116:119], v[80:83], v[44:47]\n"
    "ds_read_b128 v[80:83], v240 offset:8192\n"
    "buffer_load_dwordx4 v246, s[8:11], s39 offen offset:1024 lds\n"
    "v_mfma_f32_16x16x32_bf16 v[60:63], v[116:119], v[88:91], v[60:63]\n"
    "ds_read_b128 v[84:87], v241 offset:8192\n"
    "v_mfma_f32_16x16x32_bf16 v[60:63], v[124:127], v[92:95], v[60:63]\n"
    "v_mfma_f32_16x16x32_bf16 v[52:55], v[120:123], v[92:95], v[52:55]\n"
    "s_add_u32 m0, s40, 114688\n"
    "v_mfma_f32_16x16x32_bf16 v[52:55], v[112:115], v[88:91], v[52:55]\n"
    "buffer_load_dwordx4 v244, s[4:7], s38 offen lds\n"
    "s_waitcnt lgkmcnt(4)\n"
    "v_mfma_f32_16x16x32_bf16 v[0:3], v[64:67], v[96:99], v[0:3]\n"
    "ds_read_b128 v[88:91], v240 offset:12288\n"
    "v_mfma_f32_16x16x32_bf16 v[0:3], v[72:75], v[100:103], v[0:3]\n"
    "ds_read_b128 v[92:95], v241 offset:12288\n"
    "buffer_load_dwordx4 v244, s[4:7], s38 offen offset:1024 lds\n"
    "v_mfma_f32_16x16x32_bf16 v[8:11], v[76:79], v[100:103], v[8:11]\n"
    "ds_read_b64_tr_b16 v[112:113], v243\n"
    "v_mfma_f32_16x16x32_bf16 v[8:11], v[68:71], v[96:99], v[8:11]\n"
    "ds_read_b64_tr_b16 v[114:115], v243 offset:1024\n"
    "s_waitcnt lgkmcnt(6)\n"
    "v_mfma_f32_16x16x32_bf16 v[24:27], v[68:71], v[104:107], v[24:27]\n"
    "ds_read_b64_tr_b16 v[120:121], v243 offset:8192\n"
    "buffer_load_dwordx4 v[224:227], v251, s[20:23], s54 offen sc1\n"
    "v_mfma_f32_16x16x32_bf16 v[24:27], v[76:79], v[108:111], v[24:27]\n"
    "ds_read_b64_tr_b16 v[122:123], v243 offset:9216\n"
    "v_mfma_f32_16x16x32_bf16 v[16:19], v[72:75], v[108:111], v[16:19]\n"
    "ds_read_b64_tr_b16 v[124:125], v243 offset:24576\n"
    "v_mfma_f32_16x16x32_bf16 v[16:19], v[64:67], v[104:107], v[16:19]\n"
    "ds_read_b64_tr_b16 v[126:127], v243 offset:25600\n"
    "s_waitcnt lgkmcnt(8)\n"
    "v_mfma_f32_16x16x32_bf16 v[32:35], v[64:67], v[80:83], v[32:35]\n"
    "ds_read_b64_tr_b16 v[116:117], v243 offset:16384\n"
    "buffer_load_dwordx4 v[228:231], v251, s[20:23], s54 offen offset:16 sc1\n"
    "v_mfma_f32_16x16x32_bf16 v[32:35], v[72:75], v[84:87], v[32:35]\n"
    "ds_read_b64_tr_b16 v[118:119], v243 offset:17408\n"
    "v_mfma_f32_16x16x32_bf16 v[40:43], v[76:79], v[84:87], v[40:43]\n"
    "s_add_u32 m0, s42, 114688\n"
    "v_mfma_f32_16x16x32_bf16 v[40:43], v[68:71], v[80:83], v[40:43]\n"
    "buffer_load_dwordx4 v245, s[12:15], s39 offen lds\n"
    "s_waitcnt lgkmcnt(8)\n"
    "v_mfma_f32_16x16x32_bf16 v[56:59], v[68:71], v[88:91], v[56:59]\n"
    "v_add_u32 v240, 0xc000, v240\n"
    "v_mfma_f32_16x16x32_bf16 v[56:59], v[76:79], v[92:95], v[56:59]\n"
    "v_add_u32 v241, 0xc000, v241\n"
    "v_mfma_f32_16x16x32_bf16 v[48:51], v[72:75], v[92:95], v[48:51]\n"
    "v_add_u32 v242, 0xc000, v242\n"
    "v_mfma_f32_16x16x32_bf16 v[48:51], v[64:67], v[88:91], v[48:51]\n"
    "buffer_load_dwordx4 v246, s[12:15], s39 offen offset:1024 lds\n"
    "v_add_u32 v243, 0xc000, v243\n"
    "s_waitcnt vmcnt(10) lgkmcnt(0)\n"
    "s_barrier\n"
    "ds_read_b64_tr_b16 v[64:65], v242\n"
    "v_mfma_f32_16x16x32_bf16 v[4:7], v[112:115], v[96:99], v[4:7]\n"
    "ds_read_b64_tr_b16 v[66:67], v242 offset:1024\n"
    "v_mfma_f32_16x16x32_bf16 v[4:7], v[120:123], v[100:103], v[4:7]\n"
    "ds_read_b64_tr_b16 v[72:73], v242 offset:8192\n"
    "buffer_load_dwordx4 v[216:219], v247, s[20:23], s28 offen sc1\n"
    "v_mfma_f32_16x16x32_bf16 v[12:15], v[124:127], v[100:103], v[12:15]\n"
    "ds_read_b64_tr_b16 v[74:75], v242 offset:9216\n"
    "v_mfma_f32_16x16x32_bf16 v[12:15], v[116:119], v[96:99], v[12:15]\n"
    "ds_read_b128 v[96:99], v240\n"
    "v_mfma_f32_16x16x32_bf16 v[28:31], v[116:119], v[104:107], v[28:31]\n"
    "ds_read_b128 v[100:103], v241\n"
    "s_lshl_b32 s62, s45, 3\n"
    "s_add_u32 m0, s43, 16384\n"
    "v_mfma_f32_16x16x32_bf16 v[28:31], v[124:127], v[108:111], v[28:31]\n"
    "ds_read_b64_tr_b16 v[76:77], v242 offset:24576\n"
    "buffer_load_dwordx4 v248, s[16:19], s62 offen lds\n"
    "v_mfma_f32_16x16x32_bf16 v[20:23], v[120:123], v[108:111], v[20:23]\n"
    "ds_read_b64_tr_b16 v[78:79], v242 offset:25600\n"
    "v_mfma_f32_16x16x32_bf16 v[20:23], v[112:115], v[104:107], v[20:23]\n"
    "ds_read_b64_tr_b16 v[68:69], v242 offset:16384\n"
    "v_mfma_f32_16x16x32_bf16 v[36:39], v[112:115], v[80:83], v[36:39]\n"
    "ds_read_b64_tr_b16 v[70:71], v242 offset:17408\n"
    "v_mfma_f32_16x16x32_bf16 v[36:39], v[120:123], v[84:87], v[36:39]\n"
    "ds_read_b128 v[104:107], v240 offset:4096\n"
    "v_mfma_f32_16x16x32_bf16 v[44:47], v[124:127], v[84:87], v[44:47]\n"
    "ds_read_b128 v[108:111], v241 offset:4096\n"
    "buffer_load_dwordx4 v249, s[16:19], s62 offen offset:1024 lds\n"
    "v_mfma_f32_16x16x32_bf16 v[44:47], v[116:119], v[80:83], v[44:47]\n"
    "ds_read_b128 v[80:83], v240 offset:8192\n"
    "v_mfma_f32_16x16x32_bf16 v[60:63], v[116:119], v[88:91], v[60:63]\n"
    "ds_read_b128 v[84:87], v241 offset:8192\n"
    "v_mfma_f32_16x16x32_bf16 v[60:63], v[124:127], v[92:95], v[60:63]\n"
    "v_mfma_f32_16x16x32_bf16 v[52:55], v[120:123], v[92:95], v[52:55]\n"
    "s_lshl_b32 s62, s46, 3\n"
    "s_add_u32 m0, s43, 32768\n"
    "v_mfma_f32_16x16x32_bf16 v[52:55], v[112:115], v[88:91], v[52:55]\n"
    "buffer_load_dwordx4 v248, s[16:19], s62 offen lds\n"
    "s_waitcnt lgkmcnt(4)\n"
    "v_mfma_f32_16x16x32_bf16 v[0:3], v[64:67], v[96:99], v[0:3]\n"
    "ds_read_b128 v[88:91], v240 offset:12288\n"
    "v_mfma_f32_16x16x32_bf16 v[0:3], v[72:75], v[100:103], v[0:3]\n"
    "ds_read_b128 v[92:95], v241 offset:12288\n"
    "v_mfma_f32_16x16x32_bf16 v[8:11], v[76:79], v[100:103], v[8:11]\n"
    "ds_read_b64_tr_b16 v[112:113], v243\n"
    "v_mfma_f32_16x16x32_bf16 v[8:11], v[68:71], v[96:99], v[8:11]\n"
    "ds_read_b64_tr_b16 v[114:115], v243 offset:1024\n"
    "buffer_load_dwordx4 v249, s[16:19], s62 offen offset:1024 lds\n"
    "s_waitcnt lgkmcnt(6)\n"
    "v_mfma_f32_16x16x32_bf16 v[24:27], v[68:71], v[104:107], v[24:27]\n"
    "ds_read_b64_tr_b16 v[120:121], v243 offset:8192\n"
    "v_mfma_f32_16x16x32_bf16 v[24:27], v[76:79], v[108:111], v[24:27]\n"
    "ds_read_b64_tr_b16 v[122:123], v243 offset:9216\n"
    "v_mfma_f32_16x16x32_bf16 v[16:19], v[72:75], v[108:111], v[16:19]\n"
    "ds_read_b64_tr_b16 v[124:125], v243 offset:24576\n"
    "v_mfma_f32_16x16x32_bf16 v[16:19], v[64:67], v[104:107], v[16:19]\n"
    "ds_read_b64_tr_b16 v[126:127], v243 offset:25600\n"
    "s_lshl_b32 s62, s47, 3\n"
    "s_add_u32 m0, s43, 49152\n"
    "s_waitcnt lgkmcnt(8)\n"
    "v_mfma_f32_16x16x32_bf16 v[32:35], v[64:67], v[80:83], v[32:35]\n"
    "ds_read_b64_tr_b16 v[116:117], v243 offset:16384\n"
    "buffer_load_dwordx4 v248, s[16:19], s62 offen lds\n"
    "v_mfma_f32_16x16x32_bf16 v[32:35], v[72:75], v[84:87], v[32:35]\n"
    "ds_read_b64_tr_b16 v[118:119], v243 offset:17408\n"
    "v_mfma_f32_16x16x32_bf16 v[40:43], v[76:79], v[84:87], v[40:43]\n"
    "v_mfma_f32_16x16x32_bf16 v[40:43], v[68:71], v[80:83], v[40:43]\n"
    "s_waitcnt lgkmcnt(8)\n"
    "v_mfma_f32_16x16x32_bf16 v[56:59], v[68:71], v[88:91], v[56:59]\n"
    "v_add_u32 v240, 0xc000, v240\n"
    "v_mfma_f32_16x16x32_bf16 v[56:59], v[76:79], v[92:95], v[56:59]\n"
    "buffer_load_dwordx4 v249, s[16:19], s62 offen offset:1024 lds\n"
    "v_add_u32 v241, 0xc000, v241\n"
    "v_mfma_f32_16x16x32_bf16 v[48:51], v[72:75], v[92:95], v[48:51]\n"
    "v_add_u32 v242, 0xc000, v242\n"
    "v_mfma_f32_16x16x32_bf16 v[48:51], v[64:67], v[88:91], v[48:51]\n"
    "v_add_u32 v243, 0xc000, v243\n"
    "s_waitcnt vmcnt(7) lgkmcnt(0)\n"
    "s_barrier\n"
    "ds_read_b64_tr_b16 v[64:65], v242\n"
    "v_mfma_f32_16x16x32_bf16 v[4:7], v[112:115], v[96:99], v[4:7]\n"
    "ds_read_b64_tr_b16 v[66:67], v242 offset:1024\n"
    "v_xor_b32 v220, s36, v225\n"
    "v_bitop3_b32 v220, v227, s36, v220 bitop3:0xbe\n"
    "v_mfma_f32_16x16x32_bf16 v[4:7], v[120:123], v[100:103], v[4:7]\n"
    "ds_read_b64_tr_b16 v[72:73], v242 offset:8192\n"
    "v_bitop3_b32 v220, v229, s36, v220 bitop3:0xbe\n"
    "v_bitop3_b32 v220, v231, s36, v220 bitop3:0xbe\n"
    "v_mfma_f32_16x16x32_bf16 v[12:15], v[124:127], v[100:103], v[12:15]\n"
    "ds_read_b64_tr_b16 v[74:75], v242 offset:9216\n"
    "s_lshl_b32 s62, s48, 3\n"
    "s_add_u32 m0, s43, 65536\n"
    "v_mfma_f32_16x16x32_bf16 v[12:15], v[116:119], v[96:99], v[12:15]\n"
    "ds_read_b128 v[96:99], v240\n"
    "buffer_load_dwordx4 v248, s[16:19], s62 offen lds\n"
    "v_cmp_ne_u32 vcc, 0, v220\n"
    "v_mfma_f32_16x16x32_bf16 v[28:31], v[116:119], v[104:107], v[28:31]\n"
    "ds_read_b128 v[100:103], v241\n"
    "s_cbranch_vccnz Lsp0_stA\n"
    "Lret_stA:\n"
    "v_and_b32 v225, 0xffff0000, v224\n"
    "v_lshlrev_b32 v224, 16, v224\n"
    "v_mfma_f32_16x16x32_bf16 v[28:31], v[124:127], v[108:111], v[28:31]\n"
    "ds_read_b64_tr_b16 v[76:77], v242 offset:24576\n"
    "v_rcp_f32 v209, v225\n"
    "v_mfma_f32_16x16x32_bf16 v[20:23], v[120:123], v[108:111], v[20:23]\n"
    "ds_read_b64_tr_b16 v[78:79], v242 offset:25600\n"
    "v_mul_f32 v225, v225, v225\n"
    "v_mfma_f32_16x16x32_bf16 v[20:23], v[112:115], v[104:107], v[20:23]\n"
    "ds_read_b64_tr_b16 v[68:69], v242 offset:16384\n"
    "v_mul_f32 v208, 0xc0135761, v209\n"
    "v_mul_f32 v209, v209, v209\n"
    "v_mfma_f32_16x16x32_bf16 v[36:39], v[112:115], v[80:83], v[36:39]\n"
    "ds_read_b64_tr_b16 v[70:71], v242 offset:17408\n"
    "buffer_load_dwordx4 v249, s[16:19], s62 offen offset:1024 lds\n"
    "v_mul_f32 v209, v209, v208\n"
    "v_mfma_f32_16x16x32_bf16 v[36:39], v[120:123], v[84:87], v[36:39]\n"
    "ds_read_b128 v[104:107], v240 offset:4096\n"
    "v_mul_f32 v209, 0x3d372713, v209\n"
    "v_mfma_f32_16x16x32_bf16 v[44:47], v[124:127], v[84:87], v[44:47]\n"
    "ds_read_b128 v[108:111], v241 offset:4096\n"
    "v_and_b32 v227, 0xffff0000, v226\n"
    "v_lshlrev_b32 v226, 16, v226\n"
    "v_mfma_f32_16x16x32_bf16 v[44:47], v[116:119], v[80:83], v[44:47]\n"
    "ds_read_b128 v[80:83], v240 offset:8192\n"
    "v_rcp_f32 v211, v227\n"
    "v_mfma_f32_16x16x32_bf16 v[60:63], v[116:119], v[88:91], v[60:63]\n"
    "ds_read_b128 v[84:87], v241 offset:8192\n"
    "s_lshl_b32 s62, s49, 3\n"
    "s_add_u32 m0, s43, 81920\n"
    "ds_read_b64_tr_b16 v[144:145], v236\n"
    "ds_read_b64_tr_b16 v[146:147], v236 offset:2048\n"
    "v_mul_f32 v227, v227, v227\n"
    "v_mfma_f32_16x16x32_bf16 v[60:63], v[124:127], v[92:95], v[60:63]\n"
    "buffer_load_dwordx4 v248, s[16:19], s62 offen lds\n"
    "ds_read_b64_tr_b16 v[148:149], v237\n"
    "ds_read_b64_tr_b16 v[150:151], v237 offset:2048\n"
    "v_mul_f32 v210, 0xc0135761, v211\n"
    "v_mul_f32 v211, v211, v211\n"
    "v_mfma_f32_16x16x32_bf16 v[52:55], v[120:123], v[92:95], v[52:55]\n"
    "ds_read_b64_tr_b16 v[152:153], v238\n"
    "ds_read_b64_tr_b16 v[154:155], v238 offset:2048\n"
    "v_mul_f32 v211, v211, v210\n"
    "v_mfma_f32_16x16x32_bf16 v[52:55], v[112:115], v[88:91], v[52:55]\n"
    "ds_read_b64_tr_b16 v[156:157], v239\n"
    "ds_read_b64_tr_b16 v[158:159], v239 offset:2048\n"
    "v_mul_f32 v211, 0x3d372713, v211\n"
    "s_waitcnt lgkmcnt(12)\n"
    "v_mfma_f32_16x16x32_bf16 v[0:3], v[64:67], v[96:99], v[0:3]\n"
    "ds_read_b128 v[88:91], v240 offset:12288\n"
    "v_and_b32 v229, 0xffff0000, v228\n"
    "v_mfma_f32_16x16x32_bf16 v[0:3], v[72:75], v[100:103], v[0:3]\n"
    "ds_read_b128 v[92:95], v241 offset:12288\n"
    "v_lshlrev_b32 v228, 16, v228\n"
    "v_mfma_f32_16x16x32_bf16 v[8:11], v[76:79], v[100:103], v[8:11]\n"
    "ds_read_b64_tr_b16 v[112:113], v243\n"
    "v_rcp_f32 v213, v229\n"
    "v_mfma_f32_16x16x32_bf16 v[8:11], v[68:71], v[96:99], v[8:11]\n"
    "ds_read_b64_tr_b16 v[114:115], v243 offset:1024\n"
    "buffer_load_dwordx4 v249, s[16:19], s62 offen offset:1024 lds\n"
    "v_mul_f32 v229, v229, v229\n"
    "s_waitcnt lgkmcnt(14)\n"
    "v_mfma_f32_16x16x32_bf16 v[24:27], v[68:71], v[104:107], v[24:27]\n"
    "ds_read_b64_tr_b16 v[120:121], v243 offset:8192\n"
    "v_mul_f32 v212, 0xc0135761, v213\n"
    "s_and_b32 s3, s32, 3\n"
    "v_mfma_f32_16x16x32_bf16 v[24:27], v[76:79], v[108:111], v[24:27]\n"
    "ds_read_b64_tr_b16 v[122:123], v243 offset:9216\n"
    "v_mul_f32 v213, v213, v213\n"
    "s_lshl_b32 s3, s3, 12\n"
    "v_mfma_f32_16x16x32_bf16 v[16:19], v[72:75], v[108:111], v[16:19]\n"
    "ds_read_b64_tr_b16 v[124:125], v243 offset:24576\n"
    "v_mul_f32 v213, v213, v212\n"
    "v_bfe_u32 v251, v250, 4, 4\n"
    "v_mfma_f32_16x16x32_bf16 v[16:19], v[64:67], v[104:107], v[16:19]\n"
    "ds_read_b64_tr_b16 v[126:127], v243 offset:25600\n"
    "s_lshl_b32 s62, s50, 3\n"
    "s_add_u32 m0, s43, 98304\n"
    "v_mul_f32 v213, 0x3d372713, v213\n"
    "v_lshrrev_b32 v254, 9, v250\n"
    "s_waitcnt lgkmcnt(14)\n"
    "v_mfma_f32_16x16x32_bf16 v[32:35], v[64:67], v[80:83], v[32:35]\n"
    "ds_read_b64_tr_b16 v[116:117], v243 offset:16384\n"
    "buffer_load_dwordx4 v248, s[16:19], s62 offen lds\n"
    "v_and_b32 v231, 0xffff0000, v230\n"
    "v_lshl_or_b32 v251, v254, 4, v251\n"
    "v_mfma_f32_16x16x32_bf16 v[32:35], v[72:75], v[84:87], v[32:35]\n"
    "ds_read_b64_tr_b16 v[118:119], v243 offset:17408\n"
    "s_waitcnt vmcnt(11)\n"
    "v_xor_b32 v220, s36, v217\n"
    "v_bitop3_b32 v220, v219, s36, v220 bitop3:0xbe\n"
    "v_lshlrev_b32 v230, 16, v230\n"
    "v_bfe_u32 v254, v250, 8, 1\n"
    "v_mfma_f32_16x16x32_bf16 v[40:43], v[76:79], v[84:87], v[40:43]\n"
    "v_cmp_ne_u32 vcc, 0, v220\n"
    "v_rcp_f32 v215, v231\n"
    "v_lshl_or_b32 v251, v254, 7, v251\n"
    "v_mfma_f32_16x16x32_bf16 v[40:43], v[68:71], v[80:83], v[40:43]\n"
    "s_cbranch_vccnz Lsp0_cxA\n"
    "Lret_cxA:\n"
    "v_mul_f32 v231, v231, v231\n"
    "v_lshrrev_b32 v255, 4, v250\n"
    "s_waitcnt lgkmcnt(8)\n"
    "v_mfma_f32_16x16x32_bf16 v[56:59], v[68:71], v[88:91], v[56:59]\n"
    "v_permlane32_swap_b32 v216, v218\n"
    "v_mov_b32 v217, 0\n"
    "v_mul_f32 v214, 0xc0135761, v215\n"
    "s_add_u32 s3, s3, 3072\n"
    "v_mfma_f32_16x16x32_bf16 v[56:59], v[76:79], v[92:95], v[56:59]\n"
    "v_add_f32 v216, v216, v218\n"
    "v_mov_b32 v218, v216\n"
    "buffer_load_dwordx4 v249, s[16:19], s62 offen offset:1024 lds\n"
    "v_mul_f32 v215, v215, v215\n"
    "v_lshl_add_u32 v251, v251, 2, s3\n"
    "v_mfma_f32_16x16x32_bf16 v[48:51], v[72:75], v[92:95], v[48:51]\n"
    "v_permlane16_swap_b32 v216, v218\n"
    "v_mul_f32 v215, v215, v214\n"
    "v_and_b32 v255, 48, v255\n"
    "v_mfma_f32_16x16x32_bf16 v[48:51], v[64:67], v[88:91], v[48:51]\n"
    "v_add_f32_dpp v217, v216, v218 quad_perm:[0,1,2,3] row_mask:0x5 bank_mask:0xf\n"
    "v_mul_f32 v215, 0x3d372713, v215\n"
    "v_add_u32 v255, s3, v255\n"
    "v_xor_b32 v254, 0x1000, v255\n"
    "s_waitcnt vmcnt(6) lgkmcnt(0)\n"
    "s_barrier\n"
    "s_setprio 2\n"
    "ds_write_b32 v251, v217\n"
    "v_mfma_f32_16x16x32_bf16 v[4:7], v[112:115], v[96:99], v[4:7]\n"
    "v_mfma_f32_16x16x32_bf16 v[4:7], v[120:123], v[100:103], v[4:7]\n"
    "v_mfma_f32_16x16x32_bf16 v[12:15], v[124:127], v[100:103], v[12:15]\n"
    "v_mfma_f32_16x16x32_bf16 v[12:15], v[116:119], v[96:99], v[12:15]\n"
    "v_mfma_f32_16x16x32_bf16 v[28:31], v[116:119], v[104:107], v[28:31]\n"
    "v_mfma_f32_16x16x32_bf16 v[28:31], v[124:127], v[108:111], v[28:31]\n"
    "v_mfma_f32_16x16x32_bf16 v[20:23], v[120:123], v[108:111], v[20:23]\n"
    "v_mfma_f32_16x16x32_bf16 v[20:23], v[112:115], v[104:107], v[20:23]\n"
    "v_mfma_f32_16x16x32_bf16 v[36:39], v[112:115], v[80:83], v[36:39]\n"
    "v_mfma_f32_16x16x32_bf16 v[36:39], v[120:123], v[84:87], v[36:39]\n"
    "s_waitcnt lgkmcnt(0)\n"
    "s_barrier\n"
    "ds_read_b128 v[96:99], v255\n"
    "ds_read_b128 v[100:103], v255 offset:64\n"
    "ds_read_b128 v[216:219], v254 offset:64\n"
    "ds_read_b128 v[104:107], v254\n"
    "v_and_b32 v251, 0x1f0, v250\n"
    "v_lshrrev_b32 v251, 1, v251\n"
    "s_lshr_b32 s54, s58, 5\n"
    "s_lshl_b32 s54, s54, 8\n"
    "s_add_u32 s54, s54, 32768\n"
    "v_mfma_f32_16x16x32_bf16 v[44:47], v[124:127], v[84:87], v[44:47]\n"
    "v_mfma_f32_16x16x32_bf16 v[44:47], v[116:119], v[80:83], v[44:47]\n"
    "v_mfma_f32_16x16x32_bf16 v[60:63], v[116:119], v[88:91], v[60:63]\n"
    "v_mfma_f32_16x16x32_bf16 v[60:63], v[124:127], v[92:95], v[60:63]\n"
    "buffer_load_dwordx2 v[252:253], v251, s[20:23], s54 offen sc1\n"
    "v_mfma_f32_16x16x32_bf16 v[52:55], v[120:123], v[92:95], v[52:55]\n"
    "v_mfma_f32_16x16x32_bf16 v[52:55], v[112:115], v[88:91], v[52:55]\n"
    "s_waitcnt lgkmcnt(3)\n"
    "v_fma_f32 v0, -v224, v96, v0\n"
    "v_fma_f32 v1, -v224, v97, v1\n"
    "v_fma_f32 v2, -v224, v98, v2\n"
    "v_fma_f32 v3, -v224, v99, v3\n"
    "s_waitcnt lgkmcnt(0)\n"
    "v_fma_f32 v4, -v224, v104, v4\n"
    "v_fma_f32 v5, -v224, v105, v5\n"
    "v_fma_f32 v6, -v224, v106, v6\n"
    "v_fma_f32 v7, -v224, v107, v7\n"
    "v_mul_f32 v112, v0, v0\n"
    "v_mul_f32 v113, v1, v1\n"
    "v_mul_f32 v114, v2, v2\n"
    "v_mul_f32 v115, v3, v3\n"
    "v_mul_f32 v116, v4, v4\n"
    "v_mul_f32 v117, v5, v5\n"
    "v_mul_f32 v118, v6, v6\n"
    "v_mul_f32 v119, v7, v7\n"
    "v_fma_f32 v112, v209, v112, v208\n"
    "v_fma_f32 v113, v209, v113, v208\n"
    "v_fma_f32 v114, v209, v114, v208\n"
    "v_fma_f32 v115, v209, v115, v208\n"
    "v_fma_f32 v116, v209, v116, v208\n"
    "v_fma_f32 v117, v209, v117, v208\n"
    "v_fma_f32 v118, v209, v118, v208\n"
    "v_fma_f32 v119, v209, v119, v208\n"
    "v_mul_f32 v112, v112, v0\n"
    "v_mul_f32 v113, v113, v1\n"
    "v_mul_f32 v114, v114, v2\n"
    "v_mul_f32 v115, v115, v3\n"
    "v_mul_f32 v116, v116, v4\n"
    "v_mul_f32 v117, v117, v5\n"
    "v_mul_f32 v118, v118, v6\n"
    "v_mul_f32 v119, v119, v7\n"
    "v_exp_f32 v112, v112\n"
    "v_fma_f32 v8, -v224, v100, v8\n"
    "v_exp_f32 v113, v113\n"
    "v_fma_f32 v9, -v224, v101, v9\n"
    "v_exp_f32 v114, v114\n"
    "v_fma_f32 v10, -v224, v102, v10\n"
    "v_exp_f32 v115, v115\n"
    "v_fma_f32 v11, -v224, v103, v11\n"
    "v_exp_f32 v116, v116\n"
    "v_fma_f32 v12, -v224, v216, v12\n"
    "v_exp_f32 v117, v117\n"
    "v_fma_f32 v13, -v224, v217, v13\n"
    "v_exp_f32 v118, v118\n"
    "v_fma_f32 v14, -v224, v218, v14\n"
    "v_exp_f32 v119, v119\n"
    "v_fma_f32 v15, -v224, v219, v15\n"
    "v_pk_fma_f32 v[112:113], v[224:225], v[112:113], v[224:225] op_sel:[1,0,1]\n"
    "v_pk_fma_f32 v[114:115], v[224:225], v[114:115], v[224:225] op_sel:[1,0,1]\n"
    "v_pk_fma_f32 v[116:117], v[224:225], v[116:117], v[224:225] op_sel:[1,0,1]\n"
    "v_pk_fma_f32 v[118:119], v[224:225], v[118:119], v[224:225] op_sel:[1,0,1]\n"
    "v_rcp_f32 v112, v112\n"
    "v_pk_mul_f32 v[8:9], v[8:9], v[0:1]\n"
    "v_rcp_f32 v113, v113\n"
    "v_rcp_f32 v114, v114\n"
    "v_pk_mul_f32 v[10:11], v[10:11], v[2:3]\n"
    "v_rcp_f32 v115, v115\n"
    "v_rcp_f32 v116, v116\n"
    "v_pk_mul_f32 v[12:13], v[12:13], v[4:5]\n"
    "v_rcp_f32 v117, v117\n"
    "v_rcp_f32 v118, v118\n"
    "v_pk_mul_f32 v[14:15], v[14:15], v[6:7]\n"
    "v_rcp_f32 v119, v119\n"
    "v_pk_mul_f32 v[8:9], v[8:9], v[112:113]\n"
    "v_pk_mul_f32 v[10:11], v[10:11], v[114:115]\n"
    "v_pk_mul_f32 v[12:13], v[12:13], v[116:117]\n"
    "v_pk_mul_f32 v[14:15], v[14:15], v[118:119]\n"
    "v_cvt_pk_bf16_f32 v0, v8, v9\n"
    "v_cvt_pk_bf16_f32 v1, v10, v11\n"
    "v_cvt_pk_bf16_f32 v2, v12, v13\n"
    "v_cvt_pk_bf16_f32 v3, v14, v15\n"
    "buffer_store_dwordx4 v[0:3], v250, s[20:23], s51 offen sc1\n"
    "v_pk_fma_f32 v[16:17], v[226:227], v[96:97], v[16:17] op_sel_hi:[0,1,1] neg_lo:[1,0,0] neg_hi:[1,0,0]\n"
    "v_pk_fma_f32 v[18:19], v[226:227], v[98:99], v[18:19] op_sel_hi:[0,1,1] neg_lo:[1,0,0] neg_hi:[1,0,0]\n"
    "v_pk_fma_f32 v[20:21], v[226:227], v[104:105], v[20:21] op_sel_hi:[0,1,1] neg_lo:[1,0,0] neg_hi:[1,0,0]\n"
    "v_pk_fma_f32 v[22:23], v[226:227], v[106:107], v[22:23] op_sel_hi:[0,1,1] neg_lo:[1,0,0] neg_hi:[1,0,0]\n"
    "v_pk_mul_f32 v[112:113], v[16:17], v[16:17]\n"
    "v_pk_mul_f32 v[114:115], v[18:19], v[18:19]\n"
    "v_pk_mul_f32 v[116:117], v[20:21], v[20:21]\n"
    "v_pk_mul_f32 v[118:119], v[22:23], v[22:23]\n"
    "v_pk_fma_f32 v[112:113], v[210:211], v[112:113], v[210:211] op_sel:[1,0,0] op_sel_hi:[1,1,0]\n"
    "v_pk_fma_f32 v[114:115], v[210:211], v[114:115], v[210:211] op_sel:[1,0,0] op_sel_hi:[1,1,0]\n"
    "v_pk_fma_f32 v[116:117], v[210:211], v[116:117], v[210:211] op_sel:[1,0,0] op_sel_hi:[1,1,0]\n"
    "v_pk_fma_f32 v[118:119], v[210:211], v[118:119], v[210:211] op_sel:[1,0,0] op_sel_hi:[1,1,0]\n"
    "v_pk_mul_f32 v[112:113], v[112:113], v[16:17]\n"
    "v_pk_mul_f32 v[114:115], v[114:115], v[18:19]\n"
    "v_pk_mul_f32 v[116:117], v[116:117], v[20:21]\n"
    "v_pk_mul_f32 v[118:119], v[118:119], v[22:23]\n"
    "v_exp_f32 v112, v112\n"
    "v_pk_fma_f32 v[24:25], v[226:227], v[100:101], v[24:25] op_sel_hi:[0,1,1] neg_lo:[1,0,0] neg_hi:[1,0,0]\n"
    "v_exp_f32 v113, v113\n"
    "v_exp_f32 v114, v114\n"
    "v_pk_fma_f32 v[26:27], v[226:227], v[102:103], v[26:27] op_sel_hi:[0,1,1] neg_lo:[1,0,0] neg_hi:[1,0,0]\n"
    "v_exp_f32 v115, v115\n"
    "v_exp_f32 v116, v116\n"
    "v_pk_fma_f32 v[28:29], v[226:227], v[216:217], v[28:29] op_sel_hi:[0,1,1] neg_lo:[1,0,0] neg_hi:[1,0,0]\n"
    "v_exp_f32 v117, v117\n"
    "v_exp_f32 v118, v118\n"
    "v_pk_fma_f32 v[30:31], v[226:227], v[218:219], v[30:31] op_sel_hi:[0,1,1] neg_lo:[1,0,0] neg_hi:[1,0,0]\n"
    "v_exp_f32 v119, v119\n"
    "v_pk_fma_f32 v[112:113], v[226:227], v[112:113], v[226:227] op_sel:[1,0,1]\n"
    "v_pk_fma_f32 v[114:115], v[226:227], v[114:115], v[226:227] op_sel:[1,0,1]\n"
    "v_pk_fma_f32 v[116:117], v[226:227], v[116:117], v[226:227] op_sel:[1,0,1]\n"
    "v_pk_fma_f32 v[118:119], v[226:227], v[118:119], v[226:227] op_sel:[1,0,1]\n"
    "v_rcp_f32 v112, v112\n"
    "v_pk_mul_f32 v[24:25], v[24:25], v[16:17]\n"
    "v_rcp_f32 v113, v113\n"
    "v_rcp_f32 v114, v114\n"
    "v_pk_mul_f32 v[26:27], v[26:27], v[18:19]\n"
    "v_rcp_f32 v115, v115\n"
    "v_rcp_f32 v116, v116\n"
    "v_pk_mul_f32 v[28:29], v[28:29], v[20:21]\n"
    "v_rcp_f32 v117, v117\n"
    "v_rcp_f32 v118, v118\n"
    "v_pk_mul_f32 v[30:31], v[30:31], v[22:23]\n"
    "v_rcp_f32 v119, v119\n"
    "v_pk_mul_f32 v[24:25], v[24:25], v[112:113]\n"
    "v_pk_mul_f32 v[26:27], v[26:27], v[114:115]\n"
    "v_pk_mul_f32 v[28:29], v[28:29], v[116:117]\n"
    "v_pk_mul_f32 v[30:31], v[30:31], v[118:119]\n"
    "v_cvt_pk_bf16_f32 v16, v24, v25\n"
    "v_cvt_pk_bf16_f32 v17, v26, v27\n"
    "v_cvt_pk_bf16_f32 v18, v28, v29\n"
    "v_cvt_pk_bf16_f32 v19, v30, v31\n"
    "buffer_store_dwordx4 v[16:19], v250, s[20:23], s51 offen offset:1024 sc1\n"
    "s_waitcnt vmcnt(2)\n"
    "v_cmp_ne_u64 vcc, s[36:37], v[252:253]\n"
    "s_nop 0\n"
    "s_cbranch_vccnz Lsp0_az\n"
    "Lret_az:\n"
    "v_lshrrev_b32 v254, 4, v250\n"
    "v_and_b32 v254, 48, v254\n"
    "v_xor_b32 v254, v254, v250\n"
    "v_xor_b32 v255, 64, v254\n"
    "s_lshl_b32 s3, s32, 11\n"
    "s_add_u32 m0, s3, 147456\n"
    "s_add_u32 s62, s53, 2621440\n"
    "buffer_load_dwordx4 v254, s[20:23], s53 offen sc1 lds\n"
    "buffer_load_dwordx4 v255, s[20:23], s53 offen offset:1024 sc1 lds\n"
    "buffer_load_dwordx4 v[8:11], v250, s[20:23], s62 offen sc1\n"
    "buffer_load_dwordx4 v[12:15], v250, s[20:23], s62 offen offset:1024 sc1\n"
    "v_pk_fma_f32 v[32:33], v[228:229], v[96:97], v[32:33] op_sel_hi:[0,1,1] neg_lo:[1,0,0] neg_hi:[1,0,0]\n"
    "v_pk_fma_f32 v[34:35], v[228:229], v[98:99], v[34:35] op_sel_hi:[0,1,1] neg_lo:[1,0,0] neg_hi:[1,0,0]\n"
    "v_pk_fma_f32 v[36:37], v[228:229], v[104:105], v[36:37] op_sel_hi:[0,1,1] neg_lo:[1,0,0] neg_hi:[1,0,0]\n"
    "v_pk_fma_f32 v[38:39], v[228:229], v[106:107], v[38:39] op_sel_hi:[0,1,1] neg_lo:[1,0,0] neg_hi:[1,0,0]\n"
    "v_pk_mul_f32 v[112:113], v[32:33], v[32:33]\n"
    "v_pk_mul_f32 v[114:115], v[34:35], v[34:35]\n"
    "v_pk_mul_f32 v[116:117], v[36:37], v[36:37]\n"
    "v_pk_mul_f32 v[118:119], v[38:39], v[38:39]\n"
    "v_pk_fma_f32 v[112:113], v[212:213], v[112:113], v[212:213] op_sel:[1,0,0] op_sel_hi:[1,1,0]\n"
    "v_pk_fma_f32 v[114:115], v[212:213], v[114:115], v[212:213] op_sel:[1,0,0] op_sel_hi:[1,1,0]\n"
    "v_pk_fma_f32 v[116:117], v[212:213], v[116:117], v[212:213] op_sel:[1,0,0] op_sel_hi:[1,1,0]\n"
    "v_pk_fma_f32 v[118:119], v[212:213], v[118:119], v[212:213] op_sel:[1,0,0] op_sel_hi:[1,1,0]\n"
    "v_pk_mul_f32 v[112:113], v[112:113], v[32:33]\n"
    "v_pk_mul_f32 v[114:115], v[114:115], v[34:35]\n"
    "v_pk_mul_f32 v[116:117], v[116:117], v[36:37]\n"
    "v_pk_mul_f32 v[118:119], v[118:119], v[38:39]\n"
    "v_exp_f32 v112, v112\n"
    "v_pk_fma_f32 v[40:41], v[228:229], v[100:101], v[40:41] op_sel_hi:[0,1,1] neg_lo:[1,0,0] neg_hi:[1,0,0]\n"
    "v_exp_f32 v113, v113\n"
    "v_exp_f32 v114, v114\n"
    "v_pk_fma_f32 v[42:43], v[228:229], v[102:103], v[42:43] op_sel_hi:[0,1,1] neg_lo:[1,0,0] neg_hi:[1,0,0]\n"
    "v_exp_f32 v115, v115\n"
    "v_exp_f32 v116, v116\n"
    "v_pk_fma_f32 v[44:45], v[228:229], v[216:217], v[44:45] op_sel_hi:[0,1,1] neg_lo:[1,0,0] neg_hi:[1,0,0]\n"
    "v_exp_f32 v117, v117\n"
    "v_exp_f32 v118, v118\n"
    "v_pk_fma_f32 v[46:47], v[228:229], v[218:219], v[46:47] op_sel_hi:[0,1,1] neg_lo:[1,0,0] neg_hi:[1,0,0]\n"
    "v_exp_f32 v119, v119\n"
    "v_pk_fma_f32 v[112:113], v[228:229], v[112:113], v[228:229] op_sel:[1,0,1]\n"
    "v_pk_fma_f32 v[114:115], v[228:229], v[114:115], v[228:229] op_sel:[1,0,1]\n"
    "v_pk_fma_f32 v[116:117], v[228:229], v[116:117], v[228:229] op_sel:[1,0,1]\n"
    "v_pk_fma_f32 v[118:119], v[228:229], v[118:119], v[228:229] op_sel:[1,0,1]\n"
    "v_rcp_f32 v112, v112\n"
    "v_pk_mul_f32 v[40:41], v[40:41], v[32:33]\n"
    "v_rcp_f32 v113, v113\n"
    "v_rcp_f32 v114, v114\n"
    "v_pk_mul_f32 v[42:43], v[42:43], v[34:35]\n"
    "v_rcp_f32 v115, v115\n"
    "v_rcp_f32 v116, v116\n"
    "v_pk_mul_f32 v[44:45], v[44:45], v[36:37]\n"
    "v_rcp_f32 v117, v117\n"
    "v_rcp_f32 v118, v118\n"
    "v_pk_mul_f32 v[46:47], v[46:47], v[38:39]\n"
    "v_rcp_f32 v119, v119\n"
    "v_pk_mul_f32 v[40:41], v[40:41], v[112:113]\n"
    "v_pk_mul_f32 v[42:43], v[42:43], v[114:115]\n"
    "v_pk_mul_f32 v[44:45], v[44:45], v[116:117]\n"
    "v_pk_mul_f32 v[46:47], v[46:47], v[118:119]\n"
    "v_cvt_pk_bf16_f32 v32, v40, v41\n"
    "v_cvt_pk_bf16_f32 v33, v42, v43\n"
    "v_cvt_pk_bf16_f32 v34, v44, v45\n"
    "v_cvt_pk_bf16_f32 v35, v46, v47\n"
    "buffer_store_dwordx4 v[32:35], v250, s[20:23], s51 offen offset:2048 sc1\n"
    "v_pk_fma_f32 v[48:49], v[230:231], v[96:97], v[48:49] op_sel_hi:[0,1,1] neg_lo:[1,0,0] neg_hi:[1,0,0]\n"
    "v_pk_fma_f32 v[50:51], v[230:231], v[98:99], v[50:51] op_sel_hi:[0,1,1] neg_lo:[1,0,0] neg_hi:[1,0,0]\n"
    "v_pk_fma_f32 v[52:53], v[230:231], v[104:105], v[52:53] op_sel_hi:[0,1,1] neg_lo:[1,0,0] neg_hi:[1,0,0]\n"
    "v_pk_fma_f32 v[54:55], v[230:231], v[106:107], v[54:55] op_sel_hi:[0,1,1] neg_lo:[1,0,0] neg_hi:[1,0,0]\n"
    "v_pk_mul_f32 v[112:113], v[48:49], v[48:49]\n"
    "v_pk_mul_f32 v[114:115], v[50:51], v[50:51]\n"
    "v_pk_mul_f32 v[116:117], v[52:53], v[52:53]\n"
    "v_pk_mul_f32 v[118:119], v[54:55], v[54:55]\n"
    "v_pk_fma_f32 v[112:113], v[214:215], v[112:113], v[214:215] op_sel:[1,0,0] op_sel_hi:[1,1,0]\n"
    "v_pk_fma_f32 v[114:115], v[214:215], v[114:115], v[214:215] op_sel:[1,0,0] op_sel_hi:[1,1,0]\n"
    "v_pk_fma_f32 v[116:117], v[214:215], v[116:117], v[214:215] op_sel:[1,0,0] op_sel_hi:[1,1,0]\n"
    "v_pk_fma_f32 v[118:119], v[214:215], v[118:119], v[214:215] op_sel:[1,0,0] op_sel_hi:[1,1,0]\n"
    "v_pk_mul_f32 v[112:113], v[112:113], v[48:49]\n"
    "v_pk_mul_f32 v[114:115], v[114:115], v[50:51]\n"
    "v_pk_mul_f32 v[116:117], v[116:117], v[52:53]\n"
    "v_pk_mul_f32 v[118:119], v[118:119], v[54:55]\n"
    "v_exp_f32 v112, v112\n"
    "v_pk_fma_f32 v[56:57], v[230:231], v[100:101], v[56:57] op_sel_hi:[0,1,1] neg_lo:[1,0,0] neg_hi:[1,0,0]\n"
    "v_exp_f32 v113, v113\n"
    "v_exp_f32 v114, v114\n"
    "v_pk_fma_f32 v[58:59], v[230:231], v[102:103], v[58:59] op_sel_hi:[0,1,1] neg_lo:[1,0,0] neg_hi:[1,0,0]\n"
    "v_exp_f32 v115, v115\n"
    "v_exp_f32 v116, v116\n"
    "v_pk_fma_f32 v[60:61], v[230:231], v[216:217], v[60:61] op_sel_hi:[0,1,1] neg_lo:[1,0,0] neg_hi:[1,0,0]\n"
    "v_exp_f32 v117, v117\n"
    "v_exp_f32 v118, v118\n"
    "v_pk_fma_f32 v[62:63], v[230:231], v[218:219], v[62:63] op_sel_hi:[0,1,1] neg_lo:[1,0,0] neg_hi:[1,0,0]\n"
    "v_exp_f32 v119, v119\n"
    "v_pk_fma_f32 v[112:113], v[230:231], v[112:113], v[230:231] op_sel:[1,0,1]\n"
    "v_pk_fma_f32 v[114:115], v[230:231], v[114:115], v[230:231] op_sel:[1,0,1]\n"
    "v_pk_fma_f32 v[116:117], v[230:231], v[116:117], v[230:231] op_sel:[1,0,1]\n"
    "v_pk_fma_f32 v[118:119], v[230:231], v[118:119], v[230:231] op_sel:[1,0,1]\n"
    "v_rcp_f32 v112, v112\n"
    "v_pk_mul_f32 v[56:57], v[56:57], v[48:49]\n"
    "v_rcp_f32 v113, v113\n"
    "v_rcp_f32 v114, v114\n"
    "v_pk_mul_f32 v[58:59], v[58:59], v[50:51]\n"
    "v_rcp_f32 v115, v115\n"
    "v_rcp_f32 v116, v116\n"
    "v_pk_mul_f32 v[60:61], v[60:61], v[52:53]\n"
    "v_rcp_f32 v117, v117\n"
    "v_rcp_f32 v118, v118\n"
    "v_pk_mul_f32 v[62:63], v[62:63], v[54:55]\n"
    "v_rcp_f32 v119, v119\n"
    "v_pk_mul_f32 v[56:57], v[56:57], v[112:113]\n"
    "v_pk_mul_f32 v[58:59], v[58:59], v[114:115]\n"
    "v_pk_mul_f32 v[60:61], v[60:61], v[116:117]\n"
    "v_pk_mul_f32 v[62:63], v[62:63], v[118:119]\n"
    "v_cvt_pk_bf16_f32 v48, v56, v57\n"
    "v_cvt_pk_bf16_f32 v49, v58, v59\n"
    "v_cvt_pk_bf16_f32 v50, v60, v61\n"
    "v_cvt_pk_bf16_f32 v51, v62, v63\n"
    "buffer_store_dwordx4 v[48:51], v250, s[20:23], s51 offen offset:3072 sc1\n"
    "ds_read_b64_tr_b16 v[64:65], v236 offset:16384\n"
    "ds_read_b64_tr_b16 v[66:67], v236 offset:18432\n"
    "ds_read_b64_tr_b16 v[68:69], v237 offset:16384\n"
    "ds_read_b64_tr_b16 v[70:71], v237 offset:18432\n"
    "ds_read_b64_tr_b16 v[72:73], v238 offset:16384\n"
    "ds_read_b64_tr_b16 v[74:75], v238 offset:18432\n"
    "ds_read_b64_tr_b16 v[76:77], v239 offset:16384\n"
    "ds_read_b64_tr_b16 v[78:79], v239 offset:18432\n"
    "ds_read_b64_tr_b16 v[80:81], v236 offset:32768\n"
    "ds_read_b64_tr_b16 v[82:83], v236 offset:34816\n"
    "ds_read_b64_tr_b16 v[84:85], v237 offset:32768\n"
    "ds_read_b64_tr_b16 v[86:87], v237 offset:34816\n"
    "ds_read_b64_tr_b16 v[88:89], v238 offset:32768\n"
    "ds_read_b64_tr_b16 v[90:91], v238 offset:34816\n"
    "ds_read_b64_tr_b16 v[92:93], v239 offset:32768\n"
    "ds_read_b64_tr_b16 v[94:95], v239 offset:34816\n"
    "ds_read_b64_tr_b16 v[220:221], v236 offset:49152\n"
    "ds_read_b64_tr_b16 v[222:223], v236 offset:51200\n"
    "ds_read_b64_tr_b16 v[240:241], v237 offset:49152\n"
    "ds_read_b64_tr_b16 v[242:243], v237 offset:51200\n"
    "ds_read_b64_tr_b16 v[244:245], v238 offset:49152\n"
    "ds_read_b64_tr_b16 v[246:247], v238 offset:51200\n"
    "ds_read_b64_tr_b16 v[232:233], v239 offset:49152\n"
    "ds_read_b64_tr_b16 v[234:235], v239 offset:51200\n"
    "v_mov_b32 v254, s43\n"
    "v_mov_b32 v255, 1\n"
    "s_mov_b64 exec, 1\n"
    "ds_write_b32 v254, v255 offset:3596\n"
    "s_mov_b64 exec, -1\n"
    "Lafft:\n"
    "v_bfe_u32 v0, v250, 4, 1\n"
    "v_mul_u32_u24 v0, 0x8000, v0\n"
    "v_lshl_or_b32 v0, v0, 16, v0\n"
    "v_xor_b32 v1, 0x80008000, v0\n"
    "s_mov_b32 s63, 0x80008000\n"
    "v_xor_b32 v96, v0, v200\n"
    "v_xor_b32 v100, v0, v204\n"
    "v_xor_b32 v104, s63, v204\n"
    "v_xor_b32 v108, v1, v204\n"
    "v_xor_b32 v97, v0, v201\n"
    "v_xor_b32 v101, v0, v205\n"
    "v_xor_b32 v105, s63, v205\n"
    "v_xor_b32 v109, v1, v205\n"
    "v_xor_b32 v98, v0, v202\n"
    "v_xor_b32 v102, v0, v206\n"
    "v_xor_b32 v106, s63, v206\n"
    "v_xor_b32 v110, v1, v206\n"
    "v_xor_b32 v99, v0, v203\n"
    "v_xor_b32 v103, v0, v207\n"
    "v_xor_b32 v107, s63, v207\n"
    "v_xor_b32 v111, v1, v207\n"
    "v_bfe_u32 v2, v250, 5, 3\n"
    "v_lshrrev_b32 v3, 8, v250\n"
    "v_xor_b32 v2, v2, v3\n"
    "v_and_b32 v3, 0xf0, v250\n"
    "v_lshlrev_b32 v3, 3, v3\n"
    "v_lshl_or_b32 v2, v2, 4, v3\n"
    "v_add_u32 v224, 147456, v2\n"
    "v_xor_b32 v225, 64, v224\n"
    "s_and_b32 s3, s32, 2\n"
    "s_lshl_b32 s60, s3, 10\n"
    "s_add_u32 s60, s60, 155648\n"
    "v_add_u32 v227, s60, v250\n"
    "s_lshl_b32 s3, s3, 9\n"
    "s_and_b32 s60, s32, 1\n"
    "s_lshl3_add_u32 s3, s60, s3\n"
    "s_add_u32 s3, s3, 155648\n"
    "v_add_u32 v226, s3, v250\n"
    "v_mov_b32 v228, 3584\n"
    "v_mov_b32 v229, 1\n"
    "s_brev_b32 s61, 1\n"
    "v_mov_b32 v208, s36\n"
    "v_mov_b32 v209, s37\n"
    "s_waitcnt vmcnt(2)\n"
    "s_mov_b64 exec, 1\n"
    "ds_add_u32 v228, v229 offset:0\n"
    "s_mov_b64 exec, -1\n"
    "s_waitcnt vmcnt(0)\n"
    "s_mov_b64 exec, 1\n"
    "buffer_store_dwordx2 v[208:209], off, s[20:23], s52 sc1\n"
    "s_mov_b64 exec, -1\n"
    "s_setprio 0\n"
    "LsS:\n"
    "ds_read_b32 v230, v228 offset:0\n"
    "ds_read_b128 v[32:35], v224\n"
    "ds_read_b128 v[40:43], v224 offset:2048\n"
    "ds_read_b128 v[36:39], v225\n"
    "ds_read_b128 v[44:47], v225 offset:2048\n"
    "ds_read_b128 v[48:51], v224 offset:4096\n"
    "ds_read_b128 v[56:59], v224 offset:6144\n"
    "ds_read_b128 v[52:55], v225 offset:4096\n"
    "ds_read_b128 v[60:63], v225 offset:6144\n"
    "s_waitcnt lgkmcnt(8)\n"
    "v_readfirstlane_b32 s3, v230\n"
    "s_add_u32 s59, s59, 0x1000\n"
    "s_cselect_b32 s3, 4, s3\n"
    "s_cmp_lt_u32 s3, 4\n"
    "s_cbranch_scc1 LsS\n"
    "s_waitcnt lgkmcnt(7)\n"
    "v_mfma_f32_16x16x32_bf16 v[16:19], v[32:35], v[200:203], 0\n"
    "v_xor_b32 v112, s61, v160\n"
    "v_xor_b32 v116, s61, v164\n"
    "v_mfma_f32_16x16x32_bf16 v[24:27], v[32:35], v[104:107], 0\n"
    "v_xor_b32 v120, s63, v164\n"
    "v_xor_b32 v124, 0x8000, v164\n"
    "s_waitcnt lgkmcnt(6)\n"
    "v_mfma_f32_16x16x32_bf16 v[20:23], v[40:43], v[200:203], 0\n"
    "v_xor_b32 v113, s61, v161\n"
    "v_xor_b32 v117, s61, v165\n"
    "v_mfma_f32_16x16x32_bf16 v[28:31], v[40:43], v[104:107], 0\n"
    "v_xor_b32 v121, s63, v165\n"
    "v_xor_b32 v125, 0x8000, v165\n"
    "s_waitcnt lgkmcnt(5)\n"
    "v_mfma_f32_16x16x32_bf16 v[16:19], v[36:39], v[96:99], v[16:19]\n"
    "v_xor_b32 v114, s61, v162\n"
    "v_xor_b32 v118, s61, v166\n"
    "v_mfma_f32_16x16x32_bf16 v[24:27], v[36:39], v[108:111], v[24:27]\n"
    "v_xor_b32 v122, s63, v166\n"
    "v_xor_b32 v126, 0x8000, v166\n"
    "s_waitcnt lgkmcnt(4)\n"
    "v_mfma_f32_16x16x32_bf16 v[20:23], v[44:47], v[96:99], v[20:23]\n"
    "v_xor_b32 v115, s61, v163\n"
    "v_xor_b32 v119, s61, v167\n"
    "v_mfma_f32_16x16x32_bf16 v[28:31], v[44:47], v[108:111], v[28:31]\n"
    "v_xor_b32 v123, s63, v167\n"
    "v_xor_b32 v127, 0x8000, v167\n"
    "s_waitcnt lgkmcnt(3)\n"
    "v_mfma_f32_16x16x32_bf16 v[16:19], v[48:51], v[204:207], v[16:19]\n"
    "v_lshlrev_b32 v32, 16, v8\n"
    "v_and_b32 v33, 0xffff0000, v8\n"
    "v_mfma_f32_16x16x32_bf16 v[24:27], v[48:51], v[200:203], v[24:27]\n"
    "s_waitcnt lgkmcnt(2)\n"
    "v_mfma_f32_16x16x32_bf16 v[20:23], v[56:59], v[204:207], v[20:23]\n"
    "v_lshlrev_b32 v34, 16, v9\n"
    "v_and_b32 v35, 0xffff0000, v9\n"
    "v_mfma_f32_16x16x32_bf16 v[28:31], v[56:59], v[200:203], v[28:31]\n"
    "s_waitcnt lgkmcnt(1)\n"
    "v_mfma_f32_16x16x32_bf16 v[16:19], v[52:55], v[100:103], v[16:19]\n"
    "v_lshlrev_b32 v40, 16, v10\n"
    "v_and_b32 v41, 0xffff0000, v10\n"
    "v_mfma_f32_16x16x32_bf16 v[24:27], v[52:55], v[96:99], v[24:27]\n"
    "s_waitcnt lgkmcnt(0)\n"
    "v_mfma_f32_16x16x32_bf16 v[20:23], v[60:63], v[100:103], v[20:23]\n"
    "v_lshlrev_b32 v42, 16, v11\n"
    "v_and_b32 v43, 0xffff0000, v11\n"
    "v_mfma_f32_16x16x32_bf16 v[28:31], v[60:63], v[96:99], v[28:31]\n"
    "v_lshlrev_b32 v36, 16, v12\n"
    "v_and_b32 v37, 0xffff0000, v12\n"
    "v_lshlrev_b32 v38, 16, v13\n"
    "v_and_b32 v39, 0xffff0000, v13\n"
    "v_lshlrev_b32 v44, 16, v14\n"
    "v_and_b32 v45, 0xffff0000, v14\n"
    "v_lshlrev_b32 v46, 16, v15\n"
    "v_and_b32 v47, 0xffff0000, v15\n"
    "v_pk_mul_f32 v[48:49], v[24:25], v[176:177]\n"
    "v_pk_mul_f32 v[50:51], v[26:27], v[178:179]\n"
    "v_pk_mul_f32 v[52:53], v[28:29], v[180:181]\n"
    "v_pk_mul_f32 v[54:55], v[30:31], v[182:183]\n"
    "v_pk_mul_f32 v[56:57], v[16:17], v[176:177]\n"
    "v_pk_mul_f32 v[58:59], v[18:19], v[178:179]\n"
    "v_pk_mul_f32 v[60:61], v[20:21], v[180:181]\n"
    "v_pk_mul_f32 v[62:63], v[22:23], v[182:183]\n"
    "v_pk_fma_f32 v[16:17], v[16:17], v[168:169], v[48:49]\n"
    "v_pk_fma_f32 v[18:19], v[18:19], v[170:171], v[50:51]\n"
    "v_pk_fma_f32 v[20:21], v[20:21], v[172:173], v[52:53]\n"
    "v_pk_fma_f32 v[22:23], v[22:23], v[174:175], v[54:55]\n"
    "v_pk_fma_f32 v[24:25], v[24:25], v[168:169], v[56:57] neg_lo:[0,0,1] neg_hi:[0,0,1]\n"
    "v_pk_fma_f32 v[26:27], v[26:27], v[170:171], v[58:59] neg_lo:[0,0,1] neg_hi:[0,0,1]\n"
    "v_pk_fma_f32 v[28:29], v[28:29], v[172:173], v[60:61] neg_lo:[0,0,1] neg_hi:[0,0,1]\n"
    "v_pk_fma_f32 v[30:31], v[30:31], v[174:175], v[62:63] neg_lo:[0,0,1] neg_hi:[0,0,1]\n"
    "v_cvt_pk_bf16_f32 v0, v16, v17\n"
    "v_cvt_pk_bf16_f32 v1, v18, v19\n"
    "v_cvt_pk_bf16_f32 v2, v20, v21\n"
    "v_cvt_pk_bf16_f32 v3, v22, v23\n"
    "v_cvt_pk_bf16_f32 v4, v24, v25\n"
    "v_cvt_pk_bf16_f32 v5, v26, v27\n"
    "v_cvt_pk_bf16_f32 v6, v28, v29\n"
    "v_cvt_pk_bf16_f32 v7, v30, v31\n"
    "v_mfma_f32_16x16x32_bf16 v[16:19], v[160:163], v[0:3], 0\n"
    "s_nop 0\n"
    "v_mfma_f32_16x16x32_bf16 v[24:27], v[160:163], v[4:7], 0\n"
    "v_mfma_f32_16x16x32_bf16 v[20:23], v[112:115], v[0:3], 0\n"
    "v_mfma_f32_16x16x32_bf16 v[28:31], v[112:115], v[4:7], 0\n"
    "v_mfma_f32_16x16x32_bf16 v[16:19], v[164:167], v[4:7], v[16:19]\n"
    "v_mfma_f32_16x16x32_bf16 v[24:27], v[120:123], v[0:3], v[24:27]\n"
    "v_mfma_f32_16x16x32_bf16 v[20:23], v[116:119], v[4:7], v[20:23]\n"
    "v_mfma_f32_16x16x32_bf16 v[28:31], v[124:127], v[0:3], v[28:31]\n"
    "s_nop 5\n"
    "v_pk_mul_f32 v[48:49], v[24:25], v[40:41]\n"
    "v_pk_mul_f32 v[50:51], v[26:27], v[42:43]\n"
    "v_pk_mul_f32 v[52:53], v[28:29], v[44:45]\n"
    "v_pk_mul_f32 v[54:55], v[30:31], v[46:47]\n"
    "v_pk_mul_f32 v[56:57], v[16:17], v[40:41]\n"
    "v_pk_mul_f32 v[58:59], v[18:19], v[42:43]\n"
    "v_pk_mul_f32 v[60:61], v[20:21], v[44:45]\n"
    "v_pk_mul_f32 v[62:63], v[22:23], v[46:47]\n"
    "v_pk_fma_f32 v[16:17], v[16:17], v[32:33], v[48:49] neg_lo:[0,0,1] neg_hi:[0,0,1]\n"
    "v_pk_fma_f32 v[18:19], v[18:19], v[34:35], v[50:51] neg_lo:[0,0,1] neg_hi:[0,0,1]\n"
    "v_pk_fma_f32 v[20:21], v[20:21], v[36:37], v[52:53] neg_lo:[0,0,1] neg_hi:[0,0,1]\n"
    "v_pk_fma_f32 v[22:23], v[22:23], v[38:39], v[54:55] neg_lo:[0,0,1] neg_hi:[0,0,1]\n"
    "v_pk_fma_f32 v[24:25], v[24:25], v[32:33], v[56:57]\n"
    "v_pk_fma_f32 v[26:27], v[26:27], v[34:35], v[58:59]\n"
    "v_pk_fma_f32 v[28:29], v[28:29], v[36:37], v[60:61]\n"
    "v_pk_fma_f32 v[30:31], v[30:31], v[38:39], v[62:63]\n"
    "v_cvt_pk_bf16_f32 v0, v16, v17\n"
    "v_cvt_pk_bf16_f32 v1, v18, v19\n"
    "v_cvt_pk_bf16_f32 v2, v20, v21\n"
    "v_cvt_pk_bf16_f32 v3, v22, v23\n"
    "v_cvt_pk_bf16_f32 v4, v24, v25\n"
    "v_cvt_pk_bf16_f32 v5, v26, v27\n"
    "v_cvt_pk_bf16_f32 v6, v28, v29\n"
    "v_cvt_pk_bf16_f32 v7, v30, v31\n"
    "v_mfma_f32_16x16x32_bf16 v[16:19], v[0:3], v[160:163], 0\n"
    "v_mfma_f32_16x16x32_bf16 v[24:27], v[0:3], v[164:167], 0\n"
    "v_mfma_f32_16x16x32_bf16 v[20:23], v[0:3], v[112:115], 0\n"
    "v_mfma_f32_16x16x32_bf16 v[28:31], v[0:3], v[116:119], 0\n"
    "v_mfma_f32_16x16x32_bf16 v[16:19], v[4:7], v[120:123], v[16:19]\n"
    "v_mfma_f32_16x16x32_bf16 v[24:27], v[4:7], v[160:163], v[24:27]\n"
    "v_mfma_f32_16x16x32_bf16 v[20:23], v[4:7], v[124:127], v[20:23]\n"
    "v_mfma_f32_16x16x32_bf16 v[28:31], v[4:7], v[112:115], v[28:31]\n"
    "s_nop 5\n"
    "v_pk_mul_f32 v[48:49], v[24:25], v[192:193]\n"
    "v_pk_mul_f32 v[50:51], v[26:27], v[194:195]\n"
    "v_pk_mul_f32 v[52:53], v[28:29], v[196:197]\n"
    "v_pk_mul_f32 v[54:55], v[30:31], v[198:199]\n"
    "v_pk_mul_f32 v[56:57], v[16:17], v[192:193]\n"
    "v_pk_mul_f32 v[58:59], v[18:19], v[194:195]\n"
    "v_pk_mul_f32 v[60:61], v[20:21], v[196:197]\n"
    "v_pk_mul_f32 v[62:63], v[22:23], v[198:199]\n"
    "v_pk_fma_f32 v[16:17], v[16:17], v[184:185], v[48:49] neg_lo:[0,0,1] neg_hi:[0,0,1]\n"
    "v_pk_fma_f32 v[18:19], v[18:19], v[186:187], v[50:51] neg_lo:[0,0,1] neg_hi:[0,0,1]\n"
    "v_pk_fma_f32 v[20:21], v[20:21], v[188:189], v[52:53] neg_lo:[0,0,1] neg_hi:[0,0,1]\n"
    "v_pk_fma_f32 v[22:23], v[22:23], v[190:191], v[54:55] neg_lo:[0,0,1] neg_hi:[0,0,1]\n"
    "v_pk_fma_f32 v[24:25], v[24:25], v[184:185], v[56:57]\n"
    "v_pk_fma_f32 v[26:27], v[26:27], v[186:187], v[58:59]\n"
    "v_pk_fma_f32 v[28:29], v[28:29], v[188:189], v[60:61]\n"
    "v_pk_fma_f32 v[30:31], v[30:31], v[190:191], v[62:63]\n"
    "v_cvt_pk_bf16_f32 v0, v16, v17\n"
    "v_cvt_pk_bf16_f32 v1, v18, v19\n"
    "v_cvt_pk_bf16_f32 v2, v20, v21\n"
    "v_cvt_pk_bf16_f32 v3, v22, v23\n"
    "v_cvt_pk_bf16_f32 v4, v24, v25\n"
    "v_cvt_pk_bf16_f32 v5, v26, v27\n"
    "v_cvt_pk_bf16_f32 v6, v28, v29\n"
    "v_cvt_pk_bf16_f32 v7, v30, v31\n"
    "ds_write_b64 v226, v[0:1] offset:0\n"
    "ds_write_b64 v226, v[2:3] offset:2048\n"
    "ds_write_b64 v226, v[4:5] offset:4096\n"
    "ds_write_b64 v226, v[6:7] offset:6144\n"
    "v_and_b32 v211, 0x70, v250\n"
    "v_min_u32 v211, 0x60, v211\n"
    "v_lshlrev_b32 v211, 1, v211\n"
    "s_lshl_b32 s3, s56, 12\n"
    "s_lshr_b32 s60, s32, 2\n"
    "s_lshl_b32 s60, s60, 11\n"
    "s_add_u32 s3, s3, s60\n"
    "s_add_u32 s3, s3, 8388608\n"
    "v_add_u32 v210, s3, v250\n"
    "s_lshl_b32 s56, s56, 5\n"
    "s_add_u32 s56, s56, 43008\n"
    "buffer_load_dwordx2 v[252:253], v211, s[20:23], s56 offen sc1\n"
    "s_mov_b64 exec, 1\n"
    "ds_add_u32 v228, v229 offset:4\n"
    "s_mov_b64 exec, -1\n"
    "LsQ:\n"
    "ds_read_b32 v230, v228 offset:4\n"
    "ds_read_b128 v[32:35], v227\n"
    "ds_read_b128 v[36:39], v227 offset:1024\n"
    "ds_read_b128 v[40:43], v227 offset:4096\n"
    "ds_read_b128 v[44:47], v227 offset:5120\n"
    "s_waitcnt lgkmcnt(4)\n"
    "v_readfirstlane_b32 s3, v230\n"
    "s_add_u32 s59, s59, 0x1000\n"
    "s_cselect_b32 s3, 4, s3\n"
    "s_cmp_lt_u32 s3, 4\n"
    "s_cbranch_scc1 LsQ\n"
    "s_waitcnt lgkmcnt(3)\n"
    "v_mfma_f32_16x16x32_bf16 v[16:19], v[200:203], v[32:35], 0\n"
    "v_xor_b32 v48, s61, v32\n"
    "v_xor_b32 v49, s61, v33\n"
    "v_mfma_f32_16x16x32_bf16 v[24:27], v[204:207], v[32:35], 0\n"
    "v_xor_b32 v50, s61, v34\n"
    "v_xor_b32 v51, s61, v35\n"
    "s_waitcnt lgkmcnt(2)\n"
    "v_mfma_f32_16x16x32_bf16 v[16:19], v[96:99], v[36:39], v[16:19]\n"
    "v_xor_b32 v52, s61, v36\n"
    "v_xor_b32 v53, s61, v37\n"
    "v_mfma_f32_16x16x32_bf16 v[24:27], v[100:103], v[36:39], v[24:27]\n"
    "v_xor_b32 v54, s61, v38\n"
    "v_xor_b32 v55, s61, v39\n"
    "s_waitcnt lgkmcnt(1)\n"
    "v_mfma_f32_16x16x32_bf16 v[16:19], v[104:107], v[40:43], v[16:19]\n"
    "v_xor_b32 v56, s61, v40\n"
    "v_xor_b32 v57, s61, v41\n"
    "v_mfma_f32_16x16x32_bf16 v[24:27], v[200:203], v[40:43], v[24:27]\n"
    "v_xor_b32 v58, s61, v42\n"
    "v_xor_b32 v59, s61, v43\n"
    "s_waitcnt lgkmcnt(0)\n"
    "v_mfma_f32_16x16x32_bf16 v[16:19], v[108:111], v[44:47], v[16:19]\n"
    "v_xor_b32 v60, s61, v44\n"
    "v_xor_b32 v61, s61, v45\n"
    "v_mfma_f32_16x16x32_bf16 v[24:27], v[96:99], v[44:47], v[24:27]\n"
    "v_xor_b32 v62, s61, v46\n"
    "v_xor_b32 v63, s61, v47\n"
    "v_mfma_f32_16x16x32_bf16 v[20:23], v[200:203], v[48:51], 0\n"
    "v_lshrrev_b32 v8, 4, v250\n"
    "v_and_b32 v9, 48, v8\n"
    "v_xor_b32 v9, v9, v250\n"
    "v_mfma_f32_16x16x32_bf16 v[28:31], v[204:207], v[48:51], 0\n"
    "v_and_b32 v9, 0x70, v9\n"
    "v_and_b32 v8, 15, v8\n"
    "v_lshl_or_b32 v9, v8, 7, v9\n"
    "v_mfma_f32_16x16x32_bf16 v[20:23], v[96:99], v[52:55], v[20:23]\n"
    "s_lshl_b32 s3, s32, 11\n"
    "s_add_u32 s3, s3, 147456\n"
    "s_and_b32 s60, s32, 2\n"
    "v_mfma_f32_16x16x32_bf16 v[28:31], v[100:103], v[52:55], v[28:31]\n"
    "s_lshl4_add_u32 s62, s60, 0\n"
    "s_lshl1_add_u32 s62, s62, s3\n"
    "v_xor_b32 v9, s62, v9\n"
    "v_mfma_f32_16x16x32_bf16 v[20:23], v[104:107], v[56:59], v[20:23]\n"
    "v_xor_b32 v10, 64, v9\n"
    "v_lshrrev_b32 v8, 7, v250\n"
    "v_lshlrev_b32 v11, 4, v8\n"
    "v_mfma_f32_16x16x32_bf16 v[28:31], v[200:203], v[56:59], v[28:31]\n"
    "v_xor_b32 v11, v11, v250\n"
    "v_add_u32 v11, s3, v11\n"
    "v_and_b32 v12, 0x70, v250\n"
    "v_mfma_f32_16x16x32_bf16 v[20:23], v[108:111], v[60:63], v[20:23]\n"
    "v_lshl_or_b32 v12, v8, 16, v12\n"
    "v_mfma_f32_16x16x32_bf16 v[28:31], v[96:99], v[60:63], v[28:31]\n"
    "Lay:\n"
    "v_cvt_pk_bf16_f32 v0, v16, v24\n"
    "v_cvt_pk_bf16_f32 v1, v17, v25\n"
    "v_cvt_pk_bf16_f32 v2, v18, v26\n"
    "v_cvt_pk_bf16_f32 v3, v19, v27\n"
    "s_nop 3\n"
    "v_cvt_pk_bf16_f32 v4, v20, v28\n"
    "v_cvt_pk_bf16_f32 v5, v21, v29\n"
    "v_cvt_pk_bf16_f32 v6, v22, v30\n"
    "v_cvt_pk_bf16_f32 v7, v23, v31\n"
    "ds_write_b128 v9, v[0:3]\n"
    "ds_write_b128 v10, v[4:7]\n"
    "ds_read_b128 v[32:35], v11\n"
    "ds_read_b128 v[36:39], v11 offset:1024\n"
    "s_lshr_b32 s3, s32, 1\n"
    "s_lshl_b32 s3, s3, 12\n"
    "s_add_u32 s3, s3, s58\n"
    "s_and_b32 s60, s32, 1\n"
    "s_lshl1_add_u32 s3, s3, s60\n"
    "s_lshl_b32 s3, s3, 7\n"
    "s_add_u32 s3, s3, 6291456\n"
    "s_add_u32 s60, s3, 524288\n"
    "s_waitcnt lgkmcnt(1)\n"
    "buffer_store_dwordx4 v[32:35], v12, s[20:23], s3 offen sc1\n"
    "s_waitcnt lgkmcnt(0)\n"
    "buffer_store_dwordx4 v[36:39], v12, s[20:23], s60 offen sc1\n"
    "v_xor_b32 v48, 0x4000, v248\n"
    "v_add_u32 v49, 7168, v48\n"
    "s_lshl_b32 s3, s32, 11\n"
    "s_add_u32 m0, s3, 147456\n"
    "s_lshl3_add_u32 s62, s50, 917504\n"
    "buffer_load_dwordx4 v49, s[16:19], s62 offen offset:1024 lds\n"
    "s_setprio 0\n"
    "s_lshr_b32 s3, s32, 1\n"
    "s_lshl_b32 s3, s3, 8\n"
    "s_add_u32 s3, s3, s58\n"
    "s_mul_i32 s3, s3, 16\n"
    "s_and_b32 s60, s32, 1\n"
    "s_lshl3_add_u32 s3, s60, s3\n"
    "s_add_u32 s54, s3, 34816\n"
    "v_mov_b32 v254, s43\n"
    "v_mov_b32 v255, 1\n"
    "s_waitcnt vmcnt(3)\n"
    "v_cmp_ne_u64 vcc, s[36:37], v[252:253]\n"
    "s_nop 0\n"
    "s_cbranch_vccnz Lsp0_haA\n"
    "Lret_haA:\n"
    "buffer_load_dwordx4 v[112:115], v210, s[20:23], s44 offen sc1\n"
    "buffer_load_dwordx4 v[116:119], v210, s[20:23], s44 offen offset:1024 sc1\n"
    "buffer_load_dwordx4 v[120:123], v210, s[20:23], s45 offen sc1\n"
    "buffer_load_dwordx4 v[124:127], v210, s[20:23], s45 offen offset:1024 sc1\n"
    "buffer_load_dwordx4 v[96:99], v210, s[20:23], s46 offen sc1\n"
    "buffer_load_dwordx4 v[100:103], v210, s[20:23], s46 offen offset:1024 sc1\n"
    "s_mov_b64 exec, 1\n"
    "ds_write_b32 v254, v255 offset:3612\n"
    "s_mov_b64 exec, -1\n"
    "s_movk_i32 s63, 0x110\n"
    "v_lshrrev_b32 v212, 8, v250\n"
    "v_bfe_u32 v213, v250, 7, 1\n"
    "v_lshl_or_b32 v228, v212, 2, v213\n"
    "v_and_b32 v251, 0x70, v250\n"
    "v_lshlrev_b32 v213, 14, v213\n"
    "v_mad_u32_u24 v212, v212, s63, v213\n"
    "v_lshlrev_b32 v213, 1, v251\n"
    "v_lshl_or_b32 v251, v228, 15, v251\n"
    "s_and_b32 s60, s32, 3\n"
    "s_lshl_b32 s3, s60, 19\n"
    "s_add_u32 s57, s57, s3\n"
    "s_mul_i32 s3, s60, 0x440\n"
    "s_and_b32 s3, s3, 0xc40\n"
    "v_xor_b32 v212, v212, v213\n"
    "v_xor_b32 v212, s3, v212\n"
    "v_add_u32 v212, 16384, v212\n"
    "v_xor_b32 v213, 16, v212\n"
    "v_lshrrev_b32 v214, 4, v250\n"
    "v_and_b32 v215, 48, v214\n"
    "v_xor_b32 v215, v215, v250\n"
    "v_and_b32 v215, 0x70, v215\n"
    "v_and_b32 v214, 15, v214\n"
    "v_lshl_or_b32 v214, v214, 8, v215\n"
    "s_add_u32 s3, s43, 16384\n"
    "v_add_u32 v214, s3, v214\n"
    "v_xor_b32 v215, 64, v214\n"
    "v_add_u32 v236, 0x10000, v236\n"
    "v_add_u32 v237, 0x10000, v237\n"
    "v_add_u32 v238, 0x10000, v238\n"
    "v_add_u32 v239, 0x10000, v239\n"
    "ds_read_b32 v228, v254 offset:3604\n"
    "s_waitcnt vmcnt(7)\n"
    "s_mov_b64 exec, 1\n"
    "buffer_store_dwordx2 v[208:209], off, s[20:23], s54 sc1\n"
    "s_mov_b64 exec, -1\n"
    "s_waitcnt lgkmcnt(0)\n"
    "v_cmp_eq_u32 vcc, 0, v228\n"
    "s_nop 0\n"
    "s_cbranch_vccnz Lls_A20\n"
    "Llr_A20:\n"
    "s_waitcnt vmcnt(6)\n"
    "v_mfma_f32_16x16x32_bf16 v[0:3], v[128:131], v[112:115], 0\n"
    "v_mfma_f32_16x16x32_bf16 v[8:11], v[132:135], v[112:115], 0\n"
    "s_lshl3_add_u32 s62, s49, 917504\n"
    "s_add_u32 m0, s43, 49152\n"
    "s_nop 0\n"
    "buffer_load_dwordx4 v248, s[16:19], s62 offen lds\n"
    "v_mfma_f32_16x16x32_bf16 v[16:19], v[136:139], v[112:115], 0\n"
    "v_mfma_f32_16x16x32_bf16 v[24:27], v[140:143], v[112:115], 0\n"
    "s_waitcnt vmcnt(6)\n"
    "v_mfma_f32_16x16x32_bf16 v[28:31], v[140:143], v[116:119], 0\n"
    "buffer_load_dwordx4 v[104:107], v210, s[20:23], s47 offen sc1\n"
    "buffer_load_dwordx4 v[108:111], v210, s[20:23], s47 offen offset:1024 sc1\n"
    "v_mfma_f32_16x16x32_bf16 v[20:23], v[136:139], v[116:119], 0\n"
    "v_mfma_f32_16x16x32_bf16 v[12:15], v[132:135], v[116:119], 0\n"
    "v_mfma_f32_16x16x32_bf16 v[4:7], v[128:131], v[116:119], 0\n"
    "s_waitcnt vmcnt(7)\n"
    "v_mfma_f32_16x16x32_bf16 v[0:3], v[64:67], v[120:123], v[0:3]\n"
    "v_mfma_f32_16x16x32_bf16 v[8:11], v[68:71], v[120:123], v[8:11]\n"
    "buffer_load_dwordx4 v249, s[16:19], s62 offen offset:1024 lds\n"
    "v_mfma_f32_16x16x32_bf16 v[16:19], v[72:75], v[120:123], v[16:19]\n"
    "v_mfma_f32_16x16x32_bf16 v[24:27], v[76:79], v[120:123], v[24:27]\n"
    "s_waitcnt vmcnt(7)\n"
    "v_mfma_f32_16x16x32_bf16 v[28:31], v[76:79], v[124:127], v[28:31]\n"
    "s_add_u32 m0, s43, 51200\n"
    "s_nop 0\n"
    "buffer_load_dwordx4 v48, s[16:19], s62 offen lds\n"
    "buffer_load_dwordx4 v[160:163], v210, s[20:23], s48 offen sc1\n"
    "buffer_load_dwordx4 v[164:167], v210, s[20:23], s48 offen offset:1024 sc1\n"
    "buffer_load_dwordx2 v[252:253], v211, s[20:23], s56 offen offset:896 sc1\n"
    "v_mfma_f32_16x16x32_bf16 v[20:23], v[72:75], v[124:127], v[20:23]\n"
    "v_mfma_f32_16x16x32_bf16 v[12:15], v[68:71], v[124:127], v[12:15]\n"
    "v_mfma_f32_16x16x32_bf16 v[4:7], v[64:67], v[124:127], v[4:7]\n"
    "s_waitcnt vmcnt(10)\n"
    "v_mfma_f32_16x16x32_bf16 v[0:3], v[80:83], v[96:99], v[0:3]\n"
    "v_mfma_f32_16x16x32_bf16 v[8:11], v[84:87], v[96:99], v[8:11]\n"
    "buffer_load_dwordx4 v49, s[16:19], s62 offen offset:1024 lds\n"
    "v_mfma_f32_16x16x32_bf16 v[16:19], v[88:91], v[96:99], v[16:19]\n"
    "v_mfma_f32_16x16x32_bf16 v[24:27], v[92:95], v[96:99], v[24:27]\n"
    "s_waitcnt vmcnt(10)\n"
    "v_mfma_f32_16x16x32_bf16 v[28:31], v[92:95], v[100:103], v[28:31]\n"
    "s_lshl3_add_u32 s62, s50, 917504\n"
    "s_mov_b32 m0, s43\n"
    "s_nop 0\n"
    "buffer_load_dwordx4 v248, s[16:19], s62 offen lds\n"
    "buffer_load_dwordx4 v[168:171], v210, s[20:23], s49 offen sc1\n"
    "buffer_load_dwordx4 v[172:175], v210, s[20:23], s49 offen offset:1024 sc1\n"
    "v_mfma_f32_16x16x32_bf16 v[20:23], v[88:91], v[100:103], v[20:23]\n"
    "v_mfma_f32_16x16x32_bf16 v[12:15], v[84:87], v[100:103], v[12:15]\n"
    "v_mfma_f32_16x16x32_bf16 v[4:7], v[80:83], v[100:103], v[4:7]\n"
    "ds_read_b64_tr_b16 v[64:65], v236\n"
    "ds_read_b64_tr_b16 v[66:67], v236 offset:2048\n"
    "ds_read_b64_tr_b16 v[68:69], v237\n"
    "ds_read_b64_tr_b16 v[70:71], v237 offset:2048\n"
    "ds_read_b64_tr_b16 v[72:73], v238\n"
    "ds_read_b64_tr_b16 v[74:75], v238 offset:2048\n"
    "ds_read_b64_tr_b16 v[76:77], v239\n"
    "ds_read_b64_tr_b16 v[78:79], v239 offset:2048\n"
    "s_waitcnt vmcnt(10)\n"
    "v_mfma_f32_16x16x32_bf16 v[0:3], v[220:223], v[104:107], v[0:3]\n"
    "v_mfma_f32_16x16x32_bf16 v[8:11], v[240:243], v[104:107], v[8:11]\n"
    "buffer_load_dwordx4 v249, s[16:19], s62 offen offset:1024 lds\n"
    "v_mfma_f32_16x16x32_bf16 v[16:19], v[244:247], v[104:107], v[16:19]\n"
    "v_mfma_f32_16x16x32_bf16 v[24:27], v[232:235], v[104:107], v[24:27]\n"
    "s_waitcnt vmcnt(10)\n"
    "v_mfma_f32_16x16x32_bf16 v[28:31], v[232:235], v[108:111], v[28:31]\n"
    "s_add_u32 m0, s43, 2048\n"
    "s_nop 0\n"
    "buffer_load_dwordx4 v48, s[16:19], s62 offen lds\n"
    "buffer_load_dwordx4 v[176:179], v210, s[20:23], s50 offen sc1\n"
    "buffer_load_dwordx4 v[180:183], v210, s[20:23], s50 offen offset:1024 sc1\n"
    "v_mfma_f32_16x16x32_bf16 v[20:23], v[244:247], v[108:111], v[20:23]\n"
    "v_mfma_f32_16x16x32_bf16 v[12:15], v[240:243], v[108:111], v[12:15]\n"
    "v_mfma_f32_16x16x32_bf16 v[4:7], v[220:223], v[108:111], v[4:7]\n"
    "ds_read_b64_tr_b16 v[80:81], v236 offset:16384\n"
    "ds_read_b64_tr_b16 v[82:83], v236 offset:18432\n"
    "ds_read_b64_tr_b16 v[84:85], v237 offset:16384\n"
    "ds_read_b64_tr_b16 v[86:87], v237 offset:18432\n"
    "ds_read_b64_tr_b16 v[88:89], v238 offset:16384\n"
    "ds_read_b64_tr_b16 v[90:91], v238 offset:18432\n"
    "ds_read_b64_tr_b16 v[92:93], v239 offset:16384\n"
    "ds_read_b64_tr_b16 v[94:95], v239 offset:18432\n"
    "s_waitcnt vmcnt(10) lgkmcnt(14)\n"
    "v_mfma_f32_16x16x32_bf16 v[0:3], v[64:67], v[160:163], v[0:3]\n"
    "s_waitcnt lgkmcnt(12)\n"
    "v_mfma_f32_16x16x32_bf16 v[8:11], v[68:71], v[160:163], v[8:11]\n"
    "s_waitcnt lgkmcnt(10)\n"
    "v_mfma_f32_16x16x32_bf16 v[16:19], v[72:75], v[160:163], v[16:19]\n"
    "s_waitcnt lgkmcnt(8)\n"
    "v_mfma_f32_16x16x32_bf16 v[24:27], v[76:79], v[160:163], v[24:27]\n"
    "s_waitcnt vmcnt(9)\n"
    "v_mfma_f32_16x16x32_bf16 v[28:31], v[76:79], v[164:167], v[28:31]\n"
    "s_waitcnt vmcnt(8)\n"
    "v_cmp_ne_u64 vcc, s[36:37], v[252:253]\n"
    "s_nop 0\n"
    "s_cbranch_vccnz Lsp0_hbA\n"
    "Lret_hbA:\n"
    "v_add_u32 v210, 114688, v210\n"
    "buffer_load_dwordx4 v[184:187], v210, s[20:23], s44 offen sc1\n"
    "buffer_load_dwordx4 v[188:191], v210, s[20:23], s44 offen offset:1024 sc1\n"
    "v_mfma_f32_16x16x32_bf16 v[20:23], v[72:75], v[164:167], v[20:23]\n"
    "v_mfma_f32_16x16x32_bf16 v[12:15], v[68:71], v[164:167], v[12:15]\n"
    "v_mfma_f32_16x16x32_bf16 v[4:7], v[64:67], v[164:167], v[4:7]\n"
    "ds_read_b64_tr_b16 v[64:65], v236 offset:32768\n"
    "ds_read_b64_tr_b16 v[66:67], v236 offset:34816\n"
    "ds_read_b64_tr_b16 v[68:69], v237 offset:32768\n"
    "ds_read_b64_tr_b16 v[70:71], v237 offset:34816\n"
    "ds_read_b64_tr_b16 v[72:73], v238 offset:32768\n"
    "ds_read_b64_tr_b16 v[74:75], v238 offset:34816\n"
    "ds_read_b64_tr_b16 v[76:77], v239 offset:32768\n"
    "ds_read_b64_tr_b16 v[78:79], v239 offset:34816\n"
    "s_waitcnt vmcnt(7) lgkmcnt(14)\n"
    "v_mfma_f32_16x16x32_bf16 v[0:3], v[80:83], v[168:171], v[0:3]\n"
    "s_waitcnt lgkmcnt(12)\n"
    "v_mfma_f32_16x16x32_bf16 v[8:11], v[84:87], v[168:171], v[8:11]\n"
    "s_waitcnt lgkmcnt(10)\n"
    "v_mfma_f32_16x16x32_bf16 v[16:19], v[88:91], v[168:171], v[16:19]\n"
    "s_waitcnt lgkmcnt(8)\n"
    "v_mfma_f32_16x16x32_bf16 v[24:27], v[92:95], v[168:171], v[24:27]\n"
    "s_waitcnt vmcnt(6)\n"
    "v_mfma_f32_16x16x32_bf16 v[28:31], v[92:95], v[172:175], v[28:31]\n"
    "buffer_load_dwordx4 v[192:195], v210, s[20:23], s45 offen sc1\n"
    "buffer_load_dwordx4 v[196:199], v210, s[20:23], s45 offen offset:1024 sc1\n"
    "v_mfma_f32_16x16x32_bf16 v[20:23], v[88:91], v[172:175], v[20:23]\n"
    "v_mfma_f32_16x16x32_bf16 v[12:15], v[84:87], v[172:175], v[12:15]\n"
    "v_mfma_f32_16x16x32_bf16 v[4:7], v[80:83], v[172:175], v[4:7]\n"
    "s_waitcnt vmcnt(5) lgkmcnt(6)\n"
    "v_mfma_f32_16x16x32_bf16 v[0:3], v[64:67], v[176:179], v[0:3]\n"
    "s_waitcnt lgkmcnt(4)\n"
    "v_mfma_f32_16x16x32_bf16 v[8:11], v[68:71], v[176:179], v[8:11]\n"
    "s_waitcnt lgkmcnt(2)\n"
    "v_mfma_f32_16x16x32_bf16 v[16:19], v[72:75], v[176:179], v[16:19]\n"
    "s_waitcnt lgkmcnt(0)\n"
    "v_mfma_f32_16x16x32_bf16 v[24:27], v[76:79], v[176:179], v[24:27]\n"
    "s_waitcnt vmcnt(4)\n"
    "v_mfma_f32_16x16x32_bf16 v[28:31], v[76:79], v[180:183], v[28:31]\n"
    "buffer_load_dwordx4 v[200:203], v210, s[20:23], s46 offen sc1\n"
    "buffer_load_dwordx4 v[204:207], v210, s[20:23], s46 offen offset:1024 sc1\n"
    "v_mfma_f32_16x16x32_bf16 v[20:23], v[72:75], v[180:183], v[20:23]\n"
    "v_mfma_f32_16x16x32_bf16 v[12:15], v[68:71], v[180:183], v[12:15]\n"
    "v_mfma_f32_16x16x32_bf16 v[4:7], v[64:67], v[180:183], v[4:7]\n"
    "v_add_u32 v236, 0xc000, v236\n"
    "v_add_u32 v237, 0xc000, v237\n"
    "v_add_u32 v238, 0xc000, v238\n"
    "v_add_u32 v239, 0xc000, v239\n"
    "ds_read_b64_tr_b16 v[80:81], v236\n"
    "ds_read_b64_tr_b16 v[82:83], v236 offset:2048\n"
    "ds_read_b64_tr_b16 v[84:85], v237\n"
    "ds_read_b64_tr_b16 v[86:87], v237 offset:2048\n"
    "ds_read_b64_tr_b16 v[88:89], v238\n"
    "ds_read_b64_tr_b16 v[90:91], v238 offset:2048\n"
    "ds_read_b64_tr_b16 v[92:93], v239\n"
    "ds_read_b64_tr_b16 v[94:95], v239 offset:2048\n"
    "s_waitcnt vmcnt(5)\n"
    "v_mfma_f32_16x16x32_bf16 v[0:3], v[144:147], v[184:187], v[0:3]\n"
    "v_mfma_f32_16x16x32_bf16 v[8:11], v[148:151], v[184:187], v[8:11]\n"
    "v_mfma_f32_16x16x32_bf16 v[16:19], v[152:155], v[184:187], v[16:19]\n"
    "v_mfma_f32_16x16x32_bf16 v[24:27], v[156:159], v[184:187], v[24:27]\n"
    "s_waitcnt vmcnt(4)\n"
    "v_mfma_f32_16x16x32_bf16 v[28:31], v[156:159], v[188:191], v[28:31]\n"
    "buffer_load_dwordx4 v[128:131], v210, s[20:23], s47 offen sc1\n"
    "buffer_load_dwordx4 v[132:135], v210, s[20:23], s47 offen offset:1024 sc1\n"
    "v_mfma_f32_16x16x32_bf16 v[20:23], v[152:155], v[188:191], v[20:23]\n"
    "v_mfma_f32_16x16x32_bf16 v[12:15], v[148:151], v[188:191], v[12:15]\n"
    "v_mfma_f32_16x16x32_bf16 v[4:7], v[144:147], v[188:191], v[4:7]\n"
    "ds_read_b64_tr_b16 v[64:65], v236 offset:16384\n"
    "ds_read_b64_tr_b16 v[66:67], v236 offset:18432\n"
    "ds_read_b64_tr_b16 v[68:69], v237 offset:16384\n"
    "ds_read_b64_tr_b16 v[70:71], v237 offset:18432\n"
    "ds_read_b64_tr_b16 v[72:73], v238 offset:16384\n"
    "ds_read_b64_tr_b16 v[74:75], v238 offset:18432\n"
    "ds_read_b64_tr_b16 v[76:77], v239 offset:16384\n"
    "ds_read_b64_tr_b16 v[78:79], v239 offset:18432\n"
    "s_waitcnt vmcnt(5) lgkmcnt(14)\n"
    "v_mfma_f32_16x16x32_bf16 v[0:3], v[80:83], v[192:195], v[0:3]\n"
    "s_waitcnt lgkmcnt(12)\n"
    "v_mfma_f32_16x16x32_bf16 v[8:11], v[84:87], v[192:195], v[8:11]\n"
    "s_waitcnt lgkmcnt(10)\n"
    "v_mfma_f32_16x16x32_bf16 v[16:19], v[88:91], v[192:195], v[16:19]\n"
    "s_waitcnt lgkmcnt(8)\n"
    "v_mfma_f32_16x16x32_bf16 v[24:27], v[92:95], v[192:195], v[24:27]\n"
    "s_waitcnt vmcnt(4)\n"
    "v_mfma_f32_16x16x32_bf16 v[28:31], v[92:95], v[196:199], v[28:31]\n"
    "buffer_load_dwordx4 v[136:139], v210, s[20:23], s48 offen sc1\n"
    "buffer_load_dwordx4 v[140:143], v210, s[20:23], s48 offen offset:1024 sc1\n"
    "v_mfma_f32_16x16x32_bf16 v[20:23], v[88:91], v[196:199], v[20:23]\n"
    "buffer_load_dwordx4 v[32:35], v251, s[4:7], s57 offen\n"
    "v_mfma_f32_16x16x32_bf16 v[12:15], v[84:87], v[196:199], v[12:15]\n"
    "s_mov_b64 exec, 1\n"
    "ds_write_b32 v254, v255 offset:3600\n"
    "s_mov_b64 exec, -1\n"
    "v_mfma_f32_16x16x32_bf16 v[4:7], v[80:83], v[196:199], v[4:7]\n"
    "ds_read_b32 v228, v254 offset:3616\n"
    "s_waitcnt lgkmcnt(0)\n"
    "v_cmp_eq_u32 vcc, 0, v228\n"
    "s_nop 0\n"
    "s_cbranch_vccnz Lls_A32\n"
    "Llr_A32:\n"
    "v_add_u32 v236, 0xfffe4000, v236\n"
    "v_add_u32 v237, 0xfffe4000, v237\n"
    "v_add_u32 v238, 0xfffe4000, v238\n"
    "v_add_u32 v239, 0xfffe4000, v239\n"
    "ds_read_b64_tr_b16 v[80:81], v236 offset:16384\n"
    "ds_read_b64_tr_b16 v[82:83], v236 offset:18432\n"
    "ds_read_b64_tr_b16 v[84:85], v237 offset:16384\n"
    "ds_read_b64_tr_b16 v[86:87], v237 offset:18432\n"
    "ds_read_b64_tr_b16 v[88:89], v238 offset:16384\n"
    "ds_read_b64_tr_b16 v[90:91], v238 offset:18432\n"
    "ds_read_b64_tr_b16 v[92:93], v239 offset:16384\n"
    "ds_read_b64_tr_b16 v[94:95], v239 offset:18432\n"
    "s_waitcnt vmcnt(6)\n"
    "v_mfma_f32_16x16x32_bf16 v[0:3], v[64:67], v[200:203], v[0:3]\n"
    "v_mfma_f32_16x16x32_bf16 v[8:11], v[68:71], v[200:203], v[8:11]\n"
    "v_mfma_f32_16x16x32_bf16 v[16:19], v[72:75], v[200:203], v[16:19]\n"
    "v_mfma_f32_16x16x32_bf16 v[24:27], v[76:79], v[200:203], v[24:27]\n"
    "s_waitcnt vmcnt(5)\n"
    "v_mfma_f32_16x16x32_bf16 v[28:31], v[76:79], v[204:207], v[28:31]\n"
    "buffer_load_dwordx4 v[112:115], v210, s[20:23], s49 offen sc1\n"
    "buffer_load_dwordx4 v[116:119], v210, s[20:23], s49 offen offset:1024 sc1\n"
    "v_mfma_f32_16x16x32_bf16 v[20:23], v[72:75], v[204:207], v[20:23]\n"
    "v_mfma_f32_16x16x32_bf16 v[12:15], v[68:71], v[204:207], v[12:15]\n"
    "v_mfma_f32_16x16x32_bf16 v[4:7], v[64:67], v[204:207], v[4:7]\n"
    "ds_read_b64_tr_b16 v[64:65], v236 offset:32768\n"
    "ds_read_b64_tr_b16 v[66:67], v236 offset:34816\n"
    "ds_read_b64_tr_b16 v[68:69], v237 offset:32768\n"
    "ds_read_b64_tr_b16 v[70:71], v237 offset:34816\n"
    "ds_read_b64_tr_b16 v[72:73], v238 offset:32768\n"
    "ds_read_b64_tr_b16 v[74:75], v238 offset:34816\n"
    "ds_read_b64_tr_b16 v[76:77], v239 offset:32768\n"
    "ds_read_b64_tr_b16 v[78:79], v239 offset:34816\n"
    "s_waitcnt vmcnt(6) lgkmcnt(14)\n"
    "v_mfma_f32_16x16x32_bf16 v[0:3], v[80:83], v[128:131], v[0:3]\n"
    "s_waitcnt lgkmcnt(12)\n"
    "v_mfma_f32_16x16x32_bf16 v[8:11], v[84:87], v[128:131], v[8:11]\n"
    "s_waitcnt lgkmcnt(10)\n"
    "v_mfma_f32_16x16x32_bf16 v[16:19], v[88:91], v[128:131], v[16:19]\n"
    "s_waitcnt lgkmcnt(8)\n"
    "v_mfma_f32_16x16x32_bf16 v[24:27], v[92:95], v[128:131], v[24:27]\n"
    "s_waitcnt vmcnt(5)\n"
    "v_mfma_f32_16x16x32_bf16 v[28:31], v[92:95], v[132:135], v[28:31]\n"
    "buffer_load_dwordx4 v[120:123], v210, s[20:23], s50 offen sc1\n"
    "buffer_load_dwordx4 v[124:127], v210, s[20:23], s50 offen offset:1024 sc1\n"
    "v_mfma_f32_16x16x32_bf16 v[20:23], v[88:91], v[132:135], v[20:23]\n"
    "v_mfma_f32_16x16x32_bf16 v[12:15], v[84:87], v[132:135], v[12:15]\n"
    "v_mfma_f32_16x16x32_bf16 v[4:7], v[80:83], v[132:135], v[4:7]\n"
    "ds_read_b64_tr_b16 v[80:81], v236 offset:49152\n"
    "ds_read_b64_tr_b16 v[82:83], v236 offset:51200\n"
    "ds_read_b64_tr_b16 v[84:85], v237 offset:49152\n"
    "ds_read_b64_tr_b16 v[86:87], v237 offset:51200\n"
    "ds_read_b64_tr_b16 v[88:89], v238 offset:49152\n"
    "ds_read_b64_tr_b16 v[90:91], v238 offset:51200\n"
    "ds_read_b64_tr_b16 v[92:93], v239 offset:49152\n"
    "ds_read_b64_tr_b16 v[94:95], v239 offset:51200\n"
    "s_waitcnt vmcnt(6) lgkmcnt(14)\n"
    "v_mfma_f32_16x16x32_bf16 v[0:3], v[64:67], v[136:139], v[0:3]\n"
    "s_waitcnt lgkmcnt(12)\n"
    "v_mfma_f32_16x16x32_bf16 v[8:11], v[68:71], v[136:139], v[8:11]\n"
    "s_waitcnt lgkmcnt(10)\n"
    "v_mfma_f32_16x16x32_bf16 v[16:19], v[72:75], v[136:139], v[16:19]\n"
    "s_waitcnt lgkmcnt(8)\n"
    "v_mfma_f32_16x16x32_bf16 v[24:27], v[76:79], v[136:139], v[24:27]\n"
    "s_waitcnt vmcnt(5)\n"
    "v_mfma_f32_16x16x32_bf16 v[28:31], v[76:79], v[140:143], v[28:31]\n"
    "v_mfma_f32_16x16x32_bf16 v[20:23], v[72:75], v[140:143], v[20:23]\n"
    "v_mfma_f32_16x16x32_bf16 v[12:15], v[68:71], v[140:143], v[12:15]\n"
    "v_mfma_f32_16x16x32_bf16 v[4:7], v[64:67], v[140:143], v[4:7]\n"
    "s_lshr_b32 s3, s43, 1\n"
    "s_sub_u32 s3, 145408, s3\n"
    "ds_read_b64_tr_b16 v[64:65], v236\n"
    "ds_read_b64_tr_b16 v[68:69], v237\n"
    "ds_read_b64_tr_b16 v[72:73], v238\n"
    "ds_read_b64_tr_b16 v[76:77], v239\n"
    "s_mov_b32 exec_lo, 0\n"
    "v_add_u32 v236, s3, v236\n"
    "v_add_u32 v237, s3, v237\n"
    "v_add_u32 v238, s3, v238\n"
    "v_add_u32 v239, s3, v239\n"
    "s_mov_b32 exec_lo, -1\n"
    "ds_read_b64_tr_b16 v[66:67], v236 offset:2048\n"
    "ds_read_b64_tr_b16 v[70:71], v237 offset:2048\n"
    "ds_read_b64_tr_b16 v[74:75], v238 offset:2048\n"
    "ds_read_b64_tr_b16 v[78:79], v239 offset:2048\n"
    "s_waitcnt vmcnt(3) lgkmcnt(14)\n"
    "v_mfma_f32_16x16x32_bf16 v[0:3], v[80:83], v[112:115], v[0:3]\n"
    "s_waitcnt lgkmcnt(12)\n"
    "v_mfma_f32_16x16x32_bf16 v[8:11], v[84:87], v[112:115], v[8:11]\n"
    "s_waitcnt lgkmcnt(10)\n"
    "v_mfma_f32_16x16x32_bf16 v[16:19], v[88:91], v[112:115], v[16:19]\n"
    "s_waitcnt lgkmcnt(8)\n"
    "v_mfma_f32_16x16x32_bf16 v[24:27], v[92:95], v[112:115], v[24:27]\n"
    "s_waitcnt vmcnt(2)\n"
    "v_mfma_f32_16x16x32_bf16 v[28:31], v[92:95], v[116:119], v[28:31]\n"
    "v_mfma_f32_16x16x32_bf16 v[20:23], v[88:91], v[116:119], v[20:23]\n"
    "v_mfma_f32_16x16x32_bf16 v[12:15], v[84:87], v[116:119], v[12:15]\n"
    "v_mfma_f32_16x16x32_bf16 v[4:7], v[80:83], v[116:119], v[4:7]\n"
    "ds_read_b32 v228, v254 offset:3608\n"
    "s_waitcnt vmcnt(1) lgkmcnt(4)\n"
    "v_mfma_f32_16x16x32_bf16 v[0:3], v[64:67], v[120:123], v[0:3]\n"
    "s_waitcnt lgkmcnt(3)\n"
    "v_mfma_f32_16x16x32_bf16 v[8:11], v[68:71], v[120:123], v[8:11]\n"
    "s_waitcnt lgkmcnt(2)\n"
    "v_mfma_f32_16x16x32_bf16 v[16:19], v[72:75], v[120:123], v[16:19]\n"
    "s_waitcnt lgkmcnt(1)\n"
    "v_mfma_f32_16x16x32_bf16 v[24:27], v[76:79], v[120:123], v[24:27]\n"
    "s_waitcnt vmcnt(0)\n"
    "v_mfma_f32_16x16x32_bf16 v[4:7], v[64:67], v[124:127], v[4:7]\n"
    "v_mfma_f32_16x16x32_bf16 v[12:15], v[68:71], v[124:127], v[12:15]\n"
    "s_waitcnt lgkmcnt(0)\n"
    "v_cmp_eq_u32 vcc, 0, v228\n"
    "s_nop 0\n"
    "s_cbranch_vccnz Lls_A24\n"
    "Llr_A24:\n"
    "v_mfma_f32_16x16x32_bf16 v[20:23], v[72:75], v[124:127], v[20:23]\n"
    "ds_write_b128 v214, v[0:3]\n"
    "v_mfma_f32_16x16x32_bf16 v[28:31], v[76:79], v[124:127], v[28:31]\n"
    "ds_write_b128 v215, v[8:11]\n"
    "ds_write_b128 v214, v[16:19] offset:128\n"
    "ds_write_b128 v215, v[24:27] offset:128\n"
    "ds_write_b128 v214, v[4:7] offset:16384\n"
    "ds_write_b128 v215, v[12:15] offset:16384\n"
    "s_nop 0\n"
    "ds_write_b128 v214, v[20:23] offset:16512\n"
    "s_nop 0\n"
    "ds_write_b128 v215, v[28:31] offset:16512\n"
    "v_mov_b32 v228, 3584\n"
    "v_mov_b32 v229, 1\n"
    "s_mov_b64 exec, 1\n"
    "ds_add_u32 v228, v229 offset:44\n"
    "s_mov_b64 exec, -1\n"
    "v_lshlrev_b32 v160, 16, v32\n"
    "v_and_b32 v161, 0xffff0000, v32\n"
    "v_lshlrev_b32 v162, 16, v33\n"
    "v_and_b32 v163, 0xffff0000, v33\n"
    "v_lshlrev_b32 v164, 16, v34\n"
    "v_and_b32 v165, 0xffff0000, v34\n"
    "v_lshlrev_b32 v166, 16, v35\n"
    "v_and_b32 v167, 0xffff0000, v35\n"
    "LrA:\n"
    "ds_read_b32 v230, v228 offset:44\n"
    "s_sleep 1\n"
    "s_waitcnt lgkmcnt(0)\n"
    "v_readfirstlane_b32 s3, v230\n"
    "s_add_u32 s59, s59, 0x1000\n"
    "s_cselect_b32 s3, 4, s3\n"
    "s_cmp_lt_u32 s3, 4\n"
    "s_cbranch_scc1 LrA\n"
    "ds_read_b128 v[64:67], v212\n"
    "ds_read_b128 v[68:71], v213\n"
    "ds_read_b128 v[72:75], v212 offset:4096\n"
    "ds_read_b128 v[76:79], v213 offset:4096\n"
    "ds_read_b128 v[80:83], v212 offset:8192\n"
    "ds_read_b128 v[84:87], v213 offset:8192\n"
    "ds_read_b128 v[88:91], v212 offset:12288\n"
    "ds_read_b128 v[92:95], v213 offset:12288\n"
    "s_waitcnt lgkmcnt(5)\n"
    "v_pk_add_f32 v[64:65], v[64:65], v[72:73]\n"
    "v_pk_add_f32 v[66:67], v[66:67], v[74:75]\n"
    "s_waitcnt lgkmcnt(4)\n"
    "v_pk_add_f32 v[68:69], v[68:69], v[76:77]\n"
    "v_pk_add_f32 v[70:71], v[70:71], v[78:79]\n"
    "v_pk_add_f32 v[160:161], v[160:161], v[64:65]\n"
    "v_pk_add_f32 v[162:163], v[162:163], v[66:67]\n"
    "v_pk_add_f32 v[164:165], v[164:165], v[68:69]\n"
    "v_pk_add_f32 v[166:167], v[166:167], v[70:71]\n"
    "s_waitcnt lgkmcnt(1)\n"
    "v_pk_add_f32 v[80:81], v[80:81], v[88:89]\n"
    "v_pk_add_f32 v[82:83], v[82:83], v[90:91]\n"
    "s_waitcnt lgkmcnt(0)\n"
    "v_pk_add_f32 v[84:85], v[84:85], v[92:93]\n"
    "v_pk_add_f32 v[86:87], v[86:87], v[94:95]\n"
    "v_pk_add_f32 v[160:161], v[160:161], v[80:81]\n"
    "v_pk_add_f32 v[162:163], v[162:163], v[82:83]\n"
    "v_pk_add_f32 v[164:165], v[164:165], v[84:85]\n"
    "v_pk_add_f32 v[166:167], v[166:167], v[86:87]\n"
    "s_waitcnt lgkmcnt(0)\n"
    "s_barrier\n"
    "ds_read_b128 v[96:99], v212 offset:49152\n"
    "ds_read_b128 v[100:103], v213 offset:49152\n"
    "ds_read_b128 v[104:107], v212 offset:53248\n"
    "ds_read_b128 v[108:111], v213 offset:53248\n"
    "ds_read_b128 v[112:115], v212 offset:57344\n"
    "ds_read_b128 v[116:119], v213 offset:57344\n"
    "ds_read_b128 v[120:123], v212 offset:61440\n"
    "ds_read_b128 v[124:127], v213 offset:61440\n"
    "s_waitcnt lgkmcnt(5)\n"
    "v_pk_add_f32 v[96:97], v[96:97], v[104:105]\n"
    "v_pk_add_f32 v[98:99], v[98:99], v[106:107]\n"
    "s_waitcnt lgkmcnt(4)\n"
    "v_pk_add_f32 v[100:101], v[100:101], v[108:109]\n"
    "v_pk_add_f32 v[102:103], v[102:103], v[110:111]\n"
    "v_pk_add_f32 v[160:161], v[160:161], v[96:97]\n"
    "v_pk_add_f32 v[162:163], v[162:163], v[98:99]\n"
    "v_pk_add_f32 v[164:165], v[164:165], v[100:101]\n"
    "v_pk_add_f32 v[166:167], v[166:167], v[102:103]\n"
    "s_waitcnt lgkmcnt(1)\n"
    "v_pk_add_f32 v[112:113], v[112:113], v[120:121]\n"
    "v_pk_add_f32 v[114:115], v[114:115], v[122:123]\n"
    "s_waitcnt lgkmcnt(0)\n"
    "v_pk_add_f32 v[116:117], v[116:117], v[124:125]\n"
    "v_pk_add_f32 v[118:119], v[118:119], v[126:127]\n"
    "v_pk_add_f32 v[160:161], v[160:161], v[112:113]\n"
    "v_pk_add_f32 v[162:163], v[162:163], v[114:115]\n"
    "v_pk_add_f32 v[164:165], v[164:165], v[116:117]\n"
    "v_pk_add_f32 v[166:167], v[166:167], v[118:119]\n"
    "v_cvt_pk_bf16_f32 v0, v160, v161\n"
    "v_cvt_pk_bf16_f32 v1, v162, v163\n"
    "v_cvt_pk_bf16_f32 v2, v164, v165\n"
    "v_cvt_pk_bf16_f32 v3, v166, v167\n"
    "buffer_store_dwordx4 v[0:3], v251, s[24:27], s57 offen\n"
    "s_or_b32 s3, s2, s32\n"
    "s_cbranch_scc1 Lfin\n"
    "v_mov_b32 v4, s35\n"
    "s_mov_b64 exec, 1\n"
    "buffer_store_dword v4, off, s[20:23], 0 sc1\n"
    "Lfin:\n"
    "s_endpgm\n"
    "Lsp0_dz:\n"
    "Lspin_dz:\n"
    "s_sleep 4\n"
    "buffer_load_dwordx2 v[254:255], v251, s[20:23], s54 offen sc1\n"
    "s_waitcnt vmcnt(0)\n"
    "v_cmp_ne_u64 vcc, s[36:37], v[254:255]\n"
    "s_nop 0\n"
    "s_cbranch_vccz Lret_dz\n"
    "s_add_u32 s59, s59, 0x10000\n"
    "s_cbranch_scc0 Lspin_dz\n"
    "s_branch Lret_dz\n"
    "Lsp0_stB:\n"
    "Lspin_stB:\n"
    "s_sleep 4\n"
    "buffer_load_dwordx4 v[224:227], v251, s[20:23], s54 offen sc1\n"
    "buffer_load_dwordx4 v[228:231], v251, s[20:23], s54 offen offset:16 sc1\n"
    "s_waitcnt vmcnt(0)\n"
    "v_xor_b32 v220, s36, v225\n"
    "v_bitop3_b32 v220, v227, s36, v220 bitop3:0xbe\n"
    "v_bitop3_b32 v220, v229, s36, v220 bitop3:0xbe\n"
    "v_bitop3_b32 v220, v231, s36, v220 bitop3:0xbe\n"
    "v_cmp_ne_u32 vcc, 0, v220\n"
    "s_nop 0\n"
    "s_cbranch_vccz Lret_stB\n"
    "s_add_u32 s59, s59, 0x10000\n"
    "s_cbranch_scc0 Lspin_stB\n"
    "s_branch Lret_stB\n"
    "Lsp0_cxB:\n"
    "Lspin_cxB:\n"
    "s_sleep 4\n"
    "buffer_load_dwordx4 v[216:219], v247, s[20:23], s28 offen sc1\n"
    "s_waitcnt vmcnt(0)\n"
    "v_xor_b32 v220, s36, v217\n"
    "v_bitop3_b32 v220, v219, s36, v220 bitop3:0xbe\n"
    "v_cmp_ne_u32 vcc, 0, v220\n"
    "s_nop 0\n"
    "s_cbranch_vccz Lret_cxB\n"
    "s_add_u32 s59, s59, 0x10000\n"
    "s_cbranch_scc0 Lspin_cxB\n"
    "s_branch Lret_cxB\n"
    "Lsp0_hbB:\n"
    "Lspin_hbB:\n"
    "s_sleep 4\n"
    "buffer_load_dwordx2 v[230:231], v211, s[20:23], s56 offen offset:896 sc1\n"
    "s_waitcnt vmcnt(0)\n"
    "v_cmp_ne_u64 vcc, s[36:37], v[230:231]\n"
    "s_nop 0\n"
    "s_cbranch_vccz Lret_hbB\n"
    "s_add_u32 s59, s59, 0x10000\n"
    "s_cbranch_scc0 Lspin_hbB\n"
    "s_branch Lret_hbB\n"
    "Lsp0_y:\n"
    "Lspin_y:\n"
    "s_sleep 16\n"
    "buffer_load_dwordx4 v[216:219], v213, s[20:23], s54 offen sc1\n"
    "s_waitcnt vmcnt(0)\n"
    "v_cmp_ne_u64 vcc, s[36:37], v[216:217]\n"
    "v_cmp_ne_u64 s[60:61], s[36:37], v[218:219]\n"
    "s_nop 0\n"
    "s_or_b64 vcc, vcc, s[60:61]\n"
    "s_cbranch_vccz Lret_y\n"
    "s_add_u32 s59, s59, 0x10000\n"
    "s_cbranch_scc0 Lspin_y\n"
    "s_branch Lret_y\n"
    "Lsp0_stA:\n"
    "Lspin_stA:\n"
    "s_sleep 4\n"
    "buffer_load_dwordx4 v[224:227], v251, s[20:23], s54 offen sc1\n"
    "buffer_load_dwordx4 v[228:231], v251, s[20:23], s54 offen offset:16 sc1\n"
    "s_waitcnt vmcnt(0)\n"
    "v_xor_b32 v220, s36, v225\n"
    "v_bitop3_b32 v220, v227, s36, v220 bitop3:0xbe\n"
    "v_bitop3_b32 v220, v229, s36, v220 bitop3:0xbe\n"
    "v_bitop3_b32 v220, v231, s36, v220 bitop3:0xbe\n"
    "v_cmp_ne_u32 vcc, 0, v220\n"
    "s_nop 0\n"
    "s_cbranch_vccz Lret_stA\n"
    "s_add_u32 s59, s59, 0x10000\n"
    "s_cbranch_scc0 Lspin_stA\n"
    "s_branch Lret_stA\n"
    "Lsp0_cxA:\n"
    "Lspin_cxA:\n"
    "s_sleep 4\n"
    "buffer_load_dwordx4 v[216:219], v247, s[20:23], s28 offen sc1\n"
    "s_waitcnt vmcnt(0)\n"
    "v_xor_b32 v220, s36, v217\n"
    "v_bitop3_b32 v220, v219, s36, v220 bitop3:0xbe\n"
    "v_cmp_ne_u32 vcc, 0, v220\n"
    "s_nop 0\n"
    "s_cbranch_vccz Lret_cxA\n"
    "s_add_u32 s59, s59, 0x10000\n"
    "s_cbranch_scc0 Lspin_cxA\n"
    "s_branch Lret_cxA\n"
    "Lsp0_az:\n"
    "Lspin_az:\n"
    "s_sleep 4\n"
    "buffer_load_dwordx2 v[252:253], v251, s[20:23], s54 offen sc1\n"
    "s_waitcnt vmcnt(0)\n"
    "v_cmp_ne_u64 vcc, s[36:37], v[252:253]\n"
    "s_nop 0\n"
    "s_cbranch_vccz Lret_az\n"
    "s_add_u32 s59, s59, 0x10000\n"
    "s_cbranch_scc0 Lspin_az\n"
    "s_branch Lret_az\n"
    "Lsp0_haA:\n"
    "buffer_load_dwordx2 v[252:253], v211, s[20:23], s56 offen sc1\n"
    "s_waitcnt vmcnt(0)\n"
    "v_cmp_ne_u64 vcc, s[36:37], v[252:253]\n"
    "s_mov_b64 exec, 1\n"
    "buffer_store_dwordx2 v[208:209], off, s[20:23], s54 sc1\n"
    "s_mov_b64 exec, -1\n"
    "s_cbranch_vccz Lret_haA\n"
    "Lspin_haA:\n"
    "s_sleep 4\n"
    "buffer_load_dwordx2 v[252:253], v211, s[20:23], s56 offen sc1\n"
    "s_waitcnt vmcnt(0)\n"
    "v_cmp_ne_u64 vcc, s[36:37], v[252:253]\n"
    "s_nop 0\n"
    "s_cbranch_vccz Lret_haA\n"
    "s_add_u32 s59, s59, 0x10000\n"
    "s_cbranch_scc0 Lspin_haA\n"
    "s_branch Lret_haA\n"
    "Lsp0_hbA:\n"
    "Lspin_hbA:\n"
    "s_sleep 4\n"
    "buffer_load_dwordx2 v[252:253], v211, s[20:23], s56 offen offset:896 sc1\n"
    "s_waitcnt vmcnt(0)\n"
    "v_cmp_ne_u64 vcc, s[36:37], v[252:253]\n"
    "s_nop 0\n"
    "s_cbranch_vccz Lret_hbA\n"
    "s_add_u32 s59, s59, 0x10000\n"
    "s_cbranch_scc0 Lspin_hbA\n"
    "s_branch Lret_hbA\n"
    "Lls_B12:\n"
    "ds_read_b32 v228, v254 offset:1548\n"
    "s_add_u32 s59, s59, 0x400\n"
    "s_cbranch_scc1 Llr_B12\n"
    "s_waitcnt lgkmcnt(0)\n"
    "v_cmp_eq_u32 vcc, 0, v228\n"
    "s_nop 0\n"
    "s_cbranch_vccnz Lls_B12\n"
    "s_branch Llr_B12\n"
    "Lls_B28:\n"
    "ds_read_b32 v228, v254 offset:1564\n"
    "s_add_u32 s59, s59, 0x400\n"
    "s_cbranch_scc1 Llr_B28\n"
    "s_waitcnt lgkmcnt(0)\n"
    "v_cmp_eq_u32 vcc, 0, v228\n"
    "s_nop 0\n"
    "s_cbranch_vccnz Lls_B28\n"
    "s_branch Llr_B28\n"
    "Lls_B16:\n"
    "ds_read_b32 v228, v254 offset:1552\n"
    "s_add_u32 s59, s59, 0x400\n"
    "s_cbranch_scc1 Llr_B16\n"
    "s_waitcnt lgkmcnt(0)\n"
    "v_cmp_eq_u32 vcc, 0, v228\n"
    "s_nop 0\n"
    "s_cbranch_vccnz Lls_B16\n"
    "s_branch Llr_B16\n"
    "Lls_A20:\n"
    "ds_read_b32 v228, v254 offset:3604\n"
    "s_add_u32 s59, s59, 0x400\n"
    "s_cbranch_scc1 Llr_A20\n"
    "s_waitcnt lgkmcnt(0)\n"
    "v_cmp_eq_u32 vcc, 0, v228\n"
    "s_nop 0\n"
    "s_cbranch_vccnz Lls_A20\n"
    "s_branch Llr_A20\n"
    "Lls_A32:\n"
    "ds_read_b32 v228, v254 offset:3616\n"
    "s_add_u32 s59, s59, 0x400\n"
    "s_cbranch_scc1 Llr_A32\n"
    "s_waitcnt lgkmcnt(0)\n"
    "v_cmp_eq_u32 vcc, 0, v228\n"
    "s_nop 0\n"
    "s_cbranch_vccnz Lls_A32\n"
    "s_branch Llr_A32\n"
    "Lls_A24:\n"
    "ds_read_b32 v228, v254 offset:3608\n"
    "s_add_u32 s59, s59, 0x400\n"
    "s_cbranch_scc1 Llr_A24\n"
    "s_waitcnt lgkmcnt(0)\n"
    "v_cmp_eq_u32 vcc, 0, v228\n"
    "s_nop 0\n"
    "s_cbranch_vccnz Lls_A24\n"
    "s_branch Llr_A24\n"
    "Lend:\n"
    : "+{v0}"(tid)
    : "{s[0:1]}"(__builtin_amdgcn_kernarg_segment_ptr()), "{s2}"(blockIdx.x)
    : "v1", "v2", "v3", "v4", "v5", "v6", "v7", "v8", "v9", "v10", "v11", "v12", "v13", "v14", "v15", "v16", "v17", "v18", "v19", "v20", "v21", "v22", "v23", "v24", "v25", "v26", "v27", "v28", "v29", "v30", "v31", "v32", "v33", "v34", "v35", "v36", "v37", "v38", "v39", "v40", "v41", "v42", "v43", "v44", "v45", "v46", "v47", "v48", "v49", "v50", "v51", "v52", "v53", "v54", "v55", "v56", "v57", "v58", "v59", "v60", "v61", "v62", "v63", "v64", "v65", "v66", "v67", "v68", "v69", "v70", "v71", "v72", "v73", "v74", "v75", "v76", "v77", "v78", "v79", "v80", "v81", "v82", "v83", "v84", "v85", "v86", "v87", "v88", "v89", "v90", "v91", "v92", "v93", "v94", "v95", "v96", "v97", "v98", "v99", "v100", "v101", "v102", "v103", "v104", "v105", "v106", "v107", "v108", "v109", "v110", "v111", "v112", "v113", "v114", "v115", "v116", "v117", "v118", "v119", "v120", "v121", "v122", "v123", "v124", "v125", "v126", "v127", "v128", "v129", "v130", "v131", "v132", "v133", "v134", "v135", "v136", "v137", "v138", "v139", "v140", "v141", "v142", "v143", "v144", "v145", "v146", "v147", "v148", "v149", "v150", "v151", "v152", "v153", "v154", "v155", "v156", "v157", "v158", "v159", "v160", "v161", "v162", "v163", "v164", "v165", "v166", "v167", "v168", "v169", "v170", "v171", "v172", "v173", "v174", "v175", "v176", "v177", "v178", "v179", "v180", "v181", "v182", "v183", "v184", "v185", "v186", "v187", "v188", "v189", "v190", "v191", "v192", "v193", "v194", "v195", "v196", "v197", "v198", "v199", "v200", "v201", "v202", "v203", "v204", "v205", "v206", "v207", "v208", "v209", "v210", "v211", "v212", "v213", "v214", "v215", "v216", "v217", "v218", "v219", "v220", "v221", "v222", "v223", "v224", "v225", "v226", "v227", "v228", "v229", "v230", "v231", "v232", "v233", "v234", "v235", "v236", "v237", "v238", "v239", "v240", "v241", "v242", "v243", "v244", "v245", "v246", "v247", "v248", "v249", "v250", "v251", "v252", "v253", "v254", "v255", "s3", "s4", "s5", "s6", "s7", "s8", "s9", "s10", "s11", "s12", "s13", "s14", "s15", "s16", "s17", "s18", "s19", "s20", "s21", "s22", "s23", "s24", "s25", "s26", "s27", "s28", "s29", "s30", "s31", "s33", "s34", "s35", "s36", "s37", "s38", "s39", "s40", "s41", "s42", "s43", "s44", "s45", "s46", "s47", "s48", "s49", "s50", "s51", "s52", "s53", "s54", "s55", "s56", "s57", "s58", "s59", "s60", "s61", "s62", "s63", "vcc", "scc", "memory");
#endif
}
}

extern "C" void kernel_launch(void* const* d_in, const int* in_sizes, int n_in, void* d_out, int out_size, void* d_ws, size_t ws_size,
                              hipStream_t stream) {
  (void)in_sizes; (void)n_in; (void)out_size; (void)ws_size;
  (void)hipFuncSetAttribute((const void*)fused_kernel, hipFuncAttributeMaxDynamicSharedMemorySize, 163840);
  fused_kernel<<<256, 512, 163840, stream>>>((const char*)d_in[0], (const char*)d_in[1], (const char*)d_in[2], (const char*)d_in[4],
                                             (const char*)d_in[5], (const char*)d_in[6], (char*)d_out, (char*)d_ws);
}
```
